```python
import torch

def _softmax(x, axis=-1):
    return torch.softmax(x, dim=axis)

def _gelu(x):
    return torch.nn.functional.gelu(x, approximate='tanh')

def _silu(x):
    return torch.nn.functional.silu(x)

def _sigmoid(x):
    return torch.sigmoid(x)

def _layernorm(x, axis=-1, eps=1e-05):
    m = x.mean(dim=axis, keepdim=True)
    v = x.var(dim=axis, keepdim=True, unbiased=False)
    return (x - m) / torch.sqrt(v + eps)

def _scatter_add(tgt, idx, src, axis=0):
    return tgt.clone().index_add_(axis, idx, src)

def _segment_sum(src, seg, n):
    out = torch.zeros((n,) + src.shape[1:], dtype=src.dtype)
    return out.index_add_(0, seg, src)

def _pad(x, pw):
    flat = tuple((v for pair in reversed(pw) for v in pair))
    return torch.nn.functional.pad(x, flat)
kernel_name = 'adaLN_cond__gqa_block_cached__glu_gated_geglu__decode'

def setup_inputs():

    _w = lambda a: (lambda t: t.to(torch.bfloat16) if t.is_floating_point() else t)(torch.as_tensor(a))

    def _A_setup():
        B, Sq, D = (8, 1, 576)
        Hq, Hkv, Sk, Dh = (32, 16, 8192, 128)
        assert Hq % Hkv == 0, (Hq, Hkv)
        return {'x': _w((lambda *s: torch.randn(*s, dtype=torch.bfloat16) * 0.1)(B, Sq, D)), 'k_cache': _w((lambda *s: torch.randn(*s, dtype=torch.bfloat16) * 0.8)(B, Hkv, Sk, Dh)), 'v_cache': _w((lambda *s: torch.randn(*s, dtype=torch.bfloat16) * 3.2)(B, Hkv, Sk, Dh)), 'wq': _w((lambda *s: torch.randn(*s, dtype=torch.bfloat16) * 0.8)(D, Hq * Dh) * D ** (-0.5)), 'wo': _w((lambda *s: torch.randn(*s, dtype=torch.bfloat16) * 3.2)(Hq * Dh, D) * (Hq * Dh) ** (-0.5))}
    _A_in = _A_setup()

    def _B_setup():
        B, S, Dm, Df = (8, 1, 576, 4608)
        return {'x': _w((lambda *s: torch.randn(*s, dtype=torch.bfloat16) * 0.1)(B, S, Dm)), 'w_gate': _w((lambda *s: torch.randn(*s, dtype=torch.bfloat16) * 3.2)(Dm, Df) * Dm ** (-0.5)), 'w_up': _w((lambda *s: torch.randn(*s, dtype=torch.bfloat16) * 3.2)(Dm, Df) * Dm ** (-0.5)), 'w_down': _w((lambda *s: torch.randn(*s, dtype=torch.bfloat16) * 3.2)(Df, Dm) * Df ** (-0.5))}
    _B_in = _B_setup()
    return {'A_x': _A_in['x'], 'A_k_cache': _A_in['k_cache'], 'A_v_cache': _A_in['v_cache'], 'A_wq': _A_in['wq'], 'A_wo': _A_in['wo'], 'B_x': _B_in['x'], 'B_w_gate': _B_in['w_gate'], 'B_w_up': _B_in['w_up'], 'B_w_down': _B_in['w_down'], 'c': _w((lambda *s: torch.randn(*s, dtype=torch.bfloat16) * 0.4)(8, 576) * 8.0), 'w_mod': _w((lambda *s: torch.randn(*s, dtype=torch.bfloat16) * 0.4)(576, 6 * 576) * 576 ** (-0.5) * 8.0)}

def reference(A_x, A_k_cache, A_v_cache, A_wq, A_wo, B_x, B_w_gate, B_w_up, B_w_down, c, w_mod):

    def _A_ref(x, k_cache, v_cache, wq, wo):
        B, Sq, D = x.shape
        Hq, Dh = (32, 128)
        Hkv = k_cache.shape[1]
        rep = Hq // Hkv
        q = (lambda x, *d: x.permute(*d))((x @ wq).reshape(B, Sq, Hq, Dh), 0, 2, 1, 3)
        k = (lambda x, n, axis: torch.repeat_interleave(x, n, dim=axis))(k_cache, rep, axis=1)
        v = (lambda x, n, axis: torch.repeat_interleave(x, n, dim=axis))(v_cache, rep, axis=1)
        scores = torch.einsum('bhqd,bhkd->bhqk', q, k) * Dh ** (-0.5)
        p = _softmax(scores, axis=-1)
        o = torch.einsum('bhqk,bhkd->bhqd', p, v)
        return (lambda x, *d: x.permute(*d))(o, 0, 2, 1, 3).reshape(B, Sq, Hq * Dh) @ wo

    def _B_ref(x, w_gate, w_up, w_down):
        g = torch.einsum('bsd,df->bsf', x, w_gate)
        u = torch.einsum('bsd,df->bsf', x, w_up)
        h = _gelu(g) * u
        return torch.einsum('bsf,fd->bsd', h, w_down)
    x = A_x
    mod = (_silu(c) @ w_mod).reshape(c.shape[0], 6, 1, -1)
    sa, ca, ga, sb, cb, gb = (mod[:, 0].clone(), mod[:, 1].clone(), mod[:, 2].clone(), mod[:, 3].clone(), mod[:, 4].clone(), mod[:, 5].clone())
    h = _layernorm(x, axis=-1) * (1 + ca) + sa
    x = x + ga * _A_ref(h, A_k_cache, A_v_cache, A_wq, A_wo)
    h = _layernorm(x, axis=-1) * (1 + cb) + sb
    x = x + gb * _B_ref(h, B_w_gate, B_w_up, B_w_down)
    return x
```

```cpp
#include <hip/hip_runtime.h>
#include <hip/hip_bf16.h>
#include <stdint.h>

typedef unsigned short u16;
__device__ __forceinline__ float bf2f(u16 b){ return __builtin_bit_cast(float, (unsigned)b<<16); }
__device__ __forceinline__ u16 f2bf(float f){ unsigned u=__builtin_bit_cast(unsigned,f); u += 0x7FFFu + ((u>>16)&1u); return (u16)(u>>16);}
__device__ __forceinline__ float rb(float f){ return bf2f(f2bf(f)); }

constexpr int B=8, D=576, HQ=32, HKV=16, SK=8192, DH=128, DF=4608;

__global__ void gemv(const float* a, const u16* W, float* out, int K, int N){
  int n = blockIdx.x*64 + (threadIdx.x&63); int w = threadIdx.x>>6;
  __shared__ float red[4][8][64];
  float acc[8]={0,0,0,0,0,0,0,0};
  if(n<N) for(int k=w;k<K;k+=4){ float wv=bf2f(W[(size_t)k*N+n]);
    #pragma unroll
    for(int b=0;b<8;b++) acc[b]+=a[b*K+k]*wv; }
  for(int b=0;b<8;b++) red[w][b][threadIdx.x&63]=acc[b];
  __syncthreads();
  if(w==0 && n<N) for(int b=0;b<8;b++) out[b*N+n]=red[0][b][threadIdx.x]+red[1][b][threadIdx.x]+red[2][b][threadIdx.x]+red[3][b][threadIdx.x];
}
__global__ void k_silu(const u16* c, float* s){ int i=blockIdx.x*256+threadIdx.x; if(i<B*D){ float x=bf2f(c[i]); s[i]=x/(1.f+__expf(-x)); } }
__global__ void k_ln(const float* x, const float* mod, int so, int co, float* h){
  int b=blockIdx.x; __shared__ float red[2][256];
  float s=0; for(int i=threadIdx.x;i<D;i+=256) s+=x[b*D+i];
  red[0][threadIdx.x]=s; __syncthreads();
  for(int o=128;o>0;o>>=1){ if(threadIdx.x<o) red[0][threadIdx.x]+=red[0][threadIdx.x+o]; __syncthreads(); }
  float mean=red[0][0]/D; float v=0; for(int i=threadIdx.x;i<D;i+=256){ float d=x[b*D+i]-mean; v+=d*d; }
  red[1][threadIdx.x]=v; __syncthreads();
  for(int o=128;o>0;o>>=1){ if(threadIdx.x<o) red[1][threadIdx.x]+=red[1][threadIdx.x+o]; __syncthreads(); }
  float r=1.f/sqrtf(red[1][0]/D+1e-5f);
  for(int i=threadIdx.x;i<D;i+=256) h[b*D+i]=(x[b*D+i]-mean)*r*(1.f+mod[b*3456+co*D+i])+mod[b*3456+so*D+i];
}
__global__ void k_cvt(const u16* x, float* o, int n){ int i=blockIdx.x*256+threadIdx.x; if(i<n) o[i]=bf2f(x[i]); }
constexpr int NS=16;
__global__ void k_attn(const float* q, const u16* K, const u16* V, float* pm, float* pl, float* po){
  int s=blockIdx.x, kvh=blockIdx.y, b=blockIdx.z; int tid=threadIdx.x;
  __shared__ float qs[2][128]; __shared__ float sc[2][512]; __shared__ float red[256];
  if(tid<256) qs[tid>>7][tid&127]=q[b*4096+(kvh*2+(tid>>7))*128+(tid&127)];
  __syncthreads();
  const u16* Kp=K+((size_t)(b*HKV+kvh)*SK+s*512)*DH; const u16* Vp=V+((size_t)(b*HKV+kvh)*SK+s*512)*DH;
  for(int j=tid;j<512;j+=256){ float a0=0,a1=0; for(int d=0;d<128;d++){ float kv=bf2f(Kp[j*128+d]); a0+=kv*qs[0][d]; a1+=kv*qs[1][d]; } sc[0][j]=a0*0.08838834764831845f; sc[1][j]=a1*0.08838834764831845f; }
  __syncthreads();
  for(int h=0;h<2;h++){
    float m=fmaxf(sc[h][tid],sc[h][tid+256]); red[tid]=m; __syncthreads();
    for(int o=128;o>0;o>>=1){ if(tid<o) red[tid]=fmaxf(red[tid],red[tid+o]); __syncthreads(); }
    m=red[0]; __syncthreads();
    float e0=__expf(sc[h][tid]-m), e1=__expf(sc[h][tid+256]-m); sc[h][tid]=e0; sc[h][tid+256]=e1; red[tid]=e0+e1; __syncthreads();
    for(int o=128;o>0;o>>=1){ if(tid<o) red[tid]+=red[tid+o]; __syncthreads(); }
    if(tid==0){ int idx=((b*HQ+kvh*2+h)*NS+s); pm[idx]=m; pl[idx]=red[0]; }
    __syncthreads();
  }
  { int h=tid>>7, d=tid&127; float a=0; for(int j=0;j<512;j++) a+=sc[h][j]*bf2f(Vp[j*128+d]); po[((size_t)(b*HQ+kvh*2+h)*NS+s)*128+d]=a; }
}
__global__ void k_comb(const float* pm,const float* pl,const float* po,float* o){
  int bh=blockIdx.x; int d=threadIdx.x; float M=-INFINITY; for(int s=0;s<NS;s++) M=fmaxf(M,pm[bh*NS+s]);
  float L=0,a=0; for(int s=0;s<NS;s++){ float w=__expf(pm[bh*NS+s]-M); L+=w*pl[bh*NS+s]; a+=w*po[((size_t)bh*NS+s)*128+d]; }
  o[bh*128+d]=a/L;
}
__global__ void k_res(const float* x,const float* mod,int go,const float* y,float* out){ int i=blockIdx.x*256+threadIdx.x; if(i<B*D){ int b=i/D,d=i%D; out[i]=x[i]+mod[b*3456+go*D+d]*y[i]; } }
__global__ void k_act(const float* g,const float* u,float* a){ int i=blockIdx.x*256+threadIdx.x; if(i<B*DF){ float x=g[i]; float t=tanhf(0.7978845608028654f*(x+0.044715f*x*x*x)); a[i]=0.5f*x*(1.f+t)*u[i]; } }
__global__ void k_out(const float* x,u16* o){ int i=blockIdx.x*256+threadIdx.x; if(i<B*D) o[i]=f2bf(x[i]); }

extern "C" void kernel_launch(void* const* d_in, const int* in_sizes, int n_in, void* d_out, int out_size, void* d_ws, size_t ws_size, hipStream_t stream) {
  const u16* x=(const u16*)d_in[0]; const u16* K=(const u16*)d_in[1]; const u16* V=(const u16*)d_in[2];
  const u16* wq=(const u16*)d_in[3]; const u16* wo=(const u16*)d_in[4];
  const u16* wg=(const u16*)d_in[6]; const u16* wu=(const u16*)d_in[7]; const u16* wd=(const u16*)d_in[8];
  const u16* c=(const u16*)d_in[9]; const u16* wm=(const u16*)d_in[10];
  float* ws=(float*)d_ws; size_t o=0;
  float* s=ws+o; o+=8192; float* mod=ws+o; o+=32768; float* xf=ws+o; o+=8192; float* h=ws+o; o+=8192; float* q=ws+o; o+=32768;
  float* pm=ws+o; o+=8192; float* pl=ws+o; o+=8192; float* po=ws+o; o+=8*32*NS*128; float* oo=ws+o; o+=32768; float* y=ws+o; o+=8192;
  float* x1=ws+o; o+=8192; float* g=ws+o; o+=65536; float* u=ws+o; o+=65536; float* a=ws+o; o+=65536; float* x2=ws+o; o+=8192;
  k_silu<<<18,256,0,stream>>>(c,s);
  gemv<<<54,256,0,stream>>>(s,wm,mod,576,3456);
  k_cvt<<<18,256,0,stream>>>(x,xf,B*D);
  k_ln<<<8,256,0,stream>>>(xf,mod,0,1,h);
  gemv<<<64,256,0,stream>>>(h,wq,q,576,4096);
  k_attn<<<dim3(NS,HKV,B),256,0,stream>>>(q,K,V,pm,pl,po);
  k_comb<<<256,128,0,stream>>>(pm,pl,po,oo);
  gemv<<<9,256,0,stream>>>(oo,wo,y,4096,576);
  k_res<<<18,256,0,stream>>>(xf,mod,2,y,x1);
  k_ln<<<8,256,0,stream>>>(x1,mod,3,4,h);
  gemv<<<72,256,0,stream>>>(h,wg,g,576,4608);
  gemv<<<72,256,0,stream>>>(h,wu,u,576,4608);
  k_act<<<144,256,0,stream>>>(g,u,a);
  gemv<<<9,256,0,stream>>>(a,wd,y,4608,576);
  k_res<<<18,256,0,stream>>>(x1,mod,5,y,x2);
  k_out<<<18,256,0,stream>>>(x2,(u16*)d_out);
}
```

```cpp
#include <hip/hip_runtime.h>
#include <stdint.h>

namespace ak {

typedef unsigned short u16;
typedef unsigned long long u64;
typedef unsigned v4u __attribute__((ext_vector_type(4)));
typedef unsigned v2u __attribute__((ext_vector_type(2)));
typedef float f4 __attribute__((ext_vector_type(4)));
typedef float f2 __attribute__((ext_vector_type(2)));
typedef __bf16 bf8 __attribute__((ext_vector_type(8)));
typedef __bf16 bf2 __attribute__((ext_vector_type(2)));
typedef short s4v __attribute__((ext_vector_type(4)));
#define LAS __attribute__((address_space(3)))
#define GAS __attribute__((address_space(1)))
typedef __amdgpu_buffer_rsrc_t rsrc_t;

#define K_R 4
#define K_NP 2
#define K_PF 4
#define K_TRIG_WO 29
#define K_TRIG_FFN 31
#define K_STAMPS 0

constexpr int NB = 8, DM = 576, DFF = 4608, NMOD = 3456, NQ = 4096;
constexpr int NWG = 256, NTHR = 768;
constexpr int NTILE = 32;
constexpr int R = K_R, NP = K_NP, P = NP * 8;
constexpr int BT = (8 % R == 0) ? 1 : (R == 6 || R == 12 ? 3 : (R == 16 ? 2 : R));
static_assert((8 * BT) % R == 0 && (NTILE - NP) % BT == 0 && R <= P, "ring geometry");
constexpr float LOG2E = 1.4426950408889634f;
constexpr float SCALE = 0.08838834764831845f;
constexpr unsigned SENT = 0xFFFFFFFFu;
constexpr int AUX_NT = 2, AUX_SC1 = 16;
constexpr unsigned SPIN_CAP = 1u << 16;

constexpr int WS_EPOCH = 0, WS_ERR = 256, WS_CLR = 4096;
constexpr int WS_MOD = 8192;
constexpr int WS_QP = 65536;
constexpr int WS_OP = WS_QP + 3 * NB * NQ * 4;
constexpr int WS_ML = WS_OP + NWG * 512;
constexpr int WS_YP = WS_ML + NWG * 16;
constexpr int WS_DP = WS_YP + 2 * NB * DM * 4;
constexpr int WS_END = WS_DP + 144 * 144 * 128;
constexpr size_t WS_STAMP = 64u << 20;
constexpr int NSTAMP = 24;

constexpr int L_W = 0;
constexpr int L_SIMG = 73728;
constexpr int L_HIMG = 24576;
constexpr int HSTR = 400;
constexpr int L_WU = 36864, L_WD = 73728;
constexpr int L_GU = 110592;
constexpr int L_ACT = L_GU + 6144;
constexpr int ASTR = 80;
constexpr int L_RED2 = L_ACT + 640;
constexpr int L_X1S = L_RED2 + 1536;
constexpr int L_H2 = L_X1S + 128;
constexpr int XSTR = 1168;
static_assert(L_H2 + 8 * XSTR <= 131072, "ffn lds");
constexpr int L_MRG = 131072;
constexpr int L_Q = L_MRG + 8448;
constexpr int L_CTL = L_Q + 512;
constexpr int L_END = L_CTL + 128;
enum { C_SYNC1 = 0, C_HS = 1, C_TRIG = 2, C_MRG = 3, C_DEAD = 4, C_DMA = 5, C_E0 = 8, C_E1 = 9, C_T0 = 10, C_T1 = 11 };

struct Params {
  const u16 *x, *K, *V, *wq, *wo, *wg, *wu, *wd, *c, *wm;
  u16* out;
  unsigned char* ws;
};

__device__ __forceinline__ float asf(unsigned u) { return __builtin_bit_cast(float, u); }
__device__ __forceinline__ unsigned asu(float f) { return __builtin_bit_cast(unsigned, f); }
__device__ __forceinline__ float bflo(unsigned u) { return asf(u << 16); }
__device__ __forceinline__ float bfhi(unsigned u) { return asf(u & 0xffff0000u); }
__device__ __forceinline__ unsigned pk(float a, float b) {
  f2 v = {a, b};
  return __builtin_bit_cast(unsigned, __builtin_convertvector(v, bf2));
}
__device__ __forceinline__ float rb(float a) { return bflo(pk(a, a)); }
__device__ __forceinline__ unsigned san(unsigned u) { return u < 0xFFFFFFFEu ? u : 0xFFFFFFFEu; }
__device__ __forceinline__ v4u san4(v4u v) { return v4u{san(v.x), san(v.y), san(v.z), san(v.w)}; }
__device__ __forceinline__ unsigned umax(unsigned a, unsigned b) { return a > b ? a : b; }
__device__ __forceinline__ unsigned max4(v4u v) { return umax(umax(v.x, v.y), umax(v.z, v.w)); }
template <int C> __device__ __forceinline__ float dpp(float x) {
  return asf((unsigned)__builtin_amdgcn_mov_dpp((int)asu(x), C, 0xf, 0xf, true));
}
template <int C> __device__ __forceinline__ unsigned dppu(unsigned x) { return (unsigned)__builtin_amdgcn_mov_dpp((int)x, C, 0xf, 0xf, true); }
constexpr int ROR1 = 0x121, ROR2 = 0x122, ROR4 = 0x124, ROR8 = 0x128, QX1 = 0xB1, QX2 = 0x4E;
__device__ __forceinline__ float x32max(float x) { auto t = __builtin_amdgcn_permlane32_swap(asu(x), asu(x), false, false); return fmaxf(asf(t[0]), asf(t[1])); }
__device__ __forceinline__ float x32sum(float x) { auto t = __builtin_amdgcn_permlane32_swap(asu(x), asu(x), false, false); return asf(t[0]) + asf(t[1]); }
__device__ __forceinline__ float x16sum(float x) { auto t = __builtin_amdgcn_permlane16_swap(asu(x), asu(x), false, false); return asf(t[0]) + asf(t[1]); }
__device__ __forceinline__ float row16sum(float x) { x += dpp<ROR1>(x); x += dpp<ROR2>(x); x += dpp<ROR4>(x); x += dpp<ROR8>(x); return x; }
__device__ __forceinline__ f4 mfma(v4u a, v4u b, f4 c) {
  return __builtin_amdgcn_mfma_f32_16x16x32_bf16(__builtin_bit_cast(bf8, a), __builtin_bit_cast(bf8, b), c, 0, 0, 0);
}
__device__ __forceinline__ rsrc_t mkr(const void* p, unsigned bytes) { return __builtin_amdgcn_make_buffer_rsrc((void*)p, 0, bytes, 0x00020000); }
__device__ __forceinline__ v4u ldsc1(rsrc_t r, int off) { return __builtin_amdgcn_raw_buffer_load_b128(r, off, 0, AUX_SC1); }
__device__ __forceinline__ v2u ldsc1_2(rsrc_t r, int off) { return __builtin_amdgcn_raw_buffer_load_b64(r, off, 0, AUX_SC1); }
__device__ __forceinline__ void stsc1(v4u v, rsrc_t r, int off) { __builtin_amdgcn_raw_buffer_store_b128(v, r, off, 0, AUX_SC1); }
__device__ __forceinline__ void stsc1_2(v2u v, rsrc_t r, int off) { __builtin_amdgcn_raw_buffer_store_b64(v, r, off, 0, AUX_SC1); }
#define MEMBAR() asm volatile("" ::: "memory")
#define VMWAIT(n) asm volatile("s_waitcnt vmcnt(" #n ")" ::: "memory")
#define LGWAIT() asm volatile("s_waitcnt lgkmcnt(0)" ::: "memory")
#define RAWBAR() asm volatile("s_waitcnt lgkmcnt(0)\n\ts_barrier" ::: "memory")
__device__ __forceinline__ void dma16(rsrc_t r, int voff, unsigned soff, unsigned ldsa) {
  unsigned keep;
  asm volatile("s_mov_b32 %0, m0\n\ts_mov_b32 m0, %1\n\ts_nop 0\n\tbuffer_load_dwordx4 %2, %3, %4 offen nt lds\n\ts_mov_b32 m0, %0" : "=&s"(keep) : "s"(ldsa), "v"(voff), "s"(r), "s"(soff) : "memory");
}
__device__ __forceinline__ v4u lds128(LAS unsigned char* L, int off) { return *(LAS v4u*)(L + off); }
__device__ __forceinline__ v4u trfrag(LAS unsigned char* L, int a0, int d) {
  s4v lo = __builtin_amdgcn_ds_read_tr16_b64_v4i16((LAS s4v*)(L + a0));
  s4v hi = __builtin_amdgcn_ds_read_tr16_b64_v4i16((LAS s4v*)(L + a0 + d));
  v2u l = __builtin_bit_cast(v2u, lo), h = __builtin_bit_cast(v2u, hi);
  return v4u{l.x, l.y, h.x, h.y};
}
__device__ __forceinline__ unsigned ldsw(LAS unsigned char* L, int w) { return *(volatile LAS unsigned*)(L + L_CTL + 4 * w); }
__device__ __forceinline__ void stw(LAS unsigned char* L, int w, unsigned v) { *(volatile LAS unsigned*)(L + L_CTL + 4 * w) = v; }
__device__ __forceinline__ unsigned lds_add(LAS unsigned char* L, int w) {
  unsigned o = 0;
  if ((threadIdx.x & 63) == 0) o = __hip_atomic_fetch_add((LAS unsigned*)(L + L_CTL + 4 * w), 1u, __ATOMIC_RELAXED, __HIP_MEMORY_SCOPE_WORKGROUP);
  return __builtin_amdgcn_readfirstlane(o);
}
__device__ __forceinline__ void lds_wait(LAS unsigned char* L, int w, unsigned n) {
  while (__builtin_amdgcn_readfirstlane(ldsw(L, w)) < n) {
    if (__builtin_amdgcn_readfirstlane(ldsw(L, C_DEAD))) break;
    __builtin_amdgcn_s_sleep(1);
  }
  MEMBAR();
}
__device__ __forceinline__ bool give_up(unsigned& spins, LAS unsigned char* L) {
  if (++spins > SPIN_CAP) { stw(L, C_DEAD, 1u); return true; }
  return (spins & 15u) == 0u && __builtin_amdgcn_readfirstlane(ldsw(L, C_DEAD)) != 0u;
}
__device__ __forceinline__ u64 mix64(u64 z) {
  z = (z ^ (z >> 30)) * 0xBF58476D1CE4E5B9ull;
  z = (z ^ (z >> 27)) * 0x94D049BB133111EBull;
  return z ^ (z >> 31);
}
#define STAMP(k) do {} while (0)

__device__ __forceinline__ float sigm(float z) { return __builtin_amdgcn_rcpf(1.f + __builtin_amdgcn_exp2f(-LOG2E * z)); }
__device__ __forceinline__ float gelu_t(float x) { const float z = 0.7978845608028654f * (x + 0.044715f * x * x * x); return x * sigm(2.f * z); }

struct Soft { float m, l; f4 acc; };
struct Lane { v4u qa0, qa1; unsigned sel[2][4]; int idx0; };

__device__ __forceinline__ float quadsum(f4 d) {
  float t = dpp<0x00>(d.x);
  t += dpp<0x55>(d.y); t += dpp<0xAA>(d.z); t += dpp<0xFF>(d.w);
  return t;
}
__device__ __forceinline__ void softmax_tile(f4 D0, f4 D1, Soft& s, const Lane& c, unsigned& val0, unsigned& val1) {
  const unsigned r1 = pk(quadsum(D0), quadsum(D1));
  const unsigned r2 = pk(bflo(r1) * SCALE, bfhi(r1) * SCALE);
  const float s0 = bflo(r2), s1 = bfhi(r2);
  float mx = fmaxf(s0, s1);
  mx = fmaxf(mx, dpp<ROR4>(mx)); mx = fmaxf(mx, dpp<ROR8>(mx)); mx = x32max(mx);
  const float mn = fmaxf(s.m, mx);
  const float al = __builtin_amdgcn_exp2f((s.m - mn) * LOG2E);
  const float p0 = __builtin_amdgcn_exp2f((s0 - mn) * LOG2E), p1 = __builtin_amdgcn_exp2f((s1 - mn) * LOG2E);
  s.m = mn; s.l = s.l * al + (p0 + p1);
  auto t = __builtin_amdgcn_permlane16_swap(asu(al), asu(al), false, false);
  const float al0 = asf(t[0]), al1 = asf(t[1]);
  s.acc.x *= al0; s.acc.y *= al0; s.acc.z *= al1; s.acc.w *= al1;
  const unsigned pp = pk(p0, p1);
  val0 = (unsigned)__builtin_amdgcn_ds_bpermute(c.idx0, (int)pp);
  val1 = (unsigned)__builtin_amdgcn_ds_bpermute(c.idx0 + 128, (int)pp);
}
template <int U> __device__ __forceinline__ v4u pfrag(unsigned val0, unsigned val1, const Lane& c) {
  const unsigned v = (U & 1) ? val1 : val0;
  constexpr int y = U >> 1;
  return v4u{__builtin_amdgcn_perm(v, v, c.sel[y][0]), __builtin_amdgcn_perm(v, v, c.sel[y][1]), __builtin_amdgcn_perm(v, v, c.sel[y][2]), __builtin_amdgcn_perm(v, v, c.sel[y][3])};
}

__global__ void __launch_bounds__(NTHR, 3) fused_kernel(Params p) {
  extern __shared__ __attribute__((aligned(16))) unsigned char smem[];
  LAS unsigned char* L = (LAS unsigned char*)smem;
  const int tid = threadIdx.x, lane = tid & 63, wave = __builtin_amdgcn_readfirstlane(tid >> 6);
  const int wg = blockIdx.x;
  const unsigned ldsb = (unsigned)(size_t)L;
  STAMP(0);
  if (tid < 32) *(LAS unsigned*)(L + L_CTL + 4 * tid) = 0u;
  RAWBAR();
  const rsrc_t rws = mkr(p.ws, WS_END);
  const int xl = wg & 7, xi = wg >> 3;
  const bool isM = wg < 54, isQ = wg >= 54 && wg < 246;
  const bool isF = wg < 144;
  const int f = xl * 18 + xi;
  const int wpair = xl + 8 * ((xi - 18) >> 1);
  const bool isW = wg >= 144 && wg < 192 && wpair < 18;
  const int wsig = wpair / 9, wng = 2 * (wpair % 9) + ((xi - 18) & 1);
  const int ab = wg >> 5, akv = (wg >> 1) & 15;

  Soft st;
  if (wave < 8) {
    const unsigned char* kb = (const unsigned char*)p.K + ((size_t)wg << 20);
    const unsigned char* vb = (const unsigned char*)p.V + ((size_t)wg << 20);
    const rsrc_t rk = mkr(kb, 1 << 20), rv = mkr(vb, 1 << 20);
    const int g = lane >> 4;
    const int koff = ((lane >> 2) & 3) * 256 + (g * 4 + (lane & 3)) * 16, voff = lane * 16;
    const int wb = wave * 4096;
    v4u pre[P];
#pragma unroll
    for (int n = 0; n < P; ++n) {
      if (n >= K_PF) { if (K_PF == 2) VMWAIT(1); else if (K_PF == 3) VMWAIT(2); else if (K_PF == 4) VMWAIT(3); else if (K_PF == 6) VMWAIT(5); else if (K_PF == 8) VMWAIT(7); }
      const int so = (n >> 3) * 32768 + wb;
      pre[n] = (n & 4) ? __builtin_amdgcn_raw_buffer_load_b128(rv, voff + (n & 3) * 1024, so, AUX_NT) : __builtin_amdgcn_raw_buffer_load_b128(rk, koff + (n & 3) * 1024, so, AUX_NT);
      __builtin_amdgcn_sched_barrier(0);
    }
    Lane c;
    {
      const int i = lane & 15, beta = i >> 2, pos = i & 1;
#pragma unroll
      for (int y = 0; y < 2; ++y)
#pragma unroll
        for (int w = 0; w < 4; ++w) {
          const unsigned b0 = 2 * y, b1 = 2 * y + 1;
          c.sel[y][w] = beta != w ? 0x0c0c0c0cu : (pos ? ((b1 << 24) | (b0 << 16) | 0x0c0cu) : (0x0c0c0000u | (b1 << 8) | b0));
        }
      c.idx0 = (16 * ((i >> 1) & 1) + 4 * g) * 4;
    }
    STAMP(1);
    RAWBAR();
    STAMP(2);
    {
      const int i = lane & 15;
      const v4u qf = lds128(L, L_Q + ((i >> 2) & 1) * 256 + (4 * g + (i & 3)) * 16);
      const v4u z = {0u, 0u, 0u, 0u};
      c.qa0 = (i >> 3) ? z : qf;
      c.qa1 = (i >> 3) ? qf : z;
    }
    st.m = -INFINITY; st.l = 0.f; st.acc = f4{0.f, 0.f, 0.f, 0.f};
    v4u ring[R];
    const f4 zero = {0.f, 0.f, 0.f, 0.f};
#define ISSUE(dst, tb, n) do { const int t_ = (tb) + ((n) >> 3); const unsigned sz_ = t_ < NTILE ? (1u << 20) : 0u; const int so_ = t_ * 32768 + wb; \
      dst = ((n) & 4) ? __builtin_amdgcn_raw_buffer_load_b128(mkr(vb, sz_), voff + ((n) & 3) * 1024, so_, AUX_NT) \
                      : __builtin_amdgcn_raw_buffer_load_b128(mkr(kb, sz_), koff + ((n) & 3) * 1024, so_, AUX_NT); __builtin_amdgcn_sched_barrier(0); } while (0)
#pragma unroll
    for (int t = 0; t < NP; ++t) {
      f4 D0 = mfma(c.qa0, pre[8 * t + 0], zero); if (8 * t + 0 < R) ISSUE(ring[(8 * t + 0) % R], NP, 8 * t + 0);
      D0 = mfma(c.qa1, pre[8 * t + 1], D0);      if (8 * t + 1 < R) ISSUE(ring[(8 * t + 1) % R], NP, 8 * t + 1);
      f4 D1 = mfma(c.qa0, pre[8 * t + 2], zero); if (8 * t + 2 < R) ISSUE(ring[(8 * t + 2) % R], NP, 8 * t + 2);
      D1 = mfma(c.qa1, pre[8 * t + 3], D1);      if (8 * t + 3 < R) ISSUE(ring[(8 * t + 3) % R], NP, 8 * t + 3);
      unsigned v0, v1;
      softmax_tile(D0, D1, st, c, v0, v1);
      st.acc = mfma(pfrag<0>(v0, v1, c), pre[8 * t + 4], st.acc); if (8 * t + 4 < R) ISSUE(ring[(8 * t + 4) % R], NP, 8 * t + 4);
      st.acc = mfma(pfrag<1>(v0, v1, c), pre[8 * t + 5], st.acc); if (8 * t + 5 < R) ISSUE(ring[(8 * t + 5) % R], NP, 8 * t + 5);
      st.acc = mfma(pfrag<2>(v0, v1, c), pre[8 * t + 6], st.acc); if (8 * t + 6 < R) ISSUE(ring[(8 * t + 6) % R], NP, 8 * t + 6);
      st.acc = mfma(pfrag<3>(v0, v1, c), pre[8 * t + 7], st.acc); if (8 * t + 7 < R) ISSUE(ring[(8 * t + 7) % R], NP, 8 * t + 7);
    }
    const int trig = isW ? K_TRIG_WO : K_TRIG_FFN;
#pragma unroll 1
    for (int t0 = NP; t0 < NTILE; t0 += BT) {
#pragma unroll
      for (int tt = 0; tt < BT; ++tt) {
        if (t0 + tt == trig && lane == 0 && wave == 0) stw(L, C_TRIG, 1u);
        const int nb = 8 * tt;
        f4 D0 = mfma(c.qa0, ring[(nb + 0) % R], zero); ISSUE(ring[(nb + 0) % R], t0, nb + 0 + R);
        D0 = mfma(c.qa1, ring[(nb + 1) % R], D0);      ISSUE(ring[(nb + 1) % R], t0, nb + 1 + R);
        f4 D1 = mfma(c.qa0, ring[(nb + 2) % R], zero); ISSUE(ring[(nb + 2) % R], t0, nb + 2 + R);
        D1 = mfma(c.qa1, ring[(nb + 3) % R], D1);      ISSUE(ring[(nb + 3) % R], t0, nb + 3 + R);
        unsigned v0, v1;
        softmax_tile(D0, D1, st, c, v0, v1);
        st.acc = mfma(pfrag<0>(v0, v1, c), ring[(nb + 4) % R], st.acc); ISSUE(ring[(nb + 4) % R], t0, nb + 4 + R);
        st.acc = mfma(pfrag<1>(v0, v1, c), ring[(nb + 5) % R], st.acc); ISSUE(ring[(nb + 5) % R], t0, nb + 5 + R);
        st.acc = mfma(pfrag<2>(v0, v1, c), ring[(nb + 6) % R], st.acc); ISSUE(ring[(nb + 6) % R], t0, nb + 6 + R);
        st.acc = mfma(pfrag<3>(v0, v1, c), ring[(nb + 7) % R], st.acc); ISSUE(ring[(nb + 7) % R], t0, nb + 7 + R);
      }
    }
#undef ISSUE
    STAMP(3);
    float l = st.l;
    l += dpp<ROR4>(l); l += dpp<ROR8>(l); l = x32sum(l);
    LAS float* mg = (LAS float*)(L + L_MRG) + wave * 264;
    const int cc = lane & 15;
    *(LAS f2*)(mg + 8 + 8 * cc + 2 * g) = f2{st.acc.x, st.acc.y};
    *(LAS f2*)(mg + 8 + 128 + 8 * cc + 2 * g) = f2{st.acc.z, st.acc.w};
    if (lane == 0 || lane == 16) *(LAS f2*)(mg + 2 * g) = f2{st.m, l};
    LGWAIT();
    if (lds_add(L, C_MRG) == 7u) {
      MEMBAR();
      const int h = lane >> 5, d0 = 4 * (lane & 31);
      LAS float* m0 = (LAS float*)(L + L_MRG);
      f2 ml[8]; f4 a[8];
#pragma unroll
      for (int w = 0; w < 8; ++w) { ml[w] = *(LAS f2*)(m0 + w * 264 + 2 * h); a[w] = *(LAS f4*)(m0 + w * 264 + 8 + 128 * h + d0); }
      float M = ml[0].x;
#pragma unroll
      for (int w = 1; w < 8; ++w) M = fmaxf(M, ml[w].x);
      float Ls = 0.f; f4 O = {0.f, 0.f, 0.f, 0.f};
#pragma unroll
      for (int w = 0; w < 8; ++w) { const float e = __builtin_amdgcn_exp2f((ml[w].x - M) * LOG2E); Ls += e * ml[w].y; O += e * a[w]; }
      const float inv = 1.f / Ls;
      const unsigned o0 = pk(O.x * inv, O.y * inv), o1 = pk(O.z * inv, O.w * inv);
      const unsigned n0 = dppu<QX1>(o0), n1 = dppu<QX1>(o1);
      if (!(lane & 1)) stsc1(san4(v4u{o0, o1, n0, n1}), rws, WS_OP + wg * 512 + (h * 128 + d0) * 2);
      const unsigned M1 = __builtin_amdgcn_readlane(asu(M), 32), L1 = __builtin_amdgcn_readlane(asu(Ls), 32);
      if (lane == 0) stsc1(san4(v4u{asu(M), asu(Ls), M1, L1}), rws, WS_ML + wg * 16);
      STAMP(4);
    }
  } else {
    const int sw = wave - 8, stid = tid - 512;
    const int qi = wg - 54, qcg = qi / 3, qkq = qi % 3;
    u64 E = 0;
    if (sw == 3) {
      E = __hip_atomic_load((GAS u64*)(p.ws + WS_EPOCH), __ATOMIC_RELAXED, __HIP_MEMORY_SCOPE_AGENT);
      const v4u ss = {SENT, SENT, SENT, SENT};
      if (lane < 32) stsc1(ss, rws, WS_OP + wg * 512 + lane * 16);
      if (lane == 32) stsc1(ss, rws, WS_ML + wg * 16);
      if (isM) stsc1(ss, rws, WS_MOD + ((lane >> 3) * NMOD + wg * 64) * 2 + (lane & 7) * 16);
      if (isQ) {
#pragma unroll
        for (int j = 0; j < 2; ++j) stsc1(ss, rws, WS_QP + (((qkq * 8 + 4 * j + (lane >> 4)) * NQ) + qcg * 64) * 4 + (lane & 15) * 16);
      }
      if (isW) stsc1(ss, rws, WS_YP + ((wsig * 8 + (lane >> 3)) * DM + wng * 32) * 4 + (lane & 7) * 16);
      if (isF) {
#pragma unroll
        for (int j = 0; j < 18; ++j) stsc1(ss, rws, WS_DP + ((j * 8 + (lane >> 3)) * 144 + f) * 128 + (lane & 7) * 16);
      }
    }
    float ln[8];
    const int qb = 2 * sw + (lane >> 5), qj = lane & 31;
    const bool qact = qj < 24;
    if (isM) {
      if (sw < 3) {
        const rsrc_t rw = mkr(p.wm, DM * NMOD * 2);
        const int vo = (lane >> 3) * (NMOD * 2) + (lane & 7) * 16 + wg * 128;
#pragma unroll
        for (int j = 0; j < 24; ++j) { const int i = sw * 24 + j; dma16(rw, vo, i * 8 * NMOD * 2, ldsb + L_W + i * 1024); }
      }
      const rsrc_t rc = mkr(p.c, NB * DM * 2);
      v4u cv[3];
#pragma unroll
      for (int j = 0; j < 3; ++j) cv[j] = __builtin_amdgcn_raw_buffer_load_b128(rc, (stid + 256 * j) * 16, 0, 0);
#pragma unroll
      for (int j = 0; j < 3; ++j) {
        const int ch = stid + 256 * j;
        if (ch < 576) {
          v4u o;
#pragma unroll
          for (int e = 0; e < 4; ++e) { const float a = bflo(cv[j][e]), b = bfhi(cv[j][e]); o[e] = pk(a / (1.f + expf(-a)), b / (1.f + expf(-b))); }
          *(LAS v4u*)(L + L_SIMG + (ch / 72) * XSTR + (ch % 72) * 16) = o;
        }
      }
    } else if (isQ) {
      if (sw < 3) {
        const rsrc_t rw = mkr(p.wq, DM * NQ * 2);
        const int vo = (lane >> 3) * (NQ * 2) + (lane & 7) * 16 + qcg * 128;
#pragma unroll
        for (int j = 0; j < 8; ++j) { const int i = sw * 8 + j; dma16(rw, vo, (qkq * 192 + i * 8) * NQ * 2, ldsb + L_W + i * 1024); }
      }
      const rsrc_t rx = mkr(p.x, NB * DM * 2);
      v4u xv[3];
#pragma unroll
      for (int t = 0; t < 3; ++t) xv[t] = __builtin_amdgcn_raw_buffer_load_b128(rx, qact ? (qb * DM + 192 * t + 8 * qj) * 2 : 0x7ffffff0, 0, 0);
      float xf[3][8];
      float sum = 0.f;
#pragma unroll
      for (int t = 0; t < 3; ++t)
#pragma unroll
        for (int e = 0; e < 4; ++e) { xf[t][2 * e] = bflo(xv[t][e]); xf[t][2 * e + 1] = bfhi(xv[t][e]); sum += xf[t][2 * e] + xf[t][2 * e + 1]; }
      sum = x16sum(row16sum(sum));
      const float mean = sum * (1.f / DM);
      float vs = 0.f;
#pragma unroll
      for (int t = 0; t < 3; ++t)
#pragma unroll
        for (int e = 0; e < 8; ++e) { const float d = xf[t][e] - mean; vs += qact ? d * d : 0.f; }
      vs = x16sum(row16sum(vs));
      const float mb = rb(mean), den = rb(__builtin_sqrtf(rb(rb(vs * (1.f / DM)) + 1e-5f)));
#pragma unroll
      for (int e = 0; e < 8; ++e) { const float xe = qkq == 0 ? xf[0][e] : (qkq == 1 ? xf[1][e] : xf[2][e]); ln[e] = rb(rb(xe - mb) / den); }
    }
    if (sw == 3) {
      VMWAIT(0);
      const unsigned e0 = __builtin_amdgcn_readfirstlane((unsigned)E), e1 = __builtin_amdgcn_readfirstlane((unsigned)(E >> 32));
      E = ((u64)e1 << 32) | e0;
      if (lane == 0) { __hip_atomic_store((GAS u64*)(p.ws + WS_CLR) + wg, mix64(E + 1), __ATOMIC_RELAXED, __HIP_MEMORY_SCOPE_AGENT); stw(L, C_E0, e0); stw(L, C_E1, e1); }
      STAMP(22);
    }
    const int g = lane >> 4, q4 = (lane & 15) >> 2, p4 = lane & 3;
    if (isM) {
      STAMP(23);
      VMWAIT(0); LGWAIT();
      lds_add(L, C_SYNC1); lds_wait(L, C_SYNC1, 4);
      STAMP(5);
      f4 D = {0.f, 0.f, 0.f, 0.f};
#pragma unroll
      for (int ks = 0; ks < 18; ++ks) {
        const v4u wf = trfrag(L, L_W + (32 * ks + 8 * g + q4) * 128 + sw * 32 + p4 * 8, 4 * 128);
        const v4u sf = lds128(L, L_SIMG + (lane & 7) * XSTR + (32 * ks + 8 * g) * 2);
        D = mfma(wf, sf, D);
      }
      if ((lane & 15) < 8) stsc1_2(v2u{san(pk(D.x, D.y)), san(pk(D.z, D.w))}, rws, WS_MOD + ((lane & 7) * NMOD + wg * 64 + sw * 16 + 4 * g) * 2);
      STAMP(6);
    }
    if (sw == 3) {
      const u64 tag = mix64(E + 1);
      const unsigned t0 = (unsigned)tag, t1 = (unsigned)(tag >> 32);
      for (unsigned spins = 0;;) {
        MEMBAR();
        const v4u a = ldsc1(rws, WS_CLR + lane * 32), b = ldsc1(rws, WS_CLR + lane * 32 + 16);
        const bool ok = a.x == t0 && a.y == t1 && a.z == t0 && a.w == t1 && b.x == t0 && b.y == t1 && b.z == t0 && b.w == t1;
        if (__all(ok)) break;
        if (give_up(spins, L)) break;
        __builtin_amdgcn_s_sleep(2);
      }
      if (lane == 0) stw(L, C_HS, 1u);
      STAMP(7);
    }
    if (isQ) {
      lds_wait(L, C_HS, 1);
      v4u sa, ca;
      const int mo = qact ? WS_MOD + (qb * NMOD + 192 * qkq + 8 * qj) * 2 : 0x7ffffff0;
      for (unsigned spins = 0;;) {
        MEMBAR();
        sa = ldsc1(rws, mo); ca = ldsc1(rws, qact ? mo + DM * 2 : mo);
        if (__all(umax(max4(sa), max4(ca)) != SENT)) break;
        if (give_up(spins, L)) break;
        __builtin_amdgcn_s_sleep(2);
      }
      STAMP(8);
      v4u h;
#pragma unroll
      for (int e = 0; e < 4; ++e) {
        const unsigned c1 = pk(1.f + bflo(ca[e]), 1.f + bfhi(ca[e]));
        const unsigned m1 = pk(ln[2 * e] * bflo(c1), ln[2 * e + 1] * bfhi(c1));
        h[e] = pk(bflo(m1) + bflo(sa[e]), bfhi(m1) + bfhi(sa[e]));
      }
      if (qact) *(LAS v4u*)(L + L_HIMG + qb * HSTR + qj * 16) = h;
      VMWAIT(0); LGWAIT();
      lds_add(L, C_SYNC1); lds_wait(L, C_SYNC1, 4);
      f4 D = {0.f, 0.f, 0.f, 0.f};
#pragma unroll
      for (int ks = 0; ks < 6; ++ks) {
        const v4u wf = trfrag(L, L_W + (32 * ks + 8 * g + q4) * 128 + sw * 32 + p4 * 8, 4 * 128);
        const v4u hf = lds128(L, L_HIMG + (lane & 7) * HSTR + (32 * ks + 8 * g) * 2);
        D = mfma(wf, hf, D);
      }
      if ((lane & 15) < 8) stsc1(san4(__builtin_bit_cast(v4u, D)), rws, WS_QP + ((qkq * 8 + (lane & 7)) * NQ + qcg * 64 + sw * 16 + 4 * g) * 4);
      STAMP(9);
    }
    if (sw == 0) {
      lds_wait(L, C_HS, 1);
      v4u a0, a1, a2;
      const int qo = WS_QP + (ab * NQ + akv * 256 + 4 * lane) * 4;
      for (unsigned spins = 0;;) {
        MEMBAR();
        a0 = ldsc1(rws, qo); a1 = ldsc1(rws, qo + NB * NQ * 4); a2 = ldsc1(rws, qo + 2 * NB * NQ * 4);
        if (__all(umax(umax(max4(a0), max4(a1)), max4(a2)) != SENT)) break;
        if (give_up(spins, L)) break;
        __builtin_amdgcn_s_sleep(2);
      }
      const f4 s = (__builtin_bit_cast(f4, a0) + __builtin_bit_cast(f4, a1)) + __builtin_bit_cast(f4, a2);
      *(LAS v2u*)(L + L_Q + lane * 8) = v2u{pk(s.x, s.y), pk(s.z, s.w)};
      STAMP(10);
    }
    RAWBAR();
    if (isW || isF) {
      lds_wait(L, C_TRIG, 1);
      STAMP(11);
      if (isW) {
        const rsrc_t rw = mkr(p.wo, NQ * DM * 2);
        const int vo = (lane >> 2) * (DM * 2) + (lane & 3) * 16 + wng * 64;
#pragma unroll 4
        for (int j = 0; j < 32; ++j) { const int i = j * 4 + sw; dma16(rw, vo, (wsig * 2048 + i * 16) * DM * 2, ldsb + L_W + i * 1024); }
      } else {
        const rsrc_t rg = mkr(p.wg, DM * DFF * 2), ru = mkr(p.wu, DM * DFF * 2), rd = mkr(p.wd, DFF * DM * 2);
        const int vo = (lane >> 2) * (DFF * 2) + (lane & 3) * 16 + f * 64;
#pragma unroll 3
        for (int j = 0; j < 9; ++j) { const int i = j * 4 + sw; dma16(rg, vo, i * 16 * DFF * 2, ldsb + L_W + i * 1024); dma16(ru, vo, i * 16 * DFF * 2, ldsb + L_WU + i * 1024); }
#pragma unroll 3
        for (int j = 0; j < 9; ++j) { const int i = j * 4 + sw; dma16(rd, lane * 16, f * 32 * DM * 2 + i * 1024, ldsb + L_WD + i * 1024); }
      }
      VMWAIT(0);
      STAMP(12);
    }
  }

  const int g = lane >> 4, q4 = (lane & 15) >> 2, p4 = lane & 3;
  if (isW) {
    v4u fr[2][4];
    float wgt[2] = {0.f, 0.f};
    const int cc = lane & 15, pid = (cc & 7) * 32 + (wsig * 8 + wave) * 2 + (cc >> 3);
    if (wave < 8) {
      v4u ml;
      for (unsigned spins = 0;;) {
        MEMBAR();
        ml = ldsc1(rws, WS_ML + pid * 16);
        if (__all(max4(ml) != SENT)) break;
        if (give_up(spins, L)) break;
        __builtin_amdgcn_s_sleep(2);
      }
      STAMP(13);
#pragma unroll
      for (int hh = 0; hh < 2; ++hh)
#pragma unroll
        for (int ks = 0; ks < 4; ++ks) fr[hh][ks] = ldsc1(rws, WS_OP + pid * 512 + (hh * 128 + 32 * ks + 8 * g) * 2);
      __builtin_amdgcn_sched_barrier(0);
#pragma unroll
      for (int hh = 0; hh < 2; ++hh) {
        const float M = asf(ml[2 * hh]), Lv = asf(ml[2 * hh + 1]);
        const float M2 = dpp<ROR8>(M), L2 = dpp<ROR8>(Lv), Mx = fmaxf(M, M2);
        const float e1 = __builtin_amdgcn_exp2f((M - Mx) * LOG2E) * Lv, e2 = __builtin_amdgcn_exp2f((M2 - Mx) * LOG2E) * L2;
        wgt[hh] = e1 / (e1 + e2);
      }
    }
    RAWBAR();
    if (wave < 8) {
      v4u wf[2][4][2];
#pragma unroll
      for (int hh = 0; hh < 2; ++hh)
#pragma unroll
        for (int ks = 0; ks < 4; ++ks)
#pragma unroll
          for (int nt = 0; nt < 2; ++nt) wf[hh][ks][nt] = trfrag(L, L_W + (wave * 256 + hh * 128 + 32 * ks + 8 * g + q4) * 64 + nt * 32 + p4 * 8, 4 * 64);
      for (unsigned spins = 0;;) {
        unsigned mx = 0;
#pragma unroll
        for (int hh = 0; hh < 2; ++hh)
#pragma unroll
          for (int ks = 0; ks < 4; ++ks) mx = umax(mx, max4(fr[hh][ks]));
        if (__all(mx != SENT)) break;
        if (give_up(spins, L)) break;
        MEMBAR();
#pragma unroll
        for (int hh = 0; hh < 2; ++hh)
#pragma unroll
          for (int ks = 0; ks < 4; ++ks) fr[hh][ks] = ldsc1(rws, WS_OP + pid * 512 + (hh * 128 + 32 * ks + 8 * g) * 2);
      }
      STAMP(14);
      f4 y[2];
#pragma unroll
      for (int nt = 0; nt < 2; ++nt) {
        f4 d0 = {0.f, 0.f, 0.f, 0.f}, d1 = {0.f, 0.f, 0.f, 0.f};
#pragma unroll
        for (int ks = 0; ks < 4; ++ks) { d0 = mfma(wf[0][ks][nt], fr[0][ks], d0); d1 = mfma(wf[1][ks][nt], fr[1][ks], d1); }
        f4 t = wgt[0] * d0 + wgt[1] * d1;
        t.x += dpp<ROR8>(t.x); t.y += dpp<ROR8>(t.y); t.z += dpp<ROR8>(t.z); t.w += dpp<ROR8>(t.w);
        y[nt] = t;
      }
      if (cc < 8) {
#pragma unroll
        for (int nt = 0; nt < 2; ++nt) *(LAS f4*)(L + L_MRG + (((wave * 2 + nt) * 4 + g) * 8 + cc) * 16) = y[nt];
      }
    }
    RAWBAR();
    if (wave == 0) {
      f4 s = {0.f, 0.f, 0.f, 0.f};
#pragma unroll
      for (int w = 0; w < 8; ++w) s += *(LAS f4*)(L + L_MRG + (w * 64 + lane) * 16);
      const int nt = lane >> 5, rr = (lane >> 3) & 3, b = lane & 7;
      stsc1(san4(__builtin_bit_cast(v4u, s)), rws, WS_YP + ((wsig * 8 + b) * DM + wng * 32 + nt * 16 + 4 * rr) * 4);
      STAMP(15);
    }
  } else if (isF) {
    const int rb_ = f / 18, rc0 = (f % 18) * 32;
    unsigned gbw = 0;
    if (wave < 8) {
      const int b = wave;
      const bool act = lane < 48;
      const rsrc_t rx = mkr(p.x, NB * DM * 2);
      v2u xv[3], ga[3], sb[3], cb[3];
#pragma unroll
      for (int t = 0; t < 3; ++t) xv[t] = __builtin_amdgcn_raw_buffer_load_b64(rx, act ? (b * DM + 4 * (lane + 48 * t)) * 2 : 0x7ffffff0, 0, 0);
      for (unsigned spins = 0;;) {
        MEMBAR();
        unsigned mx = 0;
#pragma unroll
        for (int t = 0; t < 3; ++t) {
          const int o = act ? WS_MOD + (b * NMOD + 4 * (lane + 48 * t)) * 2 : 0x7ffffff0;
          ga[t] = ldsc1_2(rws, act ? o + 2 * DM * 2 : o); sb[t] = ldsc1_2(rws, act ? o + 3 * DM * 2 : o); cb[t] = ldsc1_2(rws, act ? o + 4 * DM * 2 : o);
          mx = umax(mx, umax(umax(umax(ga[t].x, ga[t].y), umax(sb[t].x, sb[t].y)), umax(cb[t].x, cb[t].y)));
        }
        if (wave == 0) { gbw = __builtin_amdgcn_raw_buffer_load_b32(rws, lane < 32 ? WS_MOD + (rb_ * NMOD + 5 * DM + rc0 + (lane & ~1)) * 2 : 0x7ffffff0, 0, AUX_SC1); mx = umax(mx, gbw); }
        if (__all(mx != SENT)) break;
        if (give_up(spins, L)) break;
      }
      for (unsigned spins = 0;;) {
        MEMBAR();
        const unsigned v = __builtin_amdgcn_raw_buffer_load_b32(rws, lane == 0 ? WS_YP + (((wave & 1) * 8 + b) * DM + rc0) * 4 : 0x7ffffff0, 0, AUX_SC1);
        if (__builtin_amdgcn_readfirstlane(v) != SENT) break;
        if (give_up(spins, L)) break;
        __builtin_amdgcn_s_sleep(2);
      }
      STAMP(16);
      v4u y0[3], y1[3];
      for (unsigned spins = 0;;) {
        MEMBAR();
        unsigned mx = 0;
#pragma unroll
        for (int t = 0; t < 3; ++t) {
          const int o = act ? WS_YP + (b * DM + 4 * (lane + 48 * t)) * 4 : 0x7ffffff0;
          y0[t] = ldsc1(rws, o); y1[t] = ldsc1(rws, act ? o + NB * DM * 4 : o);
        }
#pragma unroll
        for (int t = 0; t < 3; ++t) mx = umax(mx, umax(max4(y0[t]), max4(y1[t])));
        if (__all(mx != SENT)) break;
        if (give_up(spins, L)) break;
      }
      STAMP(17);
      float x1[3][4];
      float sum = 0.f;
#pragma unroll
      for (int t = 0; t < 3; ++t)
#pragma unroll
        for (int e = 0; e < 4; ++e) {
          const float yv = rb(asf(y0[t][e]) + asf(y1[t][e]));
          const float gv = (e & 1) ? bfhi(ga[t][e >> 1]) : bflo(ga[t][e >> 1]);
          const float xx = (e & 1) ? bfhi(xv[t][e >> 1]) : bflo(xv[t][e >> 1]);
          x1[t][e] = rb(xx + rb(gv * yv));
          sum += x1[t][e];
        }
      sum = x32sum(x16sum(row16sum(sum)));
      const float mean = sum * (1.f / DM);
      float vs = 0.f;
#pragma unroll
      for (int t = 0; t < 3; ++t)
#pragma unroll
        for (int e = 0; e < 4; ++e) { const float d = x1[t][e] - mean; vs += act ? d * d : 0.f; }
      vs = x32sum(x16sum(row16sum(vs)));
      const float mb = rb(mean), den = rb(__builtin_sqrtf(rb(rb(vs * (1.f / DM)) + 1e-5f)));
      const float rden = 1.f / den;
#pragma unroll
      for (int t = 0; t < 3; ++t) {
        float hv[4];
#pragma unroll
        for (int e = 0; e < 4; ++e) {
          const float cv = (e & 1) ? bfhi(cb[t][e >> 1]) : bflo(cb[t][e >> 1]);
          const float sv = (e & 1) ? bfhi(sb[t][e >> 1]) : bflo(sb[t][e >> 1]);
          const float lnv = rb(rb(x1[t][e] - mb) * rden);
          hv[e] = rb(lnv * rb(1.f + cv)) + sv;
        }
        if (act) {
          *(LAS v2u*)(L + L_H2 + b * XSTR + 8 * (lane + 48 * t)) = v2u{pk(hv[0], hv[1]), pk(hv[2], hv[3])};
          const int col = 4 * (lane + 48 * t) - rc0;
          if (b == rb_ && col >= 0 && col < 32) *(LAS f4*)(L + L_X1S + col * 4) = f4{x1[t][0], x1[t][1], x1[t][2], x1[t][3]};
        }
      }
    }
    RAWBAR();
    STAMP(18);
    {
      const int t = wave & 3, kt = wave >> 2;
      const int base = (t >> 1) ? L_WU : L_W;
      f4 D = {0.f, 0.f, 0.f, 0.f};
#pragma unroll
      for (int k6 = 0; k6 < 6; ++k6) {
        const int ks = kt * 6 + k6;
        const v4u wf = trfrag(L, base + (32 * ks + 8 * g + q4) * 64 + (t & 1) * 32 + p4 * 8, 4 * 64);
        const v4u hf = lds128(L, L_H2 + (lane & 7) * XSTR + (32 * ks + 8 * g) * 2);
        D = mfma(wf, hf, D);
      }
      if ((lane & 15) < 8) *(LAS f4*)(L + L_GU + (((kt * 4 + t) * 4 + g) * 8 + (lane & 7)) * 16) = D;
    }
    v4u wd[3];
#pragma unroll
    for (int j = 0; j < 3; ++j) wd[j] = trfrag(L, L_WD + (8 * g + q4) * (DM * 2) + (wave * 3 + j) * 32 + p4 * 8, 4 * DM * 2);
    RAWBAR();
    if (tid < 256) {
      const int b = tid & 7, fc = tid >> 3;
      const int o = (((fc >> 4) * 4 + ((fc >> 2) & 3)) * 8 + b) * 16 + (fc & 3) * 4;
      float gs = 0.f, us = 0.f;
#pragma unroll
      for (int kt = 0; kt < 3; ++kt) { gs += *(LAS float*)(L + L_GU + kt * 2048 + o); us += *(LAS float*)(L + L_GU + kt * 2048 + 1024 + o); }
      const float a = rb(gelu_t(rb(gs))) * rb(us);
      *(LAS u16*)(L + L_ACT + b * ASTR + fc * 2) = (u16)pk(a, a);
    }
    RAWBAR();
    {
      const v4u af = lds128(L, L_ACT + (lane & 7) * ASTR + g * 16);
#pragma unroll
      for (int j = 0; j < 3; ++j) {
        const f4 D = mfma(wd[j], af, f4{0.f, 0.f, 0.f, 0.f});
        const int n0 = (wave * 3 + j) * 16 + 4 * g, b = lane & 7;
        if ((lane & 15) < 8) stsc1(san4(__builtin_bit_cast(v4u, D)), rws, WS_DP + ((b * 18 + (n0 >> 5)) * 144 + f) * 128 + (n0 & 31) * 4);
      }
    }
    STAMP(19);
    {
      v4u a, b2;
      const int o = WS_DP + f * 144 * 128 + tid * 16;
      for (unsigned spins = 0;;) {
        MEMBAR();
        a = ldsc1(rws, o); b2 = ldsc1(rws, tid < 384 ? o + 768 * 16 : 0x7ffffff0);
        if (__all(umax(max4(a), max4(b2)) != SENT)) break;
        if (give_up(spins, L)) break;
        __builtin_amdgcn_s_sleep(1);
      }
      STAMP(20);
      f4 s = __builtin_bit_cast(f4, a) + __builtin_bit_cast(f4, b2);
#pragma unroll
      for (int e = 0; e < 4; ++e) { float v = s[e]; v += dpp<ROR8>(v); v = x32sum(x16sum(v)); s[e] = v; }
      if (lane < 8) *(LAS f4*)(L + L_RED2 + (wave * 8 + lane) * 16) = s;
    }
    RAWBAR();
    if (wave == 0) {
      float dn = 0.f;
#pragma unroll
      for (int w = 0; w < 12; ++w) dn += *(LAS float*)(L + L_RED2 + w * 128 + (lane & 31) * 4);
      const float gv = (lane & 1) ? bfhi(gbw) : bflo(gbw);
      const float o = *(LAS float*)(L + L_X1S + (lane & 31) * 4) + rb(gv * rb(dn));
      const unsigned two = pk(o, dpp<QX1>(o));
      if (lane < 32 && !(lane & 1)) *(GAS unsigned*)(p.out + rb_ * DM + rc0 + lane) = two;
      if (wg == 0 && lane == 0) {
        const u64 E = ((u64)ldsw(L, C_E1) << 32) | ldsw(L, C_E0);
        __hip_atomic_store((GAS u64*)(p.ws + WS_EPOCH), E + 1, __ATOMIC_RELAXED, __HIP_MEMORY_SCOPE_AGENT);
      }
      STAMP(21);
    }
  }
}

}

extern "C" void kernel_launch(void* const* d_in, const int* in_sizes, int n_in, void* d_out, int out_size, void* d_ws, size_t ws_size, hipStream_t stream) {
  ak::Params p;
  p.x = (const ak::u16*)d_in[0]; p.K = (const ak::u16*)d_in[1]; p.V = (const ak::u16*)d_in[2];
  p.wq = (const ak::u16*)d_in[3]; p.wo = (const ak::u16*)d_in[4];
  p.wg = (const ak::u16*)d_in[6]; p.wu = (const ak::u16*)d_in[7]; p.wd = (const ak::u16*)d_in[8];
  p.c = (const ak::u16*)d_in[9]; p.wm = (const ak::u16*)d_in[10];
  p.out = (ak::u16*)d_out; p.ws = (unsigned char*)d_ws;
  (void)hipFuncSetAttribute((const void*)ak::fused_kernel, hipFuncAttributeMaxDynamicSharedMemorySize, ak::L_END);
  ak::fused_kernel<<<ak::NWG, ak::NTHR, ak::L_END, stream>>>(p);
}
```

```cpp
#include <hip/hip_runtime.h>
#include <stdint.h>

namespace ak {

typedef unsigned short u16;
typedef unsigned long long u64;
typedef unsigned v4u __attribute__((ext_vector_type(4)));
typedef unsigned v2u __attribute__((ext_vector_type(2)));
typedef float f4 __attribute__((ext_vector_type(4)));
typedef float f2 __attribute__((ext_vector_type(2)));
typedef __bf16 bf8 __attribute__((ext_vector_type(8)));
typedef __bf16 bf2 __attribute__((ext_vector_type(2)));
typedef short s4v __attribute__((ext_vector_type(4)));
#define LAS __attribute__((address_space(3)))
#define GAS __attribute__((address_space(1)))
typedef __amdgpu_buffer_rsrc_t rsrc_t;

#define K_R 4
#define K_RTAIL 8
#define K_TTAIL 16
#define K_NP 2
#define K_PF 4
#define K_TRIG_WO 8
#define K_TRIG_FFN 33
#define K_NL 2
#define K_FB 0
#define K_FB1 2
#define K_FB2 6
#define K_FBW 1
#define K_FBPER 6
#define K_PRIO 1
#define K_WOPACE 40
#define K_WNT 1
#define K_FFNDELAY 0
#define K_WOEARLY 32
#define K_FFNEARLY 0
#define K_FPACE 60
#define K_PSL 2
#define K_WNEAR 16
#define K_WSL 3
#define K_MHOLD 1
#define K_STAMPS 0

constexpr int NB = 8, DM = 576, DFF = 4608, NMOD = 3456, NQ = 4096;
constexpr int NWG = 256, NTHR = 768;
constexpr int NTILE = 512;
constexpr int NP = K_NP, P = NP * 8;
constexpr float LOG2E = 1.4426950408889634f;
constexpr float SCALE = 0.08838834764831845f;
constexpr unsigned SENT = 0xFFFFFFFFu;
constexpr int AUX_NT = 2, AUX_SC1 = 16;
constexpr unsigned SPIN_CAP = 1u << 15, LDS_CAP = 1u << 18;

constexpr int WS_EPOCH = 0, WS_PRG = 1024, WS_CLR = 4096, WS_CLR2 = 6144;
constexpr int WS_MOD = 8192;
constexpr int WS_QP = 65536;
constexpr int WS_OP = WS_QP + 3 * NB * NQ * 4;
constexpr int WS_ML = WS_OP + NWG * 512;
constexpr int WS_YP = WS_ML + NWG * 16;
constexpr int WS_DP = WS_YP + 2 * NB * DM * 4;
constexpr int WS_END = WS_DP + 144 * 144 * 128;
constexpr size_t WS_STAMP = 64u << 20;
constexpr int NSTAMP = 32;

constexpr int L_W = 0;
constexpr int L_SIMG = 73728;
constexpr int L_HIMG = 24576;
constexpr int HSTR = 400;
constexpr int L_WU = 36864, L_WD = 73728;
constexpr int L_GU = 110592;
constexpr int L_ACT = L_GU + 6144;
constexpr int ASTR = 80;
constexpr int L_RED2 = L_ACT + 640;
constexpr int L_X1S = L_RED2 + 1536;
constexpr int L_H2 = L_X1S + 128;
constexpr int XSTR = 1168;
static_assert(L_H2 + 8 * XSTR <= 131072, "ffn lds");
constexpr int L_MRG = 131072;
constexpr int L_RED = L_MRG + 8448;
constexpr int L_KV = 28672, L_KVM = 83968;
constexpr int L_Q = 160128;
constexpr int L_CTL = L_Q + 512;
constexpr int L_STAMP = L_CTL + 128;
constexpr int L_END = L_STAMP + (K_STAMPS ? 12 * 32 * 8 : 0);
static_assert(L_KV + K_NL * 65536 <= L_Q && L_KVM + 65536 <= L_Q && L_END <= 163840, "lds map");
enum { C_SYNC1 = 0, C_HS = 1, C_TRIG = 2, C_MRG = 3, C_DEAD = 4, C_DMA0 = 5, C_DMA1 = 6, C_HS2 = 7, C_KVL = 10, C_PROG = 11, C_RR = 12, C_E0 = 8, C_E1 = 9 };

__host__ __device__ constexpr bool role_w(int wg) { return wg >= 144 && wg < 192 && (wg & 7) + 8 * (((wg >> 3) - 18) >> 1) < 18; }
#define K_WT_MC 130
#define K_WT_ML 90
#define K_WT_W 220
#define K_WT_F 30
#define K_WT_Q 25
constexpr int FKB = K_FFNEARLY == 1 ? 110 : (K_FFNEARLY == 2 ? 37 : 0);
constexpr int role_kb(int wg) { return wg < 18 ? K_WT_MC + FKB : wg < 54 ? K_WT_ML + FKB : role_w(wg) ? K_WT_W : wg < 144 ? K_WT_F + FKB : wg < 246 ? K_WT_Q : 0; }
struct SplitTab { unsigned v[256]; };
constexpr SplitTab make_split() {
  SplitTab t{};
  int o[256] = {};
  for (int i = 0; i < 256; ++i) o[i] = i;
  for (int i = 1; i < 256; ++i) {
    const int x = o[i]; int j = i - 1;
    while (j >= 0 && role_kb(o[j]) < role_kb(x)) { o[j + 1] = o[j]; --j; }
    o[j + 1] = x;
  }
  for (int h = 0; h < 128; ++h) {
    const int a = o[h], b = o[255 - h];
    const int n0 = 256 - (role_kb(a) - role_kb(b) + 8) / 16;
    t.v[a] = (unsigned)(h | (n0 << 8));
    t.v[b] = (unsigned)(h | 128 | (n0 << 8));
  }
  return t;
}
__device__ constexpr SplitTab SPLIT = make_split();

struct Params {
  const u16 *x, *K, *V, *wq, *wo, *wg, *wu, *wd, *c, *wm;
  u16* out;
  unsigned char* ws;
};

__device__ __forceinline__ float asf(unsigned u) { return __builtin_bit_cast(float, u); }
__device__ __forceinline__ unsigned asu(float f) { return __builtin_bit_cast(unsigned, f); }
__device__ __forceinline__ float bflo(unsigned u) { return asf(u << 16); }
__device__ __forceinline__ float bfhi(unsigned u) { return asf(u & 0xffff0000u); }
__device__ __forceinline__ unsigned pk(float a, float b) {
  f2 v = {a, b};
  return __builtin_bit_cast(unsigned, __builtin_convertvector(v, bf2));
}
__device__ __forceinline__ float rb(float a) { return bflo(pk(a, a)); }
__device__ __forceinline__ unsigned san(unsigned u) { return u < 0xFFFFFFFEu ? u : 0xFFFFFFFEu; }
__device__ __forceinline__ v4u san4(v4u v) { return v4u{san(v.x), san(v.y), san(v.z), san(v.w)}; }
__device__ __forceinline__ unsigned umax(unsigned a, unsigned b) { return a > b ? a : b; }
__device__ __forceinline__ unsigned max4(v4u v) { return umax(umax(v.x, v.y), umax(v.z, v.w)); }
template <int C> __device__ __forceinline__ float dpp(float x) {
  return asf((unsigned)__builtin_amdgcn_mov_dpp((int)asu(x), C, 0xf, 0xf, true));
}
template <int C> __device__ __forceinline__ unsigned dppu(unsigned x) { return (unsigned)__builtin_amdgcn_mov_dpp((int)x, C, 0xf, 0xf, true); }
constexpr int ROR1 = 0x121, ROR2 = 0x122, ROR4 = 0x124, ROR8 = 0x128, QX1 = 0xB1, QX2 = 0x4E;
__device__ __forceinline__ float x32max(float x) { auto t = __builtin_amdgcn_permlane32_swap(asu(x), asu(x), false, false); return fmaxf(asf(t[0]), asf(t[1])); }
__device__ __forceinline__ float x32sum(float x) { auto t = __builtin_amdgcn_permlane32_swap(asu(x), asu(x), false, false); return asf(t[0]) + asf(t[1]); }
__device__ __forceinline__ float x16sum(float x) { auto t = __builtin_amdgcn_permlane16_swap(asu(x), asu(x), false, false); return asf(t[0]) + asf(t[1]); }
__device__ __forceinline__ float row16sum(float x) { x += dpp<ROR1>(x); x += dpp<ROR2>(x); x += dpp<ROR4>(x); x += dpp<ROR8>(x); return x; }
__device__ __forceinline__ f4 mfma(v4u a, v4u b, f4 c) {
  return __builtin_amdgcn_mfma_f32_16x16x32_bf16(__builtin_bit_cast(bf8, a), __builtin_bit_cast(bf8, b), c, 0, 0, 0);
}
__device__ __forceinline__ rsrc_t mkr(const void* p, unsigned bytes) { return __builtin_amdgcn_make_buffer_rsrc((void*)p, 0, bytes, 0x00020000); }
__device__ __forceinline__ v4u ldsc1(rsrc_t r, int off) { return __builtin_amdgcn_raw_buffer_load_b128(r, off, 0, AUX_SC1); }
__device__ __forceinline__ v2u ldsc1_2(rsrc_t r, int off) { return __builtin_amdgcn_raw_buffer_load_b64(r, off, 0, AUX_SC1); }
__device__ __forceinline__ void stsc1(v4u v, rsrc_t r, int off) { __builtin_amdgcn_raw_buffer_store_b128(v, r, off, 0, AUX_SC1); }
__device__ __forceinline__ void stsc1_2(v2u v, rsrc_t r, int off) { __builtin_amdgcn_raw_buffer_store_b64(v, r, off, 0, AUX_SC1); }
#define MEMBAR() asm volatile("" ::: "memory")
#define VMWAIT(n) asm volatile("s_waitcnt vmcnt(" #n ")" ::: "memory")
#define LGWAIT() asm volatile("s_waitcnt lgkmcnt(0)" ::: "memory")
#define RAWBAR() asm volatile("s_waitcnt lgkmcnt(0)\n\ts_barrier" ::: "memory")
template <bool NT = true> __device__ __forceinline__ void dma16(rsrc_t r, int voff, unsigned soff, unsigned ldsa) {
  unsigned keep;
  if (NT) asm volatile("s_mov_b32 %0, m0\n\ts_mov_b32 m0, %1\n\ts_nop 0\n\tbuffer_load_dwordx4 %2, %3, %4 offen nt lds\n\ts_mov_b32 m0, %0" : "=&s"(keep) : "s"(ldsa), "v"(voff), "s"(r), "s"(soff) : "memory");
  else asm volatile("s_mov_b32 %0, m0\n\ts_mov_b32 m0, %1\n\ts_nop 0\n\tbuffer_load_dwordx4 %2, %3, %4 offen lds\n\ts_mov_b32 m0, %0" : "=&s"(keep) : "s"(ldsa), "v"(voff), "s"(r), "s"(soff) : "memory");
}
__device__ __forceinline__ v4u ld_hidden(rsrc_t r, int voff) {
  v4u v;
  asm volatile("buffer_load_dwordx4 %0, %1, %2, 0 offen" : "=&v"(v) : "v"(voff), "s"(r) : "memory");
  return v;
}
__device__ __forceinline__ v4u lds128(LAS unsigned char* L, int off) { return *(LAS v4u*)(L + off); }
__device__ __forceinline__ v4u trfrag(LAS unsigned char* L, int a0, int d) {
  s4v lo = __builtin_amdgcn_ds_read_tr16_b64_v4i16((LAS s4v*)(L + a0));
  s4v hi = __builtin_amdgcn_ds_read_tr16_b64_v4i16((LAS s4v*)(L + a0 + d));
  v2u l = __builtin_bit_cast(v2u, lo), h = __builtin_bit_cast(v2u, hi);
  return v4u{l.x, l.y, h.x, h.y};
}
__device__ __forceinline__ unsigned ldsw(LAS unsigned char* L, int w) { return *(volatile LAS unsigned*)(L + L_CTL + 4 * w); }
__device__ __forceinline__ void stw(LAS unsigned char* L, int w, unsigned v) { *(volatile LAS unsigned*)(L + L_CTL + 4 * w) = v; }
__device__ __forceinline__ unsigned lds_add(LAS unsigned char* L, int w) {
  unsigned o = 0;
  if ((threadIdx.x & 63) == 0) o = __hip_atomic_fetch_add((LAS unsigned*)(L + L_CTL + 4 * w), 1u, __ATOMIC_RELAXED, __HIP_MEMORY_SCOPE_WORKGROUP);
  return __builtin_amdgcn_readfirstlane(o);
}
template <int SLP = 1> __device__ __forceinline__ void lds_wait(LAS unsigned char* L, int w, unsigned n) {
  for (unsigned spins = 0; __builtin_amdgcn_readfirstlane(ldsw(L, w)) < n; ++spins) {
    if (__builtin_amdgcn_readfirstlane(ldsw(L, C_DEAD))) break;
    if (spins > LDS_CAP) { stw(L, C_DEAD, 1u); break; }
    __builtin_amdgcn_s_sleep(SLP);
  }
  MEMBAR();
}
__device__ __forceinline__ bool give_up(unsigned& spins, LAS unsigned char* L) {
  if (++spins > SPIN_CAP) { stw(L, C_DEAD, 1u); return true; }
  return (spins & 15u) == 0u && __builtin_amdgcn_readfirstlane(ldsw(L, C_DEAD)) != 0u;
}
__device__ __forceinline__ u64 mix64(u64 z) {
  z = (z ^ (z >> 30)) * 0xBF58476D1CE4E5B9ull;
  z = (z ^ (z >> 27)) * 0x94D049BB133111EBull;
  return z ^ (z >> 31);
}
#define STAMP(k) do {} while (0)
__device__ __forceinline__ void throttle(unsigned r) {
  asm volatile("s_cmp_gt_u32 %0, 6\n\ts_cbranch_scc1 9f\n\ts_waitcnt vmcnt(5)\n\t"
               "s_cmp_gt_u32 %0, 5\n\ts_cbranch_scc1 9f\n\ts_waitcnt vmcnt(4)\n\t"
               "s_cmp_gt_u32 %0, 4\n\ts_cbranch_scc1 9f\n\ts_waitcnt vmcnt(3)\n\t"
               "s_cmp_gt_u32 %0, 3\n\ts_cbranch_scc1 9f\n\ts_waitcnt vmcnt(2)\n\t"
               "s_cmp_gt_u32 %0, 2\n\ts_cbranch_scc1 9f\n\ts_waitcnt vmcnt(1)\n9:" ::"s"(r) : "scc", "memory");
}

__device__ __forceinline__ float sigm(float z) { return __builtin_amdgcn_rcpf(1.f + __builtin_amdgcn_exp2f(-LOG2E * z)); }
__device__ __forceinline__ float silu_acc(float a) {
  const float hi = 1.44269502162933349609375f, lo = 1.92596299112661746e-8f;
  const float t0 = -a * hi;
  const float r = __builtin_fmaf(-a, lo, __builtin_fmaf(-a, hi, -t0));
  const float e0 = __builtin_amdgcn_exp2f(fminf(t0, 126.f));
  const float d = 1.f + __builtin_fmaf(e0, r * 0.693147180559945f, e0);
  const float rc = __builtin_amdgcn_rcpf(d), q = a * rc;
  return __builtin_fmaf(__builtin_fmaf(-q, d, a), rc, q);
}
__device__ __forceinline__ float gelu_t(float x) { const float z = 0.7978845608028654f * (x + 0.044715f * x * x * x); return x * sigm(2.f * z); }

struct Soft { float m, l; f4 acc; };
struct Lane { v4u qa0, qa1; unsigned sel[2][4]; int idx0; };

__device__ __forceinline__ float quadsum(f4 d) {
  float t = dpp<0x00>(d.x);
  t += dpp<0x55>(d.y); t += dpp<0xAA>(d.z); t += dpp<0xFF>(d.w);
  return t;
}
__device__ __forceinline__ void softmax_tile(f4 D0, f4 D1, Soft& s, const Lane& c, unsigned& val0, unsigned& val1) {
  const unsigned r1 = pk(quadsum(D0), quadsum(D1));
  const unsigned r2 = pk(bflo(r1) * SCALE, bfhi(r1) * SCALE);
  const float s0 = bflo(r2), s1 = bfhi(r2);
  float mx = fmaxf(s0, s1);
  mx = fmaxf(mx, dpp<ROR4>(mx)); mx = fmaxf(mx, dpp<ROR8>(mx)); mx = x32max(mx);
  const float mn = fmaxf(s.m, mx);
  const float al = __builtin_amdgcn_exp2f((s.m - mn) * LOG2E);
  const float p0 = __builtin_amdgcn_exp2f((s0 - mn) * LOG2E), p1 = __builtin_amdgcn_exp2f((s1 - mn) * LOG2E);
  s.m = mn; s.l = s.l * al + (p0 + p1);
  auto t = __builtin_amdgcn_permlane16_swap(asu(al), asu(al), false, false);
  const float al0 = asf(t[0]), al1 = asf(t[1]);
  s.acc.x *= al0; s.acc.y *= al0; s.acc.z *= al1; s.acc.w *= al1;
  const unsigned pp = pk(p0, p1);
  val0 = (unsigned)__builtin_amdgcn_ds_bpermute(c.idx0, (int)pp);
  val1 = (unsigned)__builtin_amdgcn_ds_bpermute(c.idx0 + 128, (int)pp);
}
template <int U> __device__ __forceinline__ v4u pfrag(unsigned val0, unsigned val1, const Lane& c) {
  const unsigned v = (U & 1) ? val1 : val0;
  constexpr int y = U >> 1;
  return v4u{__builtin_amdgcn_perm(v, v, c.sel[y][0]), __builtin_amdgcn_perm(v, v, c.sel[y][1]), __builtin_amdgcn_perm(v, v, c.sel[y][2]), __builtin_amdgcn_perm(v, v, c.sel[y][3])};
}

__global__ void __launch_bounds__(NTHR, 3) fused_kernel(Params p) {
  extern __shared__ __attribute__((aligned(16))) unsigned char smem[];
  LAS unsigned char* L = (LAS unsigned char*)smem;
  const int tid = threadIdx.x, lane = tid & 63, wave = __builtin_amdgcn_readfirstlane(tid >> 6);
  const int wg = blockIdx.x;
  const unsigned ldsb = (unsigned)(size_t)L;
  STAMP(0);
  if (tid < 32) *(LAS unsigned*)(L + L_CTL + 4 * tid) = 0u;
  RAWBAR();
  const rsrc_t rws = mkr(p.ws, WS_END);
  const int xl = wg & 7, xi = wg >> 3;
  const bool isM = wg < 54, isMc = wg < 18, isQ = wg >= 54 && wg < 246;
  const bool isF = wg < 144;
  const int f = xl * 18 + xi;
  const int wpair = xl + 8 * ((xi - 18) >> 1);
  const bool isW = wg >= 144 && wg < 192 && wpair < 18;
  const int wsig = wpair / 9, wng = 2 * (wpair % 9) + ((xi - 18) & 1);
  const unsigned sp = SPLIT.v[wg];
  const int ahead = sp & 127, asl = (sp >> 7) & 1, an0 = sp >> 8;
  const int ab = ahead >> 4, akv = ahead & 15, aslot = ahead * 2 + asl;
  const int tbeg = asl ? an0 : 0, tend = asl ? NTILE : an0;
  const int qi = wg - 54, qcg = qi / 3, qkq = qi % 3;
  const int g = lane >> 4, q4 = (lane & 15) >> 2, p4 = lane & 3;

  if (wave < 8) {
    v2u E7 = {0u, 0u};
    if (wave == 7) {
      asm volatile("buffer_load_dwordx2 %0, %1, %2, 0 offen sc1" : "=&v"(E7) : "v"(WS_EPOCH), "s"(rws) : "memory");
      const v4u ss = {SENT, SENT, SENT, SENT};
      if (isM) stsc1(ss, rws, WS_MOD + ((lane >> 3) * NMOD + wg * 64) * 2 + (lane & 7) * 16);
      if (isQ) {
#pragma unroll
        for (int j = 0; j < 2; ++j) stsc1(ss, rws, WS_QP + (((qkq * 8 + 4 * j + (lane >> 4)) * NQ) + qcg * 64) * 4 + (lane & 15) * 16);
      }
      if (lane <= 32) stsc1(ss, rws, lane < 32 ? WS_OP + aslot * 512 + lane * 16 : WS_ML + aslot * 16);
      if (isW) stsc1(ss, rws, WS_YP + ((wsig * 8 + (lane >> 3)) * DM + wng * 32) * 4 + (lane & 7) * 16);
    }
    v4u cvm = {0u, 0u, 0u, 0u};
    if (isMc) cvm = ld_hidden(mkr(p.c, NB * DM * 2), tid * 16);
    if (isMc || isQ) {
      const int stride = isM ? NMOD * 2 : NQ * 2, n = isM ? 9 : 3;
      const rsrc_t rw = isM ? mkr(p.wm, DM * NMOD * 2) : mkr(p.wq, DM * NQ * 2);
      const int cb = isM ? wg * 128 : qcg * 128, r0 = isM ? 0 : qkq * 192;
      const int c16 = lane & 7;
#pragma unroll 1
      for (int j = 0; j < n; ++j) {
        const int i = wave * n + j;
        const int fz = ((lane >> 4) & 1) | ((i & 1) << 1);
        dma16(rw, (lane >> 3) * stride + ((((c16 >> 1) ^ fz) << 1) | (c16 & 1)) * 16 + cb, (r0 + i * 8) * stride, ldsb + L_W + i * 1024);
      }
    }
    auto say_cleared = [&]() {
      if (wave == 7) {
        const unsigned e0 = __builtin_amdgcn_readfirstlane(E7.x), e1 = __builtin_amdgcn_readfirstlane(E7.y);
        if (lane == 0) { __hip_atomic_store((GAS u64*)(p.ws + WS_CLR) + wg, mix64((((u64)e1 << 32) | e0) + 1), __ATOMIC_RELAXED, __HIP_MEMORY_SCOPE_AGENT); stw(L, C_E0, e0); stw(L, C_E1, e1); }
        LGWAIT();
        STAMP(22);
      }
    };
    if (isMc) {
      asm volatile("s_waitcnt vmcnt(9)" : "+v"(cvm), "+v"(E7)::"memory");
      __builtin_amdgcn_sched_barrier(0);
      say_cleared();
      v4u o;
#pragma unroll
      for (int e = 0; e < 4; ++e) o[e] = pk(silu_acc(bflo(cvm[e])), silu_acc(bfhi(cvm[e])));
      *(LAS v4u*)(L + L_SIMG + (tid / 72) * XSTR + (tid % 72) * 16) = o;
      asm volatile("s_waitcnt vmcnt(0) lgkmcnt(0)" ::: "memory");
      lds_add(L, C_DMA0);
    }
    const unsigned char* kb = (const unsigned char*)p.K + ((size_t)ahead << 21);
    const unsigned char* vb = (const unsigned char*)p.V + ((size_t)ahead << 21);
    const int koff = ((lane >> 2) & 3) * 256 + (g * 4 + (lane & 3)) * 16, voff = lane * 16;
    const int tw = tbeg + wave;
#define KVT(tb, n) (tw + 8 * ((tb) + ((n) >> 3)))
#define KVLOAD(tb, n) (((n) & 4) ? __builtin_amdgcn_raw_buffer_load_b128(mkr(vb, KVT(tb, n) < tend ? (1u << 21) : 0u), voff, KVT(tb, n) * 4096 + ((n) & 3) * 1024, AUX_NT) \
                                 : __builtin_amdgcn_raw_buffer_load_b128(mkr(kb, KVT(tb, n) < tend ? (1u << 21) : 0u), koff, KVT(tb, n) * 4096 + ((n) & 3) * 1024, AUX_NT))
#define SB() __builtin_amdgcn_sched_barrier(0)
    v4u ring[8], pre[P];
#pragma unroll
    for (int n = 0; n < 8 + P; ++n) {
      if (n == K_PF && !isMc) { asm volatile("s_waitcnt vmcnt(%1)" : "+v"(E7) : "n"(K_PF) : "memory"); __builtin_amdgcn_sched_barrier(0); say_cleared(); lds_add(L, C_DMA0); }
      if (n >= K_PF) throttle(K_PF);
      if (n < 8) ring[n] = KVLOAD(0, n); else pre[n - 8] = KVLOAD(0, n);
      SB();
    }
    const int nl = isMc ? (K_NL < 1 ? K_NL : 1) : K_NL;
    const int kvl = (isMc ? L_KVM : L_KV) + wave * (nl * 8192);
    {
      const rsrc_t rk = mkr(kb, 1 << 21), rv = mkr(vb, 1 << 21);
#pragma unroll 1
      for (int t = 0; t < nl; ++t) {
#pragma unroll
        for (int n = 0; n < 8; ++n) {
          throttle(K_PF);
          dma16((n & 4) ? rv : rk, (n & 4) ? voff : koff, (tw + 8 * (NP + 1 + t)) * 4096 + (n & 3) * 1024, ldsb + kvl + t * 8192 + n * 1024);
        }
      }
    }
    const int tn = NP + 1 + nl;
    Lane c;
    {
      const int i = lane & 15, beta = i >> 2, pos = i & 1;
#pragma unroll
      for (int y = 0; y < 2; ++y)
#pragma unroll
        for (int w = 0; w < 4; ++w) {
          const unsigned b0 = 2 * y, b1 = 2 * y + 1;
          c.sel[y][w] = beta != w ? 0x0c0c0c0cu : (pos ? ((b1 << 24) | (b0 << 16) | 0x0c0cu) : (0x0c0c0000u | (b1 << 8) | b0));
        }
      c.idx0 = (16 * ((i >> 1) & 1) + 4 * g) * 4;
    }
    if (K_PRIO && wave >= 4) __builtin_amdgcn_s_setprio(1);
    STAMP(1);
    RAWBAR();
    STAMP(2);
    {
      const int i = lane & 15;
      const v4u qf = lds128(L, L_Q + ((i >> 2) & 1) * 256 + (4 * g + (i & 3)) * 16);
      const v4u z = {0u, 0u, 0u, 0u};
      c.qa0 = (i >> 3) ? z : qf;
      c.qa1 = (i >> 3) ? qf : z;
    }
    Soft st;
    st.m = -INFINITY; st.l = 0.f; st.acc = f4{0.f, 0.f, 0.f, 0.f};
    const f4 zero = {0.f, 0.f, 0.f, 0.f};
    unsigned rr = K_R;
    {
      f4 D0 = mfma(c.qa0, ring[0], zero); SB(); ring[0] = KVLOAD(tn, 0); SB();
      D0 = mfma(c.qa1, ring[1], D0);      SB(); ring[1] = KVLOAD(tn, 1); SB();
      f4 D1 = mfma(c.qa0, ring[2], zero); SB(); ring[2] = KVLOAD(tn, 2); SB();
      D1 = mfma(c.qa1, ring[3], D1);      SB(); throttle(rr); ring[3] = KVLOAD(tn, 3); SB();
      unsigned v0, v1;
      softmax_tile(D0, D1, st, c, v0, v1);
      st.acc = mfma(pfrag<0>(v0, v1, c), ring[4], st.acc);
      st.acc = mfma(pfrag<1>(v0, v1, c), ring[5], st.acc);
      st.acc = mfma(pfrag<2>(v0, v1, c), ring[6], st.acc);
      st.acc = mfma(pfrag<3>(v0, v1, c), ring[7], st.acc);
      SB();
#pragma unroll
      for (int t = 0; t < NP; ++t) {
        D0 = mfma(c.qa0, pre[8 * t + 0], zero); D0 = mfma(c.qa1, pre[8 * t + 1], D0);
        D1 = mfma(c.qa0, pre[8 * t + 2], zero); D1 = mfma(c.qa1, pre[8 * t + 3], D1);
        softmax_tile(D0, D1, st, c, v0, v1);
        st.acc = mfma(pfrag<0>(v0, v1, c), pre[8 * t + 4], st.acc);
        st.acc = mfma(pfrag<1>(v0, v1, c), pre[8 * t + 5], st.acc);
        st.acc = mfma(pfrag<2>(v0, v1, c), pre[8 * t + 6], st.acc);
        st.acc = mfma(pfrag<3>(v0, v1, c), pre[8 * t + 7], st.acc);
        SB();
      }
      VMWAIT(4);
#pragma unroll 1
      for (int t = 0; t < nl; ++t) {
        const int a = kvl + t * 8192 + lane * 16;
        D0 = mfma(c.qa0, lds128(L, a), zero); D0 = mfma(c.qa1, lds128(L, a + 1024), D0);
        D1 = mfma(c.qa0, lds128(L, a + 2048), zero); D1 = mfma(c.qa1, lds128(L, a + 3072), D1);
        softmax_tile(D0, D1, st, c, v0, v1);
        st.acc = mfma(pfrag<0>(v0, v1, c), lds128(L, a + 4096), st.acc);
        st.acc = mfma(pfrag<1>(v0, v1, c), lds128(L, a + 5120), st.acc);
        st.acc = mfma(pfrag<2>(v0, v1, c), lds128(L, a + 6144), st.acc);
        st.acc = mfma(pfrag<3>(v0, v1, c), lds128(L, a + 7168), st.acc);
      }
      LGWAIT();
      lds_add(L, C_KVL);
      throttle(rr); ring[4] = KVLOAD(tn, 4); SB();
      throttle(rr); ring[5] = KVLOAD(tn, 5); SB();
      throttle(rr); ring[6] = KVLOAD(tn, 6); SB();
      throttle(rr); ring[7] = KVLOAD(tn, 7); SB();
    }
#pragma unroll 1
    for (int t0 = tn; tw + 8 * t0 < tend; ++t0) {
      if (t0 == K_TRIG_WO && lane == 0 && wave == 0) stw(L, C_TRIG, 1u);
      if (tw + 8 * t0 + K_TTAIL >= tend) rr = K_RTAIL;
      f4 D0 = mfma(c.qa0, ring[0], zero); SB(); throttle(rr); ring[0] = KVLOAD(t0 + 1, 0); SB();
      D0 = mfma(c.qa1, ring[1], D0);      SB(); throttle(rr); ring[1] = KVLOAD(t0 + 1, 1); SB();
      f4 D1 = mfma(c.qa0, ring[2], zero); SB(); throttle(rr); ring[2] = KVLOAD(t0 + 1, 2); SB();
      D1 = mfma(c.qa1, ring[3], D1);      SB(); throttle(rr); ring[3] = KVLOAD(t0 + 1, 3); SB();
      unsigned v0, v1;
      softmax_tile(D0, D1, st, c, v0, v1);
      st.acc = mfma(pfrag<0>(v0, v1, c), ring[4], st.acc); SB(); throttle(rr); ring[4] = KVLOAD(t0 + 1, 4); SB();
      st.acc = mfma(pfrag<1>(v0, v1, c), ring[5], st.acc); SB(); throttle(rr); ring[5] = KVLOAD(t0 + 1, 5); SB();
      st.acc = mfma(pfrag<2>(v0, v1, c), ring[6], st.acc); SB(); throttle(rr); ring[6] = KVLOAD(t0 + 1, 6); SB();
      st.acc = mfma(pfrag<3>(v0, v1, c), ring[7], st.acc); SB(); throttle(rr); ring[7] = KVLOAD(t0 + 1, 7); SB();
    }
#undef KVLOAD
    if (lds_add(L, 18) == 7u && lane == 0) stw(L, C_TRIG, 2u);
    STAMP(3);
    float l = st.l;
    l += dpp<ROR4>(l); l += dpp<ROR8>(l); l = x32sum(l);
    LAS float* mg = (LAS float*)(L + L_MRG) + wave * 264;
    const int cc = lane & 15;
    *(LAS f2*)(mg + 8 + 8 * cc + 2 * g) = f2{st.acc.x, st.acc.y};
    *(LAS f2*)(mg + 8 + 128 + 8 * cc + 2 * g) = f2{st.acc.z, st.acc.w};
    if (lane == 0 || lane == 16) *(LAS f2*)(mg + 2 * g) = f2{st.m, l};
    LGWAIT();
    if (lds_add(L, C_MRG) == 7u) {
      MEMBAR();
      const int h = lane >> 5, d0 = 4 * (lane & 31);
      LAS float* m0 = (LAS float*)(L + L_MRG);
      f2 ml[8]; f4 a[8];
#pragma unroll
      for (int w = 0; w < 8; ++w) { ml[w] = *(LAS f2*)(m0 + w * 264 + 2 * h); a[w] = *(LAS f4*)(m0 + w * 264 + 8 + 128 * h + d0); }
      float M = ml[0].x;
#pragma unroll
      for (int w = 1; w < 8; ++w) M = fmaxf(M, ml[w].x);
      float Ls = 0.f; f4 O = {0.f, 0.f, 0.f, 0.f};
#pragma unroll
      for (int w = 0; w < 8; ++w) { const float e = __builtin_amdgcn_exp2f((ml[w].x - M) * LOG2E); Ls += e * ml[w].y; O += e * a[w]; }
      const float inv = __builtin_amdgcn_rcpf(Ls);
      const unsigned o0 = pk(O.x * inv, O.y * inv), o1 = pk(O.z * inv, O.w * inv);
      const unsigned n0 = dppu<QX1>(o0), n1 = dppu<QX1>(o1);
      if (!(lane & 1)) stsc1(san4(v4u{o0, o1, n0, n1}), rws, WS_OP + aslot * 512 + (h * 128 + d0) * 2);
      const unsigned M1 = __builtin_amdgcn_readlane(asu(M), 32), L1 = __builtin_amdgcn_readlane(asu(Ls), 32);
      if (lane == 0) stsc1(san4(v4u{asu(M), asu(Ls), M1, L1}), rws, WS_ML + aslot * 16);
      STAMP(4);
    }
  } else {
    const int sw = wave - 8, stid = tid - 512;
    const u64 Es = __hip_atomic_load((GAS u64*)(p.ws + WS_EPOCH), __ATOMIC_RELAXED, __HIP_MEMORY_SCOPE_AGENT);
    const int fz = (q4 >> 1) | ((g & 1) << 1);
    auto mod_mfma = [&]() {
      STAMP(5);
      f4 D[3] = {{0.f, 0.f, 0.f, 0.f}, {0.f, 0.f, 0.f, 0.f}, {0.f, 0.f, 0.f, 0.f}};
#pragma unroll
      for (int ks = 0; ks < 18; ++ks) {
        const v4u wf = trfrag(L, L_W + (32 * ks + 8 * g + q4) * 128 + (sw ^ fz) * 32 + p4 * 8, 4 * 128);
        const v4u sf = lds128(L, L_SIMG + (lane & 7) * XSTR + (32 * ks + 8 * g) * 2);
        D[ks % 3] = mfma(wf, sf, D[ks % 3]);
      }
      const f4 Ds = (D[0] + D[1]) + D[2];
      if ((lane & 15) < 8) stsc1_2(v2u{san(pk(Ds.x, Ds.y)), san(pk(Ds.z, Ds.w))}, rws, WS_MOD + ((lane & 7) * NMOD + wg * 64 + sw * 16 + 4 * g) * 2);
      STAMP(6);
    };
    const u64 tagv = mix64((((u64)__builtin_amdgcn_readfirstlane((unsigned)(Es >> 32)) << 32) | (unsigned)__builtin_amdgcn_readfirstlane((unsigned)Es)) + 1);
    const unsigned tg0 = (unsigned)tagv, tg1 = (unsigned)(tagv >> 32);
    if (isMc) {
      if (sw == 0) {
        const v4u cv = __builtin_amdgcn_raw_buffer_load_b128(mkr(p.c, NB * DM * 2), tid * 16, 0, 0);
        v4u o;
#pragma unroll
        for (int e = 0; e < 4; ++e) o[e] = pk(silu_acc(bflo(cv[e])), silu_acc(bfhi(cv[e])));
        *(LAS v4u*)(L + L_SIMG + (tid / 72) * XSTR + (tid % 72) * 16) = o;
        LGWAIT();
        lds_add(L, C_SYNC1);
      }
      STAMP(23);
      lds_wait(L, C_SYNC1, 1); lds_wait(L, C_DMA0, 8);
      mod_mfma();
    } else if (isQ) {
      const int qb = 2 * sw + (lane >> 5), qj = lane & 31;
      const bool qact = qj < 24;
      const rsrc_t rx = mkr(p.x, NB * DM * 2);
      v4u xv[3];
#pragma unroll
      for (int t = 0; t < 3; ++t) xv[t] = __builtin_amdgcn_raw_buffer_load_b128(rx, qact ? (qb * DM + 192 * t + 8 * qj) * 2 : 0x7ffffff0, 0, 0);
      float xf[3][8];
      float sum = 0.f;
#pragma unroll
      for (int t = 0; t < 3; ++t)
#pragma unroll
        for (int e = 0; e < 4; ++e) { xf[t][2 * e] = bflo(xv[t][e]); xf[t][2 * e + 1] = bfhi(xv[t][e]); sum += xf[t][2 * e] + xf[t][2 * e + 1]; }
      sum = x16sum(row16sum(sum));
      const float mean = sum * (1.f / DM);
      float vs = 0.f;
#pragma unroll
      for (int t = 0; t < 3; ++t)
#pragma unroll
        for (int e = 0; e < 8; ++e) { const float d = xf[t][e] - mean; vs += qact ? d * d : 0.f; }
      vs = x16sum(row16sum(vs));
      const float mb = rb(mean), den = rb(__builtin_sqrtf(rb(rb(vs * (1.f / DM)) + 1e-5f)));
      float ln[8];
#pragma unroll
      for (int e = 0; e < 8; ++e) { const float xe = qkq == 0 ? xf[0][e] : (qkq == 1 ? xf[1][e] : xf[2][e]); ln[e] = rb(rb(xe - mb) / den); }
      lds_wait(L, C_DMA0, 8);
      v4u wf[6];
#pragma unroll
      for (int ks = 0; ks < 6; ++ks) wf[ks] = trfrag(L, L_W + (32 * ks + 8 * g + q4) * 128 + (sw ^ fz) * 32 + p4 * 8, 4 * 128);
      STAMP(29);
      v4u sa, ca;
      const int mo = qact ? WS_MOD + (qb * NMOD + 192 * qkq + 8 * qj) * 2 : 0x7ffffff0;
      if (sw == 0) {
        const int pw = (lane < 3 ? 0 : 6) + 3 * qkq + lane;
        const int fo = lane < 6 ? WS_CLR + pw * 8 : 0x7ffffff0, io = lane < 6 ? WS_MOD + (7 * NMOD + pw * 64 + 62) * 2 : 0x7ffffff0;
        bool cleared = false;
        for (unsigned spins = 0;;) {
          MEMBAR();
          if (!cleared) { const v2u fl = ldsc1_2(rws, fo); cleared = __all(lane >= 6 || (fl.x == tg0 && fl.y == tg1)); }
          else if (__all(__builtin_amdgcn_raw_buffer_load_b32(rws, io, 0, AUX_SC1) != SENT)) break;
          if (give_up(spins, L)) break;
          __builtin_amdgcn_s_sleep(K_PSL);
        }
        if (lane == 0) stw(L, C_HS, 1u);
      } else lds_wait(L, C_HS, 1);
      for (unsigned spins = 0;;) {
        MEMBAR();
        sa = ldsc1(rws, mo); ca = ldsc1(rws, qact ? mo + DM * 2 : mo);
        if (__all(umax(max4(sa), max4(ca)) != SENT)) break;
        if (give_up(spins, L)) break;
        __builtin_amdgcn_s_sleep(K_PSL);
      }
      STAMP(8);
      v4u h;
#pragma unroll
      for (int e = 0; e < 4; ++e) {
        const unsigned c1 = pk(1.f + bflo(ca[e]), 1.f + bfhi(ca[e]));
        const unsigned m1 = pk(ln[2 * e] * bflo(c1), ln[2 * e + 1] * bfhi(c1));
        h[e] = pk(bflo(m1) + bflo(sa[e]), bfhi(m1) + bfhi(sa[e]));
      }
      if (qact) *(LAS v4u*)(L + L_HIMG + qb * HSTR + qj * 16) = h;
      LGWAIT();
      lds_add(L, C_SYNC1); lds_wait(L, C_SYNC1, 4);
      f4 D0 = {0.f, 0.f, 0.f, 0.f}, D1 = {0.f, 0.f, 0.f, 0.f};
#pragma unroll
      for (int ks = 0; ks < 6; ks += 2) {
        D0 = mfma(wf[ks], lds128(L, L_HIMG + (lane & 7) * HSTR + (32 * ks + 8 * g) * 2), D0);
        D1 = mfma(wf[ks + 1], lds128(L, L_HIMG + (lane & 7) * HSTR + (32 * ks + 32 + 8 * g) * 2), D1);
      }
      if ((lane & 15) < 8) stsc1(san4(__builtin_bit_cast(v4u, D0 + D1)), rws, WS_QP + ((qkq * 8 + (lane & 7)) * NQ + qcg * 64 + sw * 16 + 4 * g) * 4);
      STAMP(9);
    }
    if (sw == 0) {
      v4u a0, a1, a2;
      const int qo = WS_QP + (ab * NQ + akv * 256 + 4 * lane) * 4;
      const int fo = lane < 12 ? WS_CLR + (54 + 12 * akv + lane) * 8 : 0x7ffffff0;
      const int io = lane < 12 ? WS_QP + (((lane % 3) * 8 + ab) * NQ + (4 * akv + lane / 3) * 64 + 63) * 4 : 0x7ffffff0;
      bool cleared = false;
      for (unsigned spins = 0;;) {
        MEMBAR();
        if (!cleared) { const v2u fl = ldsc1_2(rws, fo); cleared = __all(lane >= 12 || (fl.x == tg0 && fl.y == tg1)); }
        else if (__all(__builtin_amdgcn_raw_buffer_load_b32(rws, io, 0, AUX_SC1) != SENT)) break;
        if (give_up(spins, L)) break;
        __builtin_amdgcn_s_sleep(K_PSL);
      }
      for (unsigned spins = 0;;) {
        MEMBAR();
        a0 = ldsc1(rws, qo); a1 = ldsc1(rws, qo + NB * NQ * 4); a2 = ldsc1(rws, qo + 2 * NB * NQ * 4);
        if (__all(umax(umax(max4(a0), max4(a1)), max4(a2)) != SENT)) break;
        if (give_up(spins, L)) break;
        __builtin_amdgcn_s_sleep(K_PSL);
      }
      const f4 s = (__builtin_bit_cast(f4, a0) + __builtin_bit_cast(f4, a1)) + __builtin_bit_cast(f4, a2);
      *(LAS v2u*)(L + L_Q + lane * 8) = v2u{pk(s.x, s.y), pk(s.z, s.w)};
      STAMP(10);
    }
    RAWBAR();
    if (isM && !isMc) {
      lds_wait(L, C_DMA0, 8); lds_wait(L, C_KVL, 8);
      const rsrc_t rw = mkr(p.wm, DM * NMOD * 2);
      const int c16 = lane & 7;
#pragma unroll 1
      for (int j = 0; j < 18; ++j) {
        const int i = sw * 18 + j;
        const int fs = ((lane >> 4) & 1) | ((i & 1) << 1);
        dma16(rw, (lane >> 3) * (NMOD * 2) + ((((c16 >> 1) ^ fs) << 1) | (c16 & 1)) * 16 + wg * 128, i * 8 * (NMOD * 2), ldsb + L_W + i * 1024);
        if (K_WOPACE) __builtin_amdgcn_s_sleep(K_WOPACE);
      }
      const rsrc_t rc = mkr(p.c, NB * DM * 2);
#pragma unroll 1
      for (int j = 0; j < 3; ++j) {
        const int ch = stid + 256 * j;
        const v4u cv = __builtin_amdgcn_raw_buffer_load_b128(rc, ch * 16, 0, 0);
        v4u o;
#pragma unroll
        for (int e = 0; e < 4; ++e) o[e] = pk(silu_acc(bflo(cv[e])), silu_acc(bfhi(cv[e])));
        if (ch < 576) *(LAS v4u*)(L + L_SIMG + (ch / 72) * XSTR + (ch % 72) * 16) = o;
      }
      VMWAIT(0); LGWAIT();
      lds_add(L, C_SYNC1); lds_wait(L, C_SYNC1, 4);
      mod_mfma();
    }
    if (sw == 3) {
      if (isF) {
        const v4u ss = {SENT, SENT, SENT, SENT};
#pragma unroll 3
        for (int j = 0; j < 18; ++j) stsc1(ss, rws, WS_DP + ((j * 8 + (lane >> 3)) * 144 + f) * 128 + (lane & 7) * 16);
        VMWAIT(0);
        if (lane == 0) __hip_atomic_store((GAS u64*)(p.ws + WS_CLR2) + f, tagv, __ATOMIC_RELAXED, __HIP_MEMORY_SCOPE_AGENT);
      }
      for (unsigned spins = 0;;) {
        MEMBAR();
        const v4u a = ldsc1(rws, WS_CLR + lane * 32), b = ldsc1(rws, WS_CLR + lane * 32 + 16);
        const v4u c2 = ldsc1(rws, lane < 36 ? WS_CLR2 + lane * 32 : 0x7ffffff0), d2 = ldsc1(rws, lane < 36 ? WS_CLR2 + lane * 32 + 16 : 0x7ffffff0);
        bool ok = a.x == tg0 && a.y == tg1 && a.z == tg0 && a.w == tg1 && b.x == tg0 && b.y == tg1 && b.z == tg0 && b.w == tg1;
        if (isF) ok = ok && (lane >= 36 || (c2.x == tg0 && c2.y == tg1 && c2.z == tg0 && c2.w == tg1 && d2.x == tg0 && d2.y == tg1 && d2.z == tg0 && d2.w == tg1));
        if (__all(ok)) break;
        if (give_up(spins, L)) break;
        __builtin_amdgcn_s_sleep(16);
      }
      if (lane == 0) stw(L, C_HS2, 1u);
      STAMP(24);
    }
    if (isW || isF) {
      if (isM && !isMc) { lds_add(L, 17); lds_wait(L, 17, 4); }
      lds_wait(L, C_KVL, 8);
      const int sc = ((lane & 3) ^ ((lane >> 4) & 2)) * 16;
      if (isW) {
        const rsrc_t rw = mkr(p.wo, NQ * DM * 2);
        const int vo = (lane >> 2) * (DM * 2) + sc + wng * 64;
        lds_wait<16>(L, C_TRIG, 1);
        STAMP(11);
#pragma unroll 1
        for (int j = 0; j < K_WOEARLY; ++j) { const int i = j * 4 + sw; dma16<K_WNT != 0>(rw, vo, (wsig * 2048 + i * 16) * DM * 2, ldsb + L_W + i * 1024); if (K_WOPACE) __builtin_amdgcn_s_sleep(K_WOPACE); }
        lds_wait<4>(L, C_TRIG, 2);
#pragma unroll 1
        for (int j = K_WOEARLY; j < 32; ++j) { const int i = j * 4 + sw; dma16<K_WNT != 0>(rw, vo, (wsig * 2048 + i * 16) * DM * 2, ldsb + L_W + i * 1024); }
      } else {
        const rsrc_t rg = mkr(p.wg, DM * DFF * 2), ru = mkr(p.wu, DM * DFF * 2), rd = mkr(p.wd, DFF * DM * 2);
        const int vo = (lane >> 2) * (DFF * 2) + sc + f * 64;
        lds_wait<4>(L, C_TRIG, K_FFNEARLY ? 1 : 2);
        STAMP(11);
        for (int k = 0; k < K_FFNDELAY; ++k) __builtin_amdgcn_s_sleep(16);
#pragma unroll 1
        for (int j = 0; j < 9; ++j) { const int i = j * 4 + sw; dma16(rd, lane * 16, f * 32 * DM * 2 + i * 1024, ldsb + L_WD + i * 1024); if (K_FFNEARLY) __builtin_amdgcn_s_sleep(K_FPACE); }
        if (K_FFNEARLY == 2) lds_wait<4>(L, C_TRIG, 2);
#pragma unroll 1
        for (int j = 0; j < 9; ++j) {
          const int i = j * 4 + sw;
          dma16<K_WNT != 0>(rg, vo, i * 16 * DFF * 2, ldsb + L_W + i * 1024); if (K_FFNEARLY == 1) __builtin_amdgcn_s_sleep(K_FPACE);
          dma16<K_WNT != 0>(ru, vo, i * 16 * DFF * 2, ldsb + L_WU + i * 1024); if (K_FFNEARLY == 1) __builtin_amdgcn_s_sleep(K_FPACE);
        }
      }
      VMWAIT(0);
      lds_add(L, C_DMA1);
      STAMP(12);
    }
  }

  if (isW) {
    const int cc = lane & 15;
    if (wave < 8) {
      const int pid = (cc & 7) * 32 + (wsig * 8 + wave) * 2 + (cc >> 3);
      lds_wait(L, C_HS2, 1);
      lds_wait(L, C_DMA1, 4);
      v4u wf[2][4][2];
#pragma unroll
      for (int hh = 0; hh < 2; ++hh)
#pragma unroll
        for (int ks = 0; ks < 4; ++ks)
#pragma unroll
          for (int nt = 0; nt < 2; ++nt) wf[hh][ks][nt] = trfrag(L, L_W + (wave * 256 + hh * 128 + 32 * ks + 8 * g + q4) * 64 + (nt ^ (g & 1)) * 32 + p4 * 8, 4 * 64);
      v4u ml, fr[2][4];
      for (unsigned spins = 0;;) {
        MEMBAR();
        ml = ldsc1(rws, WS_ML + pid * 16);
        if (__builtin_popcountll(__builtin_amdgcn_ballot_w64(max4(ml) != SENT)) >= 4 * K_WNEAR) break;
        if (give_up(spins, L)) break;
        __builtin_amdgcn_s_sleep(K_WSL);
      }
      STAMP(13);
      for (unsigned spins = 0;;) {
        MEMBAR();
        ml = ldsc1(rws, WS_ML + pid * 16);
#pragma unroll
        for (int hh = 0; hh < 2; ++hh)
#pragma unroll
          for (int ks = 0; ks < 4; ++ks) fr[hh][ks] = ldsc1(rws, WS_OP + pid * 512 + (hh * 128 + 32 * ks + 8 * g) * 2);
        unsigned mx = max4(ml);
#pragma unroll
        for (int hh = 0; hh < 2; ++hh)
#pragma unroll
          for (int ks = 0; ks < 4; ++ks) mx = umax(mx, max4(fr[hh][ks]));
        if (__all(mx != SENT)) break;
        if (give_up(spins, L)) break;
      }
      float wgt[2];
#pragma unroll
      for (int hh = 0; hh < 2; ++hh) {
        const float M = asf(ml[2 * hh]), Lv = asf(ml[2 * hh + 1]);
        const float M2 = dpp<ROR8>(M), L2 = dpp<ROR8>(Lv), Mx = fmaxf(M, M2);
        const float e1 = __builtin_amdgcn_exp2f((M - Mx) * LOG2E) * Lv, e2 = __builtin_amdgcn_exp2f((M2 - Mx) * LOG2E) * L2;
        wgt[hh] = e1 * __builtin_amdgcn_rcpf(e1 + e2);
      }
      STAMP(14);
#pragma unroll
      for (int nt = 0; nt < 2; ++nt) {
        f4 d0 = {0.f, 0.f, 0.f, 0.f}, d1 = {0.f, 0.f, 0.f, 0.f};
#pragma unroll
        for (int ks = 0; ks < 4; ++ks) { d0 = mfma(wf[0][ks][nt], fr[0][ks], d0); d1 = mfma(wf[1][ks][nt], fr[1][ks], d1); }
        f4 t = wgt[0] * d0 + wgt[1] * d1;
        t.x += dpp<ROR8>(t.x); t.y += dpp<ROR8>(t.y); t.z += dpp<ROR8>(t.z); t.w += dpp<ROR8>(t.w);
        if (cc < 8) *(LAS f4*)(L + L_RED + (((wave * 2 + nt) * 4 + g) * 8 + cc) * 16) = t;
      }
      LGWAIT();
      if (lds_add(L, C_SYNC1 + 16) == 7u) {
        MEMBAR();
        f4 s = {0.f, 0.f, 0.f, 0.f};
#pragma unroll
        for (int w = 0; w < 8; ++w) s += *(LAS f4*)(L + L_RED + (w * 64 + lane) * 16);
        const int nt = lane >> 5, rr = (lane >> 3) & 3, b = lane & 7;
        stsc1(san4(__builtin_bit_cast(v4u, s)), rws, WS_YP + ((wsig * 8 + b) * DM + wng * 32 + nt * 16 + 4 * rr) * 4);
        STAMP(15);
      }
    }
  } else if (isF) {
    const int rb_ = f / 18, rc0 = (f % 18) * 32;
    unsigned gbw = 0;
    const int gt = wave & 3, kt = wave >> 2;
    v4u wf[6], wd[4];
    const int dp0 = wave < 6 ? 2 * wave : 6 + wave;
    if (wave < 8) {
      const int b = wave;
      const bool act = lane < 48;
      const rsrc_t rx = mkr(p.x, NB * DM * 2);
      v2u xv[3], ga[3], sb[3], cb[3];
      lds_wait(L, C_HS2, 1);
#pragma unroll
      for (int t = 0; t < 3; ++t) xv[t] = __builtin_amdgcn_raw_buffer_load_b64(rx, act ? (b * DM + 4 * (lane + 48 * t)) * 2 : 0x7ffffff0, 0, 0);
      for (unsigned spins = 0;;) {
        MEMBAR();
        unsigned mx = 0;
#pragma unroll
        for (int t = 0; t < 3; ++t) {
          const int o = act ? WS_MOD + (b * NMOD + 4 * (lane + 48 * t)) * 2 : 0x7ffffff0;
          ga[t] = ldsc1_2(rws, act ? o + 2 * DM * 2 : o); sb[t] = ldsc1_2(rws, act ? o + 3 * DM * 2 : o); cb[t] = ldsc1_2(rws, act ? o + 4 * DM * 2 : o);
          mx = umax(mx, umax(umax(umax(ga[t].x, ga[t].y), umax(sb[t].x, sb[t].y)), umax(cb[t].x, cb[t].y)));
        }
        if (wave == 0) { gbw = __builtin_amdgcn_raw_buffer_load_b32(rws, lane < 32 ? WS_MOD + (rb_ * NMOD + 5 * DM + rc0 + (lane & ~1)) * 2 : 0x7ffffff0, 0, AUX_SC1); mx = umax(mx, gbw); }
        if (__all(mx != SENT)) break;
        if (give_up(spins, L)) break;
      }
      float xf[12], gf[12], cf[12], sf[12];
#pragma unroll
      for (int t = 0; t < 3; ++t)
#pragma unroll
        for (int e = 0; e < 2; ++e) {
          xf[4 * t + 2 * e] = bflo(xv[t][e]); xf[4 * t + 2 * e + 1] = bfhi(xv[t][e]);
          gf[4 * t + 2 * e] = bflo(ga[t][e]); gf[4 * t + 2 * e + 1] = bfhi(ga[t][e]);
          cf[4 * t + 2 * e] = 1.f + bflo(cb[t][e]); cf[4 * t + 2 * e + 1] = 1.f + bfhi(cb[t][e]);
          sf[4 * t + 2 * e] = bflo(sb[t][e]); sf[4 * t + 2 * e + 1] = bfhi(sb[t][e]);
        }
#pragma unroll
      for (int e = 0; e < 12; ++e) asm volatile("" : "+v"(xf[e]), "+v"(gf[e]), "+v"(cf[e]), "+v"(sf[e]));
      lds_wait(L, C_DMA1, 4);
#pragma unroll
      for (int k6 = 0; k6 < 6; ++k6) wf[k6] = trfrag(L, ((gt >> 1) ? L_WU : L_W) + (32 * (kt * 6 + k6) + 8 * g + q4) * 64 + ((gt & 1) ^ (g & 1)) * 32 + p4 * 8, 4 * 64);
#pragma unroll
      for (int j = 0; j < 4; ++j) wd[j] = trfrag(L, L_WD + (8 * g + q4) * (DM * 2) + ((j < 2 || wave < 6) ? 2 * dp0 + j : 0) * 32 + p4 * 8, 4 * DM * 2);
      for (unsigned spins = 0;;) {
        MEMBAR();
        const unsigned v = __builtin_amdgcn_raw_buffer_load_b32(rws, lane == 0 ? WS_YP + (((wave & 1) * 8 + b) * DM + rc0) * 4 : 0x7ffffff0, 0, AUX_SC1);
        if (__builtin_amdgcn_readfirstlane(v) != SENT) break;
        if (give_up(spins, L)) break;
        __builtin_amdgcn_s_sleep(1);
      }
      STAMP(16);
      v4u y0[3], y1[3];
      for (unsigned spins = 0;;) {
        MEMBAR();
        unsigned mx = 0;
#pragma unroll
        for (int t = 0; t < 3; ++t) {
          const int o = act ? WS_YP + (b * DM + 4 * (lane + 48 * t)) * 4 : 0x7ffffff0;
          y0[t] = ldsc1(rws, o); y1[t] = ldsc1(rws, act ? o + NB * DM * 4 : o);
        }
#pragma unroll
        for (int t = 0; t < 3; ++t) mx = umax(mx, umax(max4(y0[t]), max4(y1[t])));
        if (__all(mx != SENT)) break;
        if (give_up(spins, L)) break;
      }
      STAMP(17);
      float x1[12];
#pragma unroll
      for (int e = 0; e < 12; ++e) x1[e] = __builtin_fmaf(gf[e], asf(y0[e >> 2][e & 3]) + asf(y1[e >> 2][e & 3]), xf[e]);
      const float sh = asf(__builtin_amdgcn_readfirstlane(asu(x1[0])));
      float s1 = 0.f, s2 = 0.f;
#pragma unroll
      for (int e = 0; e < 12; ++e) { const float d = x1[e] - sh; s1 += d; s2 = __builtin_fmaf(d, d, s2); }
      s1 = act ? s1 : 0.f; s2 = act ? s2 : 0.f;
      s1 += dpp<ROR1>(s1); s2 += dpp<ROR1>(s2); s1 += dpp<ROR2>(s1); s2 += dpp<ROR2>(s2);
      s1 += dpp<ROR4>(s1); s2 += dpp<ROR4>(s2); s1 += dpp<ROR8>(s1); s2 += dpp<ROR8>(s2);
      s1 = x16sum(s1); s2 = x16sum(s2); s1 = x32sum(s1); s2 = x32sum(s2);
      const float dm = s1 * (1.f / DM), var = fmaxf(__builtin_fmaf(-dm, dm, s2 * (1.f / DM)), 0.f);
      const float rstd = __builtin_amdgcn_rsqf(var + 1e-5f), mo = -(sh + dm) * rstd;
#pragma unroll
      for (int t = 0; t < 3; ++t) {
        float hv[4];
#pragma unroll
        for (int e = 0; e < 4; ++e) hv[e] = __builtin_fmaf(__builtin_fmaf(x1[4 * t + e], rstd, mo), cf[4 * t + e], sf[4 * t + e]);
        if (act) {
          *(LAS v2u*)(L + L_H2 + b * XSTR + 8 * (lane + 48 * t)) = v2u{pk(hv[0], hv[1]), pk(hv[2], hv[3])};
          const int col = 4 * (lane + 48 * t) - rc0;
          if (b == rb_ && col >= 0 && col < 32) *(LAS f4*)(L + L_X1S + col * 4) = f4{x1[4 * t], x1[4 * t + 1], x1[4 * t + 2], x1[4 * t + 3]};
        }
      }
    } else {
      lds_wait(L, C_DMA1, 4);
#pragma unroll
      for (int k6 = 0; k6 < 6; ++k6) wf[k6] = trfrag(L, ((gt >> 1) ? L_WU : L_W) + (32 * (kt * 6 + k6) + 8 * g + q4) * 64 + ((gt & 1) ^ (g & 1)) * 32 + p4 * 8, 4 * 64);
#pragma unroll
      for (int j = 0; j < 4; ++j) wd[j] = trfrag(L, L_WD + (8 * g + q4) * (DM * 2) + ((j < 2 || wave < 6) ? 2 * dp0 + j : 0) * 32 + p4 * 8, 4 * DM * 2);
    }
    RAWBAR();
    STAMP(18);
    {
      f4 D0 = {0.f, 0.f, 0.f, 0.f}, D1 = {0.f, 0.f, 0.f, 0.f};
#pragma unroll
      for (int k6 = 0; k6 < 6; k6 += 2) {
        D0 = mfma(wf[k6], lds128(L, L_H2 + (lane & 7) * XSTR + (32 * (kt * 6 + k6) + 8 * g) * 2), D0);
        D1 = mfma(wf[k6 + 1], lds128(L, L_H2 + (lane & 7) * XSTR + (32 * (kt * 6 + k6 + 1) + 8 * g) * 2), D1);
      }
      if ((lane & 15) < 8) *(LAS f4*)(L + L_GU + (((kt * 4 + gt) * 4 + g) * 8 + (lane & 7)) * 16) = D0 + D1;
    }
    RAWBAR();
    if (tid < 256) {
      const int b = tid & 7, fc = tid >> 3;
      const int o = (((fc >> 4) * 4 + ((fc >> 2) & 3)) * 8 + b) * 16 + (fc & 3) * 4;
      float gs = 0.f, us = 0.f;
#pragma unroll
      for (int k3 = 0; k3 < 3; ++k3) { gs += *(LAS float*)(L + L_GU + k3 * 2048 + o); us += *(LAS float*)(L + L_GU + k3 * 2048 + 1024 + o); }
      const float a = rb(gelu_t(rb(gs))) * rb(us);
      *(LAS u16*)(L + L_ACT + b * ASTR + fc * 2) = (u16)pk(a, a);
    }
    RAWBAR();
    {
      const v4u af = lds128(L, L_ACT + (lane & 7) * ASTR + g * 16);
      const int b = lane & 7, hi = (lane >> 3) & 1;
#pragma unroll
      for (int j = 0; j < 2; ++j) {
        if (j == 0 || wave < 6) {
          const f4 Da = mfma(wd[2 * j], af, f4{0.f, 0.f, 0.f, 0.f}), Db = mfma(wd[2 * j + 1], af, f4{0.f, 0.f, 0.f, 0.f});
          const f4 D = hi ? Db : Da;
          stsc1(san4(__builtin_bit_cast(v4u, D)), rws, WS_DP + ((b * 18 + dp0 + j) * 144 + f) * 128 + hi * 64 + g * 16);
        }
      }
    }
    STAMP(19);
    {
      v4u a, b2;
      const int o = WS_DP + f * 144 * 128 + tid * 16;
      for (unsigned spins = 0;;) {
        MEMBAR();
        a = ldsc1(rws, o); b2 = ldsc1(rws, tid < 384 ? o + 768 * 16 : 0x7ffffff0);
        if (__all(umax(max4(a), max4(b2)) != SENT)) break;
        if (give_up(spins, L)) break;
      }
      STAMP(20);
      f4 s = __builtin_bit_cast(f4, a) + __builtin_bit_cast(f4, b2);
#pragma unroll
      for (int e = 0; e < 4; ++e) { float v = s[e]; v += dpp<ROR8>(v); v = x32sum(x16sum(v)); s[e] = v; }
      if (lane < 8) *(LAS f4*)(L + L_RED2 + (wave * 8 + lane) * 16) = s;
    }
    RAWBAR();
    if (wave == 0) {
      float dn = 0.f;
#pragma unroll
      for (int w = 0; w < 12; ++w) dn += *(LAS float*)(L + L_RED2 + w * 128 + (lane & 31) * 4);
      const float gv = (lane & 1) ? bfhi(gbw) : bflo(gbw);
      const float o = *(LAS float*)(L + L_X1S + (lane & 31) * 4) + rb(gv * rb(dn));
      const unsigned two = pk(o, dpp<QX1>(o));
      if (lane < 32 && !(lane & 1)) *(GAS unsigned*)(p.out + rb_ * DM + rc0 + lane) = two;
      if (wg == 0 && lane == 0) {
        const u64 E = ((u64)ldsw(L, C_E1) << 32) | ldsw(L, C_E0);
        __hip_atomic_store((GAS u64*)(p.ws + WS_EPOCH), E + 1, __ATOMIC_RELAXED, __HIP_MEMORY_SCOPE_AGENT);
      }
      STAMP(21);
    }
  }
}

}

extern "C" void kernel_launch(void* const* d_in, const int* in_sizes, int n_in, void* d_out, int out_size, void* d_ws, size_t ws_size, hipStream_t stream) {
  ak::Params p;
  p.x = (const ak::u16*)d_in[0]; p.K = (const ak::u16*)d_in[1]; p.V = (const ak::u16*)d_in[2];
  p.wq = (const ak::u16*)d_in[3]; p.wo = (const ak::u16*)d_in[4];
  p.wg = (const ak::u16*)d_in[6]; p.wu = (const ak::u16*)d_in[7]; p.wd = (const ak::u16*)d_in[8];
  p.c = (const ak::u16*)d_in[9]; p.wm = (const ak::u16*)d_in[10];
  p.out = (ak::u16*)d_out; p.ws = (unsigned char*)d_ws;
  (void)hipFuncSetAttribute((const void*)ak::fused_kernel, hipFuncAttributeMaxDynamicSharedMemorySize, ak::L_END);
  ak::fused_kernel<<<ak::NWG, ak::NTHR, ak::L_END, stream>>>(p);
}
```

```cpp
#include <hip/hip_runtime.h>
#include <stdint.h>

namespace ak {

typedef unsigned short u16;
typedef unsigned long long u64;
typedef unsigned v4u __attribute__((ext_vector_type(4)));
typedef unsigned v2u __attribute__((ext_vector_type(2)));
typedef float f4 __attribute__((ext_vector_type(4)));
typedef float f2 __attribute__((ext_vector_type(2)));
typedef __bf16 bf8 __attribute__((ext_vector_type(8)));
typedef __bf16 bf2 __attribute__((ext_vector_type(2)));
typedef short s4v __attribute__((ext_vector_type(4)));
#define LAS __attribute__((address_space(3)))
#define GAS __attribute__((address_space(1)))
typedef __amdgpu_buffer_rsrc_t rsrc_t;

#define K_R 3
#define K_RTAIL 8
#define K_TTAIL 16
#define K_NP 2
#define K_PF 4
#define K_TRIG_WO 8
#define K_TRIG_FFN 33
#define K_NL 2
#define K_FB 0
#define K_FB1 2
#define K_FB2 6
#define K_FBW 1
#define K_FBPER 6
#define K_PRIO 1
#define K_WOPACE 40
#define K_WNT 1
#define K_FFNDELAY 0
#define K_WOEARLY 32
#define K_FFNEARLY 0
#define K_FPACE 60
#define K_PSL 2
#define K_WNEAR 16
#define K_WSL 3
#define K_FFNTRIG 0
#define K_H1NEAR 6
#define K_H2NEAR 12
#define K_X 0
#define K_YSL 3
#define K_WSL2 24
#define K_MHOLD 1
#define K_STAMPS 0

constexpr int NB = 8, DM = 576, DFF = 4608, NMOD = 3456, NQ = 4096;
constexpr int NWG = 256, NTHR = 768;
constexpr int NTILE = 512;
constexpr int NP = K_NP, P = NP * 8;
constexpr float LOG2E = 1.4426950408889634f;
constexpr float SCALE = 0.08838834764831845f;
constexpr unsigned SENT = 0xFFFFFFFFu;
constexpr int AUX_NT = 2, AUX_SC1 = 16;
constexpr unsigned SPIN_CAP = 1u << 15, LDS_CAP = 1u << 18;

constexpr int WS_EPOCH = 0, WS_PRG = 1024, WS_CLR = 4096, WS_CLR2 = 6144;
constexpr int WS_MOD = 8192;
constexpr int WS_QP = 65536;
constexpr int WS_OP = WS_QP + 3 * NB * NQ * 4;
constexpr int WS_ML = WS_OP + NWG * 512;
constexpr int WS_YP = WS_ML + NWG * 16;
constexpr int WS_DP = WS_YP + 2 * NB * DM * 4;
constexpr int WS_END = WS_DP + 72 * 144 * 128;
constexpr size_t WS_STAMP = 64u << 20;
constexpr int NSTAMP = 32;

constexpr int L_W = 0;
constexpr int L_SIMG = 73728;
constexpr int L_HIMG = 24576;
constexpr int HSTR = 400;
constexpr int L_WU = 36864, L_WD = 73728;
constexpr int L_GU = 110592;
constexpr int L_ACT = L_GU + 6144;
constexpr int ASTR = 80;
constexpr int L_RED2 = L_ACT + 640;
constexpr int L_X1S = L_RED2 + 3072;
constexpr int L_H2 = L_X1S + 256;
constexpr int XSTR = 1168;
static_assert(L_H2 + 8 * XSTR <= 131072, "ffn lds");
constexpr int L_MRG = 131072;
constexpr int L_RED = L_MRG + 8448;
constexpr int L_KV = 28672, L_KVM = 83968;
constexpr int L_Q = 160128;
constexpr int L_CTL = L_Q + 512;
constexpr int L_STAMP = L_CTL + 128;
constexpr int L_END = L_STAMP + (K_STAMPS ? 12 * 32 * 8 : 0);
static_assert(L_KV + K_NL * 65536 <= L_Q && L_KVM + 65536 <= L_Q && L_END <= 163840, "lds map");
enum { C_SYNC1 = 0, C_HS = 1, C_TRIG = 2, C_MRG = 3, C_DEAD = 4, C_DMA0 = 5, C_DMA1 = 6, C_HS2 = 7, C_KVL = 10, C_Y = 11, C_E0 = 8, C_E1 = 9 };

__host__ __device__ constexpr bool role_w(int wg) { return wg >= 144 && wg < 192 && (wg & 7) + 8 * (((wg >> 3) - 18) >> 1) < 18; }
#define K_WT_MC 130
#define K_WT_ML 90
#define K_WT_W 220
#define K_WT_F 30
#define K_WT_Q 25
constexpr int FKB = K_FFNEARLY == 1 ? 110 : (K_FFNEARLY == 2 ? 37 : 0);
constexpr int role_kb(int wg) { return wg < 18 ? K_WT_MC + FKB : wg < 54 ? K_WT_ML + FKB : role_w(wg) ? K_WT_W : wg < 144 ? K_WT_F + FKB : wg < 246 ? K_WT_Q : 0; }
struct SplitTab { unsigned v[256]; };
constexpr SplitTab make_split() {
  SplitTab t{};
  int o[256] = {};
  for (int i = 0; i < 256; ++i) o[i] = i;
  for (int i = 1; i < 256; ++i) {
    const int x = o[i]; int j = i - 1;
    while (j >= 0 && role_kb(o[j]) < role_kb(x)) { o[j + 1] = o[j]; --j; }
    o[j + 1] = x;
  }
  for (int h = 0; h < 128; ++h) {
    const int a = o[h], b = o[255 - h];
    const int n0 = 256 - (role_kb(a) - role_kb(b) + 8) / 16;
    t.v[a] = (unsigned)(h | (n0 << 8));
    t.v[b] = (unsigned)(h | 128 | (n0 << 8));
  }
  return t;
}
__device__ constexpr SplitTab SPLIT = make_split();

struct Params {
  const u16 *x, *K, *V, *wq, *wo, *wg, *wu, *wd, *c, *wm;
  u16* out;
  unsigned char* ws;
};

__device__ __forceinline__ float asf(unsigned u) { return __builtin_bit_cast(float, u); }
__device__ __forceinline__ unsigned asu(float f) { return __builtin_bit_cast(unsigned, f); }
__device__ __forceinline__ float bflo(unsigned u) { return asf(u << 16); }
__device__ __forceinline__ float bfhi(unsigned u) { return asf(u & 0xffff0000u); }
__device__ __forceinline__ unsigned pk(float a, float b) {
  f2 v = {a, b};
  return __builtin_bit_cast(unsigned, __builtin_convertvector(v, bf2));
}
__device__ __forceinline__ float rb(float a) { return bflo(pk(a, a)); }
__device__ __forceinline__ unsigned san(unsigned u) { return u < 0xFFFFFFFEu ? u : 0xFFFFFFFEu; }
__device__ __forceinline__ v4u san4(v4u v) { return v4u{san(v.x), san(v.y), san(v.z), san(v.w)}; }
__device__ __forceinline__ unsigned umax(unsigned a, unsigned b) { return a > b ? a : b; }
__device__ __forceinline__ unsigned max4(v4u v) { return umax(umax(v.x, v.y), umax(v.z, v.w)); }
template <int C> __device__ __forceinline__ float dpp(float x) {
  return asf((unsigned)__builtin_amdgcn_mov_dpp((int)asu(x), C, 0xf, 0xf, true));
}
template <int C> __device__ __forceinline__ unsigned dppu(unsigned x) { return (unsigned)__builtin_amdgcn_mov_dpp((int)x, C, 0xf, 0xf, true); }
constexpr int ROR1 = 0x121, ROR2 = 0x122, ROR4 = 0x124, ROR8 = 0x128, QX1 = 0xB1, QX2 = 0x4E;
__device__ __forceinline__ float x32max(float x) { auto t = __builtin_amdgcn_permlane32_swap(asu(x), asu(x), false, false); return fmaxf(asf(t[0]), asf(t[1])); }
__device__ __forceinline__ float x32sum(float x) { auto t = __builtin_amdgcn_permlane32_swap(asu(x), asu(x), false, false); return asf(t[0]) + asf(t[1]); }
__device__ __forceinline__ float x16sum(float x) { auto t = __builtin_amdgcn_permlane16_swap(asu(x), asu(x), false, false); return asf(t[0]) + asf(t[1]); }
__device__ __forceinline__ float row16sum(float x) { x += dpp<ROR1>(x); x += dpp<ROR2>(x); x += dpp<ROR4>(x); x += dpp<ROR8>(x); return x; }
__device__ __forceinline__ f4 mfma(v4u a, v4u b, f4 c) {
  return __builtin_amdgcn_mfma_f32_16x16x32_bf16(__builtin_bit_cast(bf8, a), __builtin_bit_cast(bf8, b), c, 0, 0, 0);
}
__device__ __forceinline__ rsrc_t mkr(const void* p, unsigned bytes) { return __builtin_amdgcn_make_buffer_rsrc((void*)p, 0, bytes, 0x00020000); }
__device__ __forceinline__ v4u ldsc1(rsrc_t r, int off) { return __builtin_amdgcn_raw_buffer_load_b128(r, off, 0, AUX_SC1); }
__device__ __forceinline__ v2u ldsc1_2(rsrc_t r, int off) { return __builtin_amdgcn_raw_buffer_load_b64(r, off, 0, AUX_SC1); }
__device__ __forceinline__ void stsc1(v4u v, rsrc_t r, int off) { __builtin_amdgcn_raw_buffer_store_b128(v, r, off, 0, AUX_SC1); }
__device__ __forceinline__ void stsc1_2(v2u v, rsrc_t r, int off) { __builtin_amdgcn_raw_buffer_store_b64(v, r, off, 0, AUX_SC1); }
#define MEMBAR() asm volatile("" ::: "memory")
#define VMWAIT(n) asm volatile("s_waitcnt vmcnt(" #n ")" ::: "memory")
#define LGWAIT() asm volatile("s_waitcnt lgkmcnt(0)" ::: "memory")
#define RAWBAR() asm volatile("s_waitcnt lgkmcnt(0)\n\ts_barrier" ::: "memory")
template <bool NT = true> __device__ __forceinline__ void dma16(rsrc_t r, int voff, unsigned soff, unsigned ldsa) {
  unsigned keep;
  if (NT) asm volatile("s_mov_b32 %0, m0\n\ts_mov_b32 m0, %1\n\ts_nop 0\n\tbuffer_load_dwordx4 %2, %3, %4 offen nt lds\n\ts_mov_b32 m0, %0" : "=&s"(keep) : "s"(ldsa), "v"(voff), "s"(r), "s"(soff) : "memory");
  else asm volatile("s_mov_b32 %0, m0\n\ts_mov_b32 m0, %1\n\ts_nop 0\n\tbuffer_load_dwordx4 %2, %3, %4 offen lds\n\ts_mov_b32 m0, %0" : "=&s"(keep) : "s"(ldsa), "v"(voff), "s"(r), "s"(soff) : "memory");
}
__device__ __forceinline__ v4u ld_hidden(rsrc_t r, int voff) {
  v4u v;
  asm volatile("buffer_load_dwordx4 %0, %1, %2, 0 offen" : "=&v"(v) : "v"(voff), "s"(r) : "memory");
  return v;
}
__device__ __forceinline__ v4u lds128(LAS unsigned char* L, int off) { return *(LAS v4u*)(L + off); }
__device__ __forceinline__ v4u trfrag(LAS unsigned char* L, int a0, int d) {
  s4v lo = __builtin_amdgcn_ds_read_tr16_b64_v4i16((LAS s4v*)(L + a0));
  s4v hi = __builtin_amdgcn_ds_read_tr16_b64_v4i16((LAS s4v*)(L + a0 + d));
  v2u l = __builtin_bit_cast(v2u, lo), h = __builtin_bit_cast(v2u, hi);
  return v4u{l.x, l.y, h.x, h.y};
}
__device__ __forceinline__ unsigned ldsw(LAS unsigned char* L, int w) { return *(volatile LAS unsigned*)(L + L_CTL + 4 * w); }
__device__ __forceinline__ void stw(LAS unsigned char* L, int w, unsigned v) { *(volatile LAS unsigned*)(L + L_CTL + 4 * w) = v; }
__device__ __forceinline__ unsigned lds_add(LAS unsigned char* L, int w) {
  unsigned o = 0;
  if ((threadIdx.x & 63) == 0) o = __hip_atomic_fetch_add((LAS unsigned*)(L + L_CTL + 4 * w), 1u, __ATOMIC_RELAXED, __HIP_MEMORY_SCOPE_WORKGROUP);
  return __builtin_amdgcn_readfirstlane(o);
}
template <int SLP = 1> __device__ __forceinline__ void lds_wait(LAS unsigned char* L, int w, unsigned n) {
  for (unsigned spins = 0; __builtin_amdgcn_readfirstlane(ldsw(L, w)) < n; ++spins) {
    if (__builtin_amdgcn_readfirstlane(ldsw(L, C_DEAD))) break;
    if (spins > LDS_CAP) { stw(L, C_DEAD, 1u); break; }
    __builtin_amdgcn_s_sleep(SLP);
  }
  MEMBAR();
}
__device__ __forceinline__ bool give_up(unsigned& spins, LAS unsigned char* L) {
  if (++spins > SPIN_CAP) { stw(L, C_DEAD, 1u); return true; }
  return (spins & 15u) == 0u && __builtin_amdgcn_readfirstlane(ldsw(L, C_DEAD)) != 0u;
}
__device__ __forceinline__ u64 mix64(u64 z) {
  z = (z ^ (z >> 30)) * 0xBF58476D1CE4E5B9ull;
  z = (z ^ (z >> 27)) * 0x94D049BB133111EBull;
  return z ^ (z >> 31);
}
#define STAMP(k) do {} while (0)
__device__ __forceinline__ void throttle(unsigned r) {
  asm volatile("s_cmp_gt_u32 %0, 6\n\ts_cbranch_scc1 9f\n\ts_waitcnt vmcnt(5)\n\t"
               "s_cmp_gt_u32 %0, 5\n\ts_cbranch_scc1 9f\n\ts_waitcnt vmcnt(4)\n\t"
               "s_cmp_gt_u32 %0, 4\n\ts_cbranch_scc1 9f\n\ts_waitcnt vmcnt(3)\n\t"
               "s_cmp_gt_u32 %0, 3\n\ts_cbranch_scc1 9f\n\ts_waitcnt vmcnt(2)\n\t"
               "s_cmp_gt_u32 %0, 2\n\ts_cbranch_scc1 9f\n\ts_waitcnt vmcnt(1)\n9:" ::"s"(r) : "scc", "memory");
}

__device__ __forceinline__ float sigm(float z) { return __builtin_amdgcn_rcpf(1.f + __builtin_amdgcn_exp2f(-LOG2E * z)); }
__device__ __forceinline__ float silu_acc(float a) {
  const float hi = 1.44269502162933349609375f, lo = 1.92596299112661746e-8f;
  const float t0 = -a * hi;
  const float r = __builtin_fmaf(-a, lo, __builtin_fmaf(-a, hi, -t0));
  const float e0 = __builtin_amdgcn_exp2f(fminf(t0, 126.f));
  const float d = 1.f + __builtin_fmaf(e0, r * 0.693147180559945f, e0);
  const float rc = __builtin_amdgcn_rcpf(d), q = a * rc;
  return __builtin_fmaf(__builtin_fmaf(-q, d, a), rc, q);
}
__device__ __forceinline__ float gelu_t(float x) { const float z = 0.7978845608028654f * (x + 0.044715f * x * x * x); return x * sigm(2.f * z); }

struct Soft { float m, l; f4 acc; };
struct Lane { v4u qa0, qa1; unsigned sel[2][4]; int idx0; };

__device__ __forceinline__ float quadsum(f4 d) {
  float t = dpp<0x00>(d.x);
  t += dpp<0x55>(d.y); t += dpp<0xAA>(d.z); t += dpp<0xFF>(d.w);
  return t;
}
__device__ __forceinline__ void softmax_tile(f4 D0, f4 D1, Soft& s, const Lane& c, unsigned& val0, unsigned& val1) {
  const unsigned r1 = pk(quadsum(D0), quadsum(D1));
  const unsigned r2 = pk(bflo(r1) * SCALE, bfhi(r1) * SCALE);
  const float s0 = bflo(r2), s1 = bfhi(r2);
  float mx = fmaxf(s0, s1);
  mx = fmaxf(mx, dpp<ROR4>(mx)); mx = fmaxf(mx, dpp<ROR8>(mx)); mx = x32max(mx);
  const float mn = fmaxf(s.m, mx);
  const float al = __builtin_amdgcn_exp2f((s.m - mn) * LOG2E);
  const float p0 = __builtin_amdgcn_exp2f((s0 - mn) * LOG2E), p1 = __builtin_amdgcn_exp2f((s1 - mn) * LOG2E);
  s.m = mn; s.l = s.l * al + (p0 + p1);
  auto t = __builtin_amdgcn_permlane16_swap(asu(al), asu(al), false, false);
  const float al0 = asf(t[0]), al1 = asf(t[1]);
  s.acc.x *= al0; s.acc.y *= al0; s.acc.z *= al1; s.acc.w *= al1;
  const unsigned pp = pk(p0, p1);
  val0 = (unsigned)__builtin_amdgcn_ds_bpermute(c.idx0, (int)pp);
  val1 = (unsigned)__builtin_amdgcn_ds_bpermute(c.idx0 + 128, (int)pp);
}
template <int U> __device__ __forceinline__ v4u pfrag(unsigned val0, unsigned val1, const Lane& c) {
  const unsigned v = (U & 1) ? val1 : val0;
  constexpr int y = U >> 1;
  return v4u{__builtin_amdgcn_perm(v, v, c.sel[y][0]), __builtin_amdgcn_perm(v, v, c.sel[y][1]), __builtin_amdgcn_perm(v, v, c.sel[y][2]), __builtin_amdgcn_perm(v, v, c.sel[y][3])};
}

__global__ void __launch_bounds__(NTHR, 3) fused_kernel(Params p) {
  extern __shared__ __attribute__((aligned(16))) unsigned char smem[];
  LAS unsigned char* L = (LAS unsigned char*)smem;
  const int tid = threadIdx.x, lane = tid & 63, wave = __builtin_amdgcn_readfirstlane(tid >> 6);
  const int wg = blockIdx.x;
  const unsigned ldsb = (unsigned)(size_t)L;
  STAMP(0);
  if (tid < 32) *(LAS unsigned*)(L + L_CTL + 4 * tid) = 0u;
  RAWBAR();
  const rsrc_t rws = mkr(p.ws, WS_END);
  const int xl = wg & 7, xi = wg >> 3;
  const bool isM = wg < 54, isMc = wg < 18, isQ = wg >= 54 && wg < 246;
  const bool isF = wg < 144;
  const int f = xl * 18 + xi;
  const int wpair = xl + 8 * ((xi - 18) >> 1);
  const bool isW = wg >= 144 && wg < 192 && wpair < 18;
  const int wsig = wpair / 9, wng = 2 * (wpair % 9) + ((xi - 18) & 1);
  const unsigned sp = SPLIT.v[wg];
  const int ahead = sp & 127, asl = (sp >> 7) & 1, an0 = sp >> 8;
  const int ab = ahead >> 4, akv = ahead & 15, aslot = ahead * 2 + asl;
  const int tbeg = asl ? an0 : 0, tend = asl ? NTILE : an0;
  const int qi = wg - 54, qcg = qi / 3, qkq = qi % 3;
  const int g = lane >> 4, q4 = (lane & 15) >> 2, p4 = lane & 3;

  if (wave < 8) {
    v2u E7 = {0u, 0u};
    if (wave == 7) {
      asm volatile("buffer_load_dwordx2 %0, %1, %2, 0 offen sc1" : "=&v"(E7) : "v"(WS_EPOCH), "s"(rws) : "memory");
      const v4u ss = {SENT, SENT, SENT, SENT};
      if (isM) stsc1(ss, rws, WS_MOD + ((lane >> 3) * NMOD + wg * 64) * 2 + (lane & 7) * 16);
      if (isQ) {
#pragma unroll
        for (int j = 0; j < 2; ++j) stsc1(ss, rws, WS_QP + (((qkq * 8 + 4 * j + (lane >> 4)) * NQ) + qcg * 64) * 4 + (lane & 15) * 16);
      }
      if (lane <= 32) stsc1(ss, rws, lane < 32 ? WS_OP + aslot * 512 + lane * 16 : WS_ML + aslot * 16);
      if (isW) stsc1(ss, rws, WS_YP + ((wsig * 8 + (lane >> 3)) * DM + wng * 32) * 4 + (lane & 7) * 16);
    }
    v4u cvm = {0u, 0u, 0u, 0u};
    if (isMc) cvm = ld_hidden(mkr(p.c, NB * DM * 2), tid * 16);
    if (isMc || isQ) {
      const int stride = isM ? NMOD * 2 : NQ * 2, n = isM ? 9 : 3;
      const rsrc_t rw = isM ? mkr(p.wm, DM * NMOD * 2) : mkr(p.wq, DM * NQ * 2);
      const int cb = isM ? wg * 128 : qcg * 128, r0 = isM ? 0 : qkq * 192;
      const int c16 = lane & 7;
#pragma unroll 1
      for (int j = 0; j < n; ++j) {
        const int i = wave * n + j;
        const int fz = ((lane >> 4) & 1) | ((i & 1) << 1);
        dma16(rw, (lane >> 3) * stride + ((((c16 >> 1) ^ fz) << 1) | (c16 & 1)) * 16 + cb, (r0 + i * 8) * stride, ldsb + L_W + i * 1024);
      }
    }
    auto say_cleared = [&]() {
      if (wave == 7) {
        const unsigned e0 = __builtin_amdgcn_readfirstlane(E7.x), e1 = __builtin_amdgcn_readfirstlane(E7.y);
        if (lane == 0) { __hip_atomic_store((GAS u64*)(p.ws + WS_CLR) + wg, mix64((((u64)e1 << 32) | e0) + 1), __ATOMIC_RELAXED, __HIP_MEMORY_SCOPE_AGENT); stw(L, C_E0, e0); stw(L, C_E1, e1); }
        LGWAIT();
        STAMP(22);
      }
    };
    if (isMc) {
      asm volatile("s_waitcnt vmcnt(9)" : "+v"(cvm), "+v"(E7)::"memory");
      __builtin_amdgcn_sched_barrier(0);
      say_cleared();
      v4u o;
#pragma unroll
      for (int e = 0; e < 4; ++e) o[e] = pk(silu_acc(bflo(cvm[e])), silu_acc(bfhi(cvm[e])));
      *(LAS v4u*)(L + L_SIMG + (tid / 72) * XSTR + (tid % 72) * 16) = o;
      asm volatile("s_waitcnt vmcnt(0) lgkmcnt(0)" ::: "memory");
      lds_add(L, C_DMA0);
    }
    const unsigned char* kb = (const unsigned char*)p.K + ((size_t)ahead << 21);
    const unsigned char* vb = (const unsigned char*)p.V + ((size_t)ahead << 21);
    const int koff = ((lane >> 2) & 3) * 256 + (g * 4 + (lane & 3)) * 16, voff = lane * 16;
    const int tw = tbeg + wave;
#define KVT(tb, n) (tw + 8 * ((tb) + ((n) >> 3)))
#define KVLOAD(tb, n) (((n) & 4) ? __builtin_amdgcn_raw_buffer_load_b128(mkr(vb, KVT(tb, n) < tend ? (1u << 21) : 0u), voff, KVT(tb, n) * 4096 + ((n) & 3) * 1024, AUX_NT) \
                                 : __builtin_amdgcn_raw_buffer_load_b128(mkr(kb, KVT(tb, n) < tend ? (1u << 21) : 0u), koff, KVT(tb, n) * 4096 + ((n) & 3) * 1024, AUX_NT))
#define SB() __builtin_amdgcn_sched_barrier(0)
    v4u ring[8], pre[P];
#pragma unroll
    for (int n = 0; n < 8 + P; ++n) {
      if (n == K_PF && !isMc) { asm volatile("s_waitcnt vmcnt(%1)" : "+v"(E7) : "n"(K_PF) : "memory"); __builtin_amdgcn_sched_barrier(0); say_cleared(); lds_add(L, C_DMA0); }
      if (n >= K_PF) throttle(K_PF);
      if (n < 8) ring[n] = KVLOAD(0, n); else pre[n - 8] = KVLOAD(0, n);
      SB();
    }
    const int nl = isMc ? (K_NL < 1 ? K_NL : 1) : K_NL;
    const int kvl = (isMc ? L_KVM : L_KV) + wave * (nl * 8192);
    {
      const rsrc_t rk = mkr(kb, 1 << 21), rv = mkr(vb, 1 << 21);
#pragma unroll 1
      for (int t = 0; t < nl; ++t) {
#pragma unroll
        for (int n = 0; n < 8; ++n) {
          throttle(K_PF);
          dma16((n & 4) ? rv : rk, (n & 4) ? voff : koff, (tw + 8 * (NP + 1 + t)) * 4096 + (n & 3) * 1024, ldsb + kvl + t * 8192 + n * 1024);
        }
      }
    }
    const int tn = NP + 1 + nl;
    Lane c;
    {
      const int i = lane & 15, beta = i >> 2, pos = i & 1;
#pragma unroll
      for (int y = 0; y < 2; ++y)
#pragma unroll
        for (int w = 0; w < 4; ++w) {
          const unsigned b0 = 2 * y, b1 = 2 * y + 1;
          c.sel[y][w] = beta != w ? 0x0c0c0c0cu : (pos ? ((b1 << 24) | (b0 << 16) | 0x0c0cu) : (0x0c0c0000u | (b1 << 8) | b0));
        }
      c.idx0 = (16 * ((i >> 1) & 1) + 4 * g) * 4;
    }
    if (K_PRIO && wave >= 4) __builtin_amdgcn_s_setprio(1);
    STAMP(1);
    RAWBAR();
    STAMP(2);
    {
      const int i = lane & 15;
      const v4u qf = lds128(L, L_Q + ((i >> 2) & 1) * 256 + (4 * g + (i & 3)) * 16);
      const v4u z = {0u, 0u, 0u, 0u};
      c.qa0 = (i >> 3) ? z : qf;
      c.qa1 = (i >> 3) ? qf : z;
    }
    Soft st;
    st.m = -INFINITY; st.l = 0.f; st.acc = f4{0.f, 0.f, 0.f, 0.f};
    const f4 zero = {0.f, 0.f, 0.f, 0.f};
    unsigned rr = K_R;
    {
      f4 D0 = mfma(c.qa0, ring[0], zero); SB(); ring[0] = KVLOAD(tn, 0); SB();
      D0 = mfma(c.qa1, ring[1], D0);      SB(); ring[1] = KVLOAD(tn, 1); SB();
      f4 D1 = mfma(c.qa0, ring[2], zero); SB(); ring[2] = KVLOAD(tn, 2); SB();
      D1 = mfma(c.qa1, ring[3], D1);      SB(); throttle(rr); ring[3] = KVLOAD(tn, 3); SB();
      unsigned v0, v1;
      softmax_tile(D0, D1, st, c, v0, v1);
      st.acc = mfma(pfrag<0>(v0, v1, c), ring[4], st.acc);
      st.acc = mfma(pfrag<1>(v0, v1, c), ring[5], st.acc);
      st.acc = mfma(pfrag<2>(v0, v1, c), ring[6], st.acc);
      st.acc = mfma(pfrag<3>(v0, v1, c), ring[7], st.acc);
      SB();
#pragma unroll
      for (int t = 0; t < NP; ++t) {
        D0 = mfma(c.qa0, pre[8 * t + 0], zero); D0 = mfma(c.qa1, pre[8 * t + 1], D0);
        D1 = mfma(c.qa0, pre[8 * t + 2], zero); D1 = mfma(c.qa1, pre[8 * t + 3], D1);
        softmax_tile(D0, D1, st, c, v0, v1);
        st.acc = mfma(pfrag<0>(v0, v1, c), pre[8 * t + 4], st.acc);
        st.acc = mfma(pfrag<1>(v0, v1, c), pre[8 * t + 5], st.acc);
        st.acc = mfma(pfrag<2>(v0, v1, c), pre[8 * t + 6], st.acc);
        st.acc = mfma(pfrag<3>(v0, v1, c), pre[8 * t + 7], st.acc);
        SB();
      }
      VMWAIT(4);
#pragma unroll 1
      for (int t = 0; t < nl; ++t) {
        const int a = kvl + t * 8192 + lane * 16;
        D0 = mfma(c.qa0, lds128(L, a), zero); D0 = mfma(c.qa1, lds128(L, a + 1024), D0);
        D1 = mfma(c.qa0, lds128(L, a + 2048), zero); D1 = mfma(c.qa1, lds128(L, a + 3072), D1);
        softmax_tile(D0, D1, st, c, v0, v1);
        st.acc = mfma(pfrag<0>(v0, v1, c), lds128(L, a + 4096), st.acc);
        st.acc = mfma(pfrag<1>(v0, v1, c), lds128(L, a + 5120), st.acc);
        st.acc = mfma(pfrag<2>(v0, v1, c), lds128(L, a + 6144), st.acc);
        st.acc = mfma(pfrag<3>(v0, v1, c), lds128(L, a + 7168), st.acc);
      }
      LGWAIT();
      lds_add(L, C_KVL);
      throttle(rr); ring[4] = KVLOAD(tn, 4); SB();
      throttle(rr); ring[5] = KVLOAD(tn, 5); SB();
      throttle(rr); ring[6] = KVLOAD(tn, 6); SB();
      throttle(rr); ring[7] = KVLOAD(tn, 7); SB();
    }
#pragma unroll 1
    for (int t0 = tn; tw + 8 * t0 < tend; ++t0) {
      if (t0 == K_TRIG_WO && lane == 0 && wave == 0) stw(L, C_TRIG, 1u);
      if (tw + 8 * t0 + K_TTAIL >= tend) rr = K_RTAIL;
      f4 D0 = mfma(c.qa0, ring[0], zero); SB(); throttle(rr); ring[0] = KVLOAD(t0 + 1, 0); SB();
      D0 = mfma(c.qa1, ring[1], D0);      SB(); throttle(rr); ring[1] = KVLOAD(t0 + 1, 1); SB();
      f4 D1 = mfma(c.qa0, ring[2], zero); SB(); throttle(rr); ring[2] = KVLOAD(t0 + 1, 2); SB();
      D1 = mfma(c.qa1, ring[3], D1);      SB(); throttle(rr); ring[3] = KVLOAD(t0 + 1, 3); SB();
      unsigned v0, v1;
      softmax_tile(D0, D1, st, c, v0, v1);
      st.acc = mfma(pfrag<0>(v0, v1, c), ring[4], st.acc); SB(); throttle(rr); ring[4] = KVLOAD(t0 + 1, 4); SB();
      st.acc = mfma(pfrag<1>(v0, v1, c), ring[5], st.acc); SB(); throttle(rr); ring[5] = KVLOAD(t0 + 1, 5); SB();
      st.acc = mfma(pfrag<2>(v0, v1, c), ring[6], st.acc); SB(); throttle(rr); ring[6] = KVLOAD(t0 + 1, 6); SB();
      st.acc = mfma(pfrag<3>(v0, v1, c), ring[7], st.acc); SB(); throttle(rr); ring[7] = KVLOAD(t0 + 1, 7); SB();
    }
#undef KVLOAD
    if (lds_add(L, 18) == 7u && lane == 0) stw(L, C_TRIG, 2u);
    STAMP(3);
    float l = st.l;
    l += dpp<ROR4>(l); l += dpp<ROR8>(l); l = x32sum(l);
    LAS float* mg = (LAS float*)(L + L_MRG) + wave * 264;
    const int cc = lane & 15;
    *(LAS f2*)(mg + 8 + 8 * cc + 2 * g) = f2{st.acc.x, st.acc.y};
    *(LAS f2*)(mg + 8 + 128 + 8 * cc + 2 * g) = f2{st.acc.z, st.acc.w};
    if (lane == 0 || lane == 16) *(LAS f2*)(mg + 2 * g) = f2{st.m, l};
    LGWAIT();
    if (lds_add(L, C_MRG) == 7u) {
      MEMBAR();
      const int h = lane >> 5, d0 = 4 * (lane & 31);
      LAS float* m0 = (LAS float*)(L + L_MRG);
      f2 ml[8]; f4 a[8];
#pragma unroll
      for (int w = 0; w < 8; ++w) { ml[w] = *(LAS f2*)(m0 + w * 264 + 2 * h); a[w] = *(LAS f4*)(m0 + w * 264 + 8 + 128 * h + d0); }
      float M = ml[0].x;
#pragma unroll
      for (int w = 1; w < 8; ++w) M = fmaxf(M, ml[w].x);
      float Ls = 0.f; f4 O = {0.f, 0.f, 0.f, 0.f};
#pragma unroll
      for (int w = 0; w < 8; ++w) { const float e = __builtin_amdgcn_exp2f((ml[w].x - M) * LOG2E); Ls += e * ml[w].y; O += e * a[w]; }
      const float inv = __builtin_amdgcn_rcpf(Ls);
      const unsigned o0 = pk(O.x * inv, O.y * inv), o1 = pk(O.z * inv, O.w * inv);
      const unsigned n0 = dppu<QX1>(o0), n1 = dppu<QX1>(o1);
      if (!(lane & 1)) stsc1(san4(v4u{o0, o1, n0, n1}), rws, WS_OP + aslot * 512 + (h * 128 + d0) * 2);
      const unsigned M1 = __builtin_amdgcn_readlane(asu(M), 32), L1 = __builtin_amdgcn_readlane(asu(Ls), 32);
      if (lane == 0) stsc1(san4(v4u{asu(M), asu(Ls), M1, L1}), rws, WS_ML + aslot * 16);
      STAMP(4);
    }
  } else {
    const int sw = wave - 8, stid = tid - 512;
    const u64 Es = __hip_atomic_load((GAS u64*)(p.ws + WS_EPOCH), __ATOMIC_RELAXED, __HIP_MEMORY_SCOPE_AGENT);
    const int fz = (q4 >> 1) | ((g & 1) << 1);
    auto mod_mfma = [&]() {
      STAMP(5);
      f4 D[3] = {{0.f, 0.f, 0.f, 0.f}, {0.f, 0.f, 0.f, 0.f}, {0.f, 0.f, 0.f, 0.f}};
#pragma unroll
      for (int ks = 0; ks < 18; ++ks) {
        const v4u wf = trfrag(L, L_W + (32 * ks + 8 * g + q4) * 128 + (sw ^ fz) * 32 + p4 * 8, 4 * 128);
        const v4u sf = lds128(L, L_SIMG + (lane & 7) * XSTR + (32 * ks + 8 * g) * 2);
        D[ks % 3] = mfma(wf, sf, D[ks % 3]);
      }
      const f4 Ds = (D[0] + D[1]) + D[2];
      if ((lane & 15) < 8) stsc1_2(v2u{san(pk(Ds.x, Ds.y)), san(pk(Ds.z, Ds.w))}, rws, WS_MOD + ((lane & 7) * NMOD + wg * 64 + sw * 16 + 4 * g) * 2);
      STAMP(6);
    };
    const u64 tagv = mix64((((u64)__builtin_amdgcn_readfirstlane((unsigned)(Es >> 32)) << 32) | (unsigned)__builtin_amdgcn_readfirstlane((unsigned)Es)) + 1);
    const unsigned tg0 = (unsigned)tagv, tg1 = (unsigned)(tagv >> 32);
    if (isMc) {
      if (sw == 0) {
        const v4u cv = __builtin_amdgcn_raw_buffer_load_b128(mkr(p.c, NB * DM * 2), tid * 16, 0, 0);
        v4u o;
#pragma unroll
        for (int e = 0; e < 4; ++e) o[e] = pk(silu_acc(bflo(cv[e])), silu_acc(bfhi(cv[e])));
        *(LAS v4u*)(L + L_SIMG + (tid / 72) * XSTR + (tid % 72) * 16) = o;
        LGWAIT();
        lds_add(L, C_SYNC1);
      }
      STAMP(23);
      lds_wait(L, C_SYNC1, 1); lds_wait(L, C_DMA0, 8);
      mod_mfma();
    } else if (isQ) {
      const int qb = 2 * sw + (lane >> 5), qj = lane & 31;
      const bool qact = qj < 24;
      const rsrc_t rx = mkr(p.x, NB * DM * 2);
      v4u xv[3];
#pragma unroll
      for (int t = 0; t < 3; ++t) xv[t] = __builtin_amdgcn_raw_buffer_load_b128(rx, qact ? (qb * DM + 192 * t + 8 * qj) * 2 : 0x7ffffff0, 0, 0);
      float xf[3][8];
      float sum = 0.f;
#pragma unroll
      for (int t = 0; t < 3; ++t)
#pragma unroll
        for (int e = 0; e < 4; ++e) { xf[t][2 * e] = bflo(xv[t][e]); xf[t][2 * e + 1] = bfhi(xv[t][e]); sum += xf[t][2 * e] + xf[t][2 * e + 1]; }
      sum = x16sum(row16sum(sum));
      const float mean = sum * (1.f / DM);
      float vs = 0.f;
#pragma unroll
      for (int t = 0; t < 3; ++t)
#pragma unroll
        for (int e = 0; e < 8; ++e) { const float d = xf[t][e] - mean; vs += qact ? d * d : 0.f; }
      vs = x16sum(row16sum(vs));
      const float mb = rb(mean), den = rb(__builtin_sqrtf(rb(rb(vs * (1.f / DM)) + 1e-5f)));
      float ln[8];
#pragma unroll
      for (int e = 0; e < 8; ++e) { const float xe = qkq == 0 ? xf[0][e] : (qkq == 1 ? xf[1][e] : xf[2][e]); ln[e] = rb(rb(xe - mb) / den); }
      lds_wait(L, C_DMA0, 8);
      v4u wf[6];
#pragma unroll
      for (int ks = 0; ks < 6; ++ks) wf[ks] = trfrag(L, L_W + (32 * ks + 8 * g + q4) * 128 + (sw ^ fz) * 32 + p4 * 8, 4 * 128);
      STAMP(29);
      if (!(K_X & 16)) {
      v4u sa, ca;
      const int mo = qact ? WS_MOD + (qb * NMOD + 192 * qkq + 8 * qj) * 2 : 0x7ffffff0;
      if (sw == 0) {
        const int pw = (lane < 3 ? 0 : 6) + 3 * qkq + lane;
        const int fo = lane < 6 ? WS_CLR + pw * 8 : 0x7ffffff0, io = lane < 6 ? WS_MOD + (7 * NMOD + pw * 64 + 62) * 2 : 0x7ffffff0;
        bool cleared = false;
        for (unsigned spins = 0;;) {
          MEMBAR();
          if (!cleared) { const v2u fl = ldsc1_2(rws, fo); cleared = __all(lane >= 6 || (fl.x == tg0 && fl.y == tg1)); }
          else if (__builtin_popcountll(__builtin_amdgcn_ballot_w64(lane < 6 && __builtin_amdgcn_raw_buffer_load_b32(rws, io, 0, AUX_SC1) != SENT)) >= K_H1NEAR) break;
          if (give_up(spins, L)) break;
          __builtin_amdgcn_s_sleep(K_PSL);
        }
        if (lane == 0) stw(L, C_HS, 1u);
      } else lds_wait(L, C_HS, 1);
      for (unsigned spins = 0;;) {
        MEMBAR();
        sa = ldsc1(rws, mo); ca = ldsc1(rws, qact ? mo + DM * 2 : mo);
        if (__all(umax(max4(sa), max4(ca)) != SENT)) break;
        if (give_up(spins, L)) break;
        __builtin_amdgcn_s_sleep(K_PSL);
      }
      STAMP(8);
      v4u h;
#pragma unroll
      for (int e = 0; e < 4; ++e) {
        const unsigned c1 = pk(1.f + bflo(ca[e]), 1.f + bfhi(ca[e]));
        const unsigned m1 = pk(ln[2 * e] * bflo(c1), ln[2 * e + 1] * bfhi(c1));
        h[e] = pk(bflo(m1) + bflo(sa[e]), bfhi(m1) + bfhi(sa[e]));
      }
      if (qact) *(LAS v4u*)(L + L_HIMG + qb * HSTR + qj * 16) = h;
      LGWAIT();
      lds_add(L, C_SYNC1); lds_wait(L, C_SYNC1, 4);
      f4 D0 = {0.f, 0.f, 0.f, 0.f}, D1 = {0.f, 0.f, 0.f, 0.f};
#pragma unroll
      for (int ks = 0; ks < 6; ks += 2) {
        D0 = mfma(wf[ks], lds128(L, L_HIMG + (lane & 7) * HSTR + (32 * ks + 8 * g) * 2), D0);
        D1 = mfma(wf[ks + 1], lds128(L, L_HIMG + (lane & 7) * HSTR + (32 * ks + 32 + 8 * g) * 2), D1);
      }
      if ((lane & 15) < 8) stsc1(san4(__builtin_bit_cast(v4u, D0 + D1)), rws, WS_QP + ((qkq * 8 + (lane & 7)) * NQ + qcg * 64 + sw * 16 + 4 * g) * 4);
      STAMP(9);
      }
    }
    if (sw == 0 && !(K_X & 8)) {
      v4u a0, a1, a2;
      const int qo = WS_QP + (ab * NQ + akv * 256 + 4 * lane) * 4;
      const int fo = lane < 12 ? WS_CLR + (54 + 12 * akv + lane) * 8 : 0x7ffffff0;
      const int io = lane < 12 ? WS_QP + (((lane % 3) * 8 + ab) * NQ + (4 * akv + lane / 3) * 64 + 63) * 4 : 0x7ffffff0;
      bool cleared = false;
      for (unsigned spins = 0;;) {
        MEMBAR();
        if (!cleared) { const v2u fl = ldsc1_2(rws, fo); cleared = __all(lane >= 12 || (fl.x == tg0 && fl.y == tg1)); }
        else if (__builtin_popcountll(__builtin_amdgcn_ballot_w64(lane < 12 && __builtin_amdgcn_raw_buffer_load_b32(rws, io, 0, AUX_SC1) != SENT)) >= K_H2NEAR) break;
        if (give_up(spins, L)) break;
        __builtin_amdgcn_s_sleep(K_PSL);
      }
      for (unsigned spins = 0;;) {
        MEMBAR();
        a0 = ldsc1(rws, qo); a1 = ldsc1(rws, qo + NB * NQ * 4); a2 = ldsc1(rws, qo + 2 * NB * NQ * 4);
        if (__all(umax(umax(max4(a0), max4(a1)), max4(a2)) != SENT)) break;
        if (give_up(spins, L)) break;
        __builtin_amdgcn_s_sleep(K_PSL);
      }
      const f4 s = (__builtin_bit_cast(f4, a0) + __builtin_bit_cast(f4, a1)) + __builtin_bit_cast(f4, a2);
      *(LAS v2u*)(L + L_Q + lane * 8) = v2u{pk(s.x, s.y), pk(s.z, s.w)};
      STAMP(10);
    }
    RAWBAR();
    if (isF && sw == 3) {
      const v4u ss = {SENT, SENT, SENT, SENT};
#pragma unroll 3
      for (int j = 0; j < (K_X & 1 ? 0 : 9); ++j) stsc1(ss, rws, WS_DP + ((j * 8 + (lane >> 3)) * 144 + f) * 128 + (lane & 7) * 16);
      VMWAIT(0);
      if (lane == 0) __hip_atomic_store((GAS u64*)(p.ws + WS_CLR2) + f, tagv, __ATOMIC_RELAXED, __HIP_MEMORY_SCOPE_AGENT);
    }
    if (isM && !isMc) {
      lds_wait(L, C_DMA0, 8); lds_wait(L, C_KVL, 8);
      const rsrc_t rw = mkr(p.wm, DM * NMOD * 2);
      const int c16 = lane & 7;
#pragma unroll 1
      for (int j = 0; j < (K_X & 4 ? 0 : 18); ++j) {
        const int i = sw * 18 + j;
        const int fs = ((lane >> 4) & 1) | ((i & 1) << 1);
        dma16(rw, (lane >> 3) * (NMOD * 2) + ((((c16 >> 1) ^ fs) << 1) | (c16 & 1)) * 16 + wg * 128, i * 8 * (NMOD * 2), ldsb + L_W + i * 1024);
        if (K_WOPACE) __builtin_amdgcn_s_sleep(K_WOPACE);
      }
      const rsrc_t rc = mkr(p.c, NB * DM * 2);
#pragma unroll 1
      for (int j = 0; j < 3; ++j) {
        const int ch = stid + 256 * j;
        const v4u cv = __builtin_amdgcn_raw_buffer_load_b128(rc, ch * 16, 0, 0);
        v4u o;
#pragma unroll
        for (int e = 0; e < 4; ++e) o[e] = pk(silu_acc(bflo(cv[e])), silu_acc(bfhi(cv[e])));
        if (ch < 576) *(LAS v4u*)(L + L_SIMG + (ch / 72) * XSTR + (ch % 72) * 16) = o;
      }
      VMWAIT(0); LGWAIT();
      lds_add(L, C_SYNC1); lds_wait(L, C_SYNC1, 4);
      mod_mfma();
    }
    if ((isF || isW) && sw == 3) {
      for (unsigned spins = 0;;) {
        MEMBAR();
        const v4u a = ldsc1(rws, WS_CLR + lane * 32), b = ldsc1(rws, WS_CLR + lane * 32 + 16);
        bool ok = a.x == tg0 && a.y == tg1 && a.z == tg0 && a.w == tg1 && b.x == tg0 && b.y == tg1 && b.z == tg0 && b.w == tg1;
        if (isF) {
          const v4u c2 = ldsc1(rws, lane < 36 ? WS_CLR2 + lane * 32 : 0x7ffffff0), d2 = ldsc1(rws, lane < 36 ? WS_CLR2 + lane * 32 + 16 : 0x7ffffff0);
          ok = ok && (lane >= 36 || (c2.x == tg0 && c2.y == tg1 && c2.z == tg0 && c2.w == tg1 && d2.x == tg0 && d2.y == tg1 && d2.z == tg0 && d2.w == tg1));
        }
        if (__all(ok)) break;
        if (give_up(spins, L)) break;
        __builtin_amdgcn_s_sleep(127);
      }
      if (lane == 0) stw(L, C_HS2, 1u);
      STAMP(24);
    }
    if (isW || isF) {
      if (isM && !isMc) { lds_add(L, 17); lds_wait(L, 17, 4); }
      lds_wait(L, C_KVL, 8);
      const int sc = ((lane & 3) ^ ((lane >> 4) & 2)) * 16;
      if (isW) {
        const rsrc_t rw = mkr(p.wo, NQ * DM * 2);
        const int vo = (lane >> 2) * (DM * 2) + sc + wng * 64;
        lds_wait<16>(L, C_TRIG, 1);
        STAMP(11);
#pragma unroll 1
        for (int j = 0; j < (K_X & 2 ? 0 : K_WOEARLY); ++j) { const int i = j * 4 + sw; dma16<K_WNT != 0>(rw, vo, (wsig * 2048 + i * 16) * DM * 2, ldsb + L_W + i * 1024); if (K_WOPACE) __builtin_amdgcn_s_sleep(K_WOPACE); }
        lds_wait<4>(L, C_TRIG, 2);
#pragma unroll 1
        for (int j = K_WOEARLY; j < 32; ++j) { const int i = j * 4 + sw; dma16<K_WNT != 0>(rw, vo, (wsig * 2048 + i * 16) * DM * 2, ldsb + L_W + i * 1024); }
      } else {
        const rsrc_t rg = mkr(p.wg, DM * DFF * 2), ru = mkr(p.wu, DM * DFF * 2), rd = mkr(p.wd, DFF * DM * 2);
        const int vo = (lane >> 2) * (DFF * 2) + sc + f * 64;
        lds_wait<4>(L, C_TRIG, K_FFNEARLY ? 1 : 2);
        STAMP(11);
        for (int k = 0; k < K_FFNDELAY; ++k) __builtin_amdgcn_s_sleep(16);
#pragma unroll 1
        for (int j = 0; j < 9; ++j) { const int i = j * 4 + sw; dma16(rd, lane * 16, f * 32 * DM * 2 + i * 1024, ldsb + L_WD + i * 1024); if (K_FFNEARLY) __builtin_amdgcn_s_sleep(K_FPACE); }
        if (K_FFNEARLY == 2) lds_wait<4>(L, C_TRIG, 2);
#pragma unroll 1
        for (int j = 0; j < 9; ++j) {
          const int i = j * 4 + sw;
          dma16<K_WNT != 0>(rg, vo, i * 16 * DFF * 2, ldsb + L_W + i * 1024); if (K_FFNEARLY == 1) __builtin_amdgcn_s_sleep(K_FPACE);
          dma16<K_WNT != 0>(ru, vo, i * 16 * DFF * 2, ldsb + L_WU + i * 1024); if (K_FFNEARLY == 1) __builtin_amdgcn_s_sleep(K_FPACE);
        }
      }
      VMWAIT(0);
      lds_add(L, C_DMA1);
      STAMP(12);
    }
  }

  if (isW) {
    const int cc = lane & 15;
    if (wave < 8) {
      const int pid = (cc & 7) * 32 + (wsig * 8 + wave) * 2 + (cc >> 3);
      lds_wait(L, C_HS2, 1);
      lds_wait(L, C_DMA1, 4);
      v4u wf[2][4][2];
#pragma unroll
      for (int hh = 0; hh < 2; ++hh)
#pragma unroll
        for (int ks = 0; ks < 4; ++ks)
#pragma unroll
          for (int nt = 0; nt < 2; ++nt) wf[hh][ks][nt] = trfrag(L, L_W + (wave * 256 + hh * 128 + 32 * ks + 8 * g + q4) * 64 + (nt ^ (g & 1)) * 32 + p4 * 8, 4 * 64);
      v4u ml, fr[2][4];
      for (unsigned spins = 0;;) {
        MEMBAR();
        ml = ldsc1(rws, WS_ML + pid * 16);
        const int nv = __builtin_popcountll(__builtin_amdgcn_ballot_w64(max4(ml) != SENT));
        if (nv >= 4 * K_WNEAR) break;
        if (give_up(spins, L)) break;
        if (nv < 4 * 12) __builtin_amdgcn_s_sleep(K_WSL2); else __builtin_amdgcn_s_sleep(K_WSL);
      }
      STAMP(13);
      for (unsigned spins = 0;;) {
        MEMBAR();
        ml = ldsc1(rws, WS_ML + pid * 16);
#pragma unroll
        for (int hh = 0; hh < 2; ++hh)
#pragma unroll
          for (int ks = 0; ks < 4; ++ks) fr[hh][ks] = ldsc1(rws, WS_OP + pid * 512 + (hh * 128 + 32 * ks + 8 * g) * 2);
        unsigned mx = max4(ml);
#pragma unroll
        for (int hh = 0; hh < 2; ++hh)
#pragma unroll
          for (int ks = 0; ks < 4; ++ks) mx = umax(mx, max4(fr[hh][ks]));
        if (__all(mx != SENT)) break;
        if (give_up(spins, L)) break;
      }
      float wgt[2];
#pragma unroll
      for (int hh = 0; hh < 2; ++hh) {
        const float M = asf(ml[2 * hh]), Lv = asf(ml[2 * hh + 1]);
        const float M2 = dpp<ROR8>(M), L2 = dpp<ROR8>(Lv), Mx = fmaxf(M, M2);
        const float e1 = __builtin_amdgcn_exp2f((M - Mx) * LOG2E) * Lv, e2 = __builtin_amdgcn_exp2f((M2 - Mx) * LOG2E) * L2;
        wgt[hh] = e1 * __builtin_amdgcn_rcpf(e1 + e2);
      }
      STAMP(14);
#pragma unroll
      for (int nt = 0; nt < 2; ++nt) {
        f4 d0 = {0.f, 0.f, 0.f, 0.f}, d1 = {0.f, 0.f, 0.f, 0.f};
#pragma unroll
        for (int ks = 0; ks < 4; ++ks) { d0 = mfma(wf[0][ks][nt], fr[0][ks], d0); d1 = mfma(wf[1][ks][nt], fr[1][ks], d1); }
        f4 t = wgt[0] * d0 + wgt[1] * d1;
        t.x += dpp<ROR8>(t.x); t.y += dpp<ROR8>(t.y); t.z += dpp<ROR8>(t.z); t.w += dpp<ROR8>(t.w);
        if (cc < 8) *(LAS f4*)(L + L_RED + (((wave * 2 + nt) * 4 + g) * 8 + cc) * 16) = t;
      }
      LGWAIT();
      if (lds_add(L, C_SYNC1 + 16) == 7u) {
        MEMBAR();
        f4 s = {0.f, 0.f, 0.f, 0.f};
#pragma unroll
        for (int w = 0; w < 8; ++w) s += *(LAS f4*)(L + L_RED + (w * 64 + lane) * 16);
        const int nt = lane >> 5, rr = (lane >> 3) & 3, b = lane & 7;
        stsc1(san4(__builtin_bit_cast(v4u, s)), rws, WS_YP + ((wsig * 8 + b) * DM + wng * 32 + nt * 16 + 4 * rr) * 4);
        STAMP(15);
      }
    }
  } else if (isF) {
    const bool isR = f < 72;
    const int rb_ = f / 9, rc0 = (f % 9) * 64;
    unsigned gbw = 0;
    const int gt = wave & 3, kt = wave >> 2;
    v4u wf[6], wd[4];
    if (wave < 8) {
      const int b = wave;
      const bool act = lane < 48;
      const rsrc_t rx = mkr(p.x, NB * DM * 2);
      v2u xv[3], ga[3], sb[3], cb[3];
      lds_wait(L, C_HS2, 1);
#pragma unroll
      for (int t = 0; t < 3; ++t) xv[t] = __builtin_amdgcn_raw_buffer_load_b64(rx, act ? (b * DM + 4 * (lane + 48 * t)) * 2 : 0x7ffffff0, 0, 0);
      for (unsigned spins = 0;;) {
        MEMBAR();
        unsigned mx = 0;
#pragma unroll
        for (int t = 0; t < 3; ++t) {
          const int o = act ? WS_MOD + (b * NMOD + 4 * (lane + 48 * t)) * 2 : 0x7ffffff0;
          ga[t] = ldsc1_2(rws, act ? o + 2 * DM * 2 : o); sb[t] = ldsc1_2(rws, act ? o + 3 * DM * 2 : o); cb[t] = ldsc1_2(rws, act ? o + 4 * DM * 2 : o);
          mx = umax(mx, umax(umax(umax(ga[t].x, ga[t].y), umax(sb[t].x, sb[t].y)), umax(cb[t].x, cb[t].y)));
        }
        if (wave == 0) { gbw = __builtin_amdgcn_raw_buffer_load_b32(rws, isR ? WS_MOD + (rb_ * NMOD + 5 * DM + rc0 + (lane & ~1)) * 2 : 0x7ffffff0, 0, AUX_SC1); mx = umax(mx, gbw); }
        if (__all(mx != SENT)) break;
        if (give_up(spins, L)) break;
      }
      float xf[12], gf[12], cf[12], sf[12];
#pragma unroll
      for (int t = 0; t < 3; ++t)
#pragma unroll
        for (int e = 0; e < 2; ++e) {
          xf[4 * t + 2 * e] = bflo(xv[t][e]); xf[4 * t + 2 * e + 1] = bfhi(xv[t][e]);
          gf[4 * t + 2 * e] = bflo(ga[t][e]); gf[4 * t + 2 * e + 1] = bfhi(ga[t][e]);
          cf[4 * t + 2 * e] = 1.f + bflo(cb[t][e]); cf[4 * t + 2 * e + 1] = 1.f + bfhi(cb[t][e]);
          sf[4 * t + 2 * e] = bflo(sb[t][e]); sf[4 * t + 2 * e + 1] = bfhi(sb[t][e]);
        }
#pragma unroll
      for (int e = 0; e < 12; ++e) asm volatile("" : "+v"(xf[e]), "+v"(gf[e]), "+v"(cf[e]), "+v"(sf[e]));
      lds_wait(L, C_DMA1, 4);
#pragma unroll
      for (int k6 = 0; k6 < 6; ++k6) wf[k6] = trfrag(L, ((gt >> 1) ? L_WU : L_W) + (32 * (kt * 6 + k6) + 8 * g + q4) * 64 + ((gt & 1) ^ (g & 1)) * 32 + p4 * 8, 4 * 64);
#pragma unroll
      for (int j = 0; j < 4; ++j) wd[j] = trfrag(L, L_WD + (8 * g + q4) * (DM * 2) + (wave < 9 ? 4 * wave + j : 0) * 32 + p4 * 8, 4 * DM * 2);
      if (wave == 0) {
        for (unsigned spins = 0;;) {
          MEMBAR();
          const unsigned v = __builtin_amdgcn_raw_buffer_load_b32(rws, lane == 0 ? WS_YP + (((f & 1) * 8 + (f & 7)) * DM + (f % 18) * 32) * 4 : 0x7ffffff0, 0, AUX_SC1);
          if (__builtin_amdgcn_readfirstlane(v) != SENT) break;
          if (give_up(spins, L)) break;
          __builtin_amdgcn_s_sleep(K_YSL);
        }
        if (lane == 0) stw(L, C_Y, 1u);
      } else lds_wait(L, C_Y, 1);
      STAMP(16);
      v4u y0[3], y1[3];
      for (unsigned spins = 0;;) {
        MEMBAR();
        unsigned mx = 0;
#pragma unroll
        for (int t = 0; t < 3; ++t) {
          const int o = act ? WS_YP + (b * DM + 4 * (lane + 48 * t)) * 4 : 0x7ffffff0;
          y0[t] = ldsc1(rws, o); y1[t] = ldsc1(rws, act ? o + NB * DM * 4 : o);
        }
#pragma unroll
        for (int t = 0; t < 3; ++t) mx = umax(mx, umax(max4(y0[t]), max4(y1[t])));
        if (__all(mx != SENT)) break;
        if (give_up(spins, L)) break;
      }
      STAMP(17);
      float x1[12];
#pragma unroll
      for (int e = 0; e < 12; ++e) x1[e] = __builtin_fmaf(gf[e], asf(y0[e >> 2][e & 3]) + asf(y1[e >> 2][e & 3]), xf[e]);
      const float sh = asf(__builtin_amdgcn_readfirstlane(asu(x1[0])));
      float s1 = 0.f, s2 = 0.f;
#pragma unroll
      for (int e = 0; e < 12; ++e) { const float d = x1[e] - sh; s1 += d; s2 = __builtin_fmaf(d, d, s2); }
      s1 = act ? s1 : 0.f; s2 = act ? s2 : 0.f;
      s1 += dpp<ROR1>(s1); s2 += dpp<ROR1>(s2); s1 += dpp<ROR2>(s1); s2 += dpp<ROR2>(s2);
      s1 += dpp<ROR4>(s1); s2 += dpp<ROR4>(s2); s1 += dpp<ROR8>(s1); s2 += dpp<ROR8>(s2);
      s1 = x16sum(s1); s2 = x16sum(s2); s1 = x32sum(s1); s2 = x32sum(s2);
      const float dm = s1 * (1.f / DM), var = fmaxf(__builtin_fmaf(-dm, dm, s2 * (1.f / DM)), 0.f);
      const float rstd = __builtin_amdgcn_rsqf(var + 1e-5f), mo = -(sh + dm) * rstd;
#pragma unroll
      for (int t = 0; t < 3; ++t) {
        float hv[4];
#pragma unroll
        for (int e = 0; e < 4; ++e) hv[e] = __builtin_fmaf(__builtin_fmaf(x1[4 * t + e], rstd, mo), cf[4 * t + e], sf[4 * t + e]);
        if (act) {
          *(LAS v2u*)(L + L_H2 + b * XSTR + 8 * (lane + 48 * t)) = v2u{pk(hv[0], hv[1]), pk(hv[2], hv[3])};
          const int col = 4 * (lane + 48 * t) - rc0;
          if (isR && b == rb_ && col >= 0 && col < 64) *(LAS f4*)(L + L_X1S + col * 4) = f4{x1[4 * t], x1[4 * t + 1], x1[4 * t + 2], x1[4 * t + 3]};
        }
      }
    } else {
      lds_wait(L, C_DMA1, 4);
#pragma unroll
      for (int k6 = 0; k6 < 6; ++k6) wf[k6] = trfrag(L, ((gt >> 1) ? L_WU : L_W) + (32 * (kt * 6 + k6) + 8 * g + q4) * 64 + ((gt & 1) ^ (g & 1)) * 32 + p4 * 8, 4 * 64);
#pragma unroll
      for (int j = 0; j < 4; ++j) wd[j] = trfrag(L, L_WD + (8 * g + q4) * (DM * 2) + (wave < 9 ? 4 * wave + j : 0) * 32 + p4 * 8, 4 * DM * 2);
    }
    RAWBAR();
    STAMP(18);
    {
      f4 D0 = {0.f, 0.f, 0.f, 0.f}, D1 = {0.f, 0.f, 0.f, 0.f};
#pragma unroll
      for (int k6 = 0; k6 < 6; k6 += 2) {
        D0 = mfma(wf[k6], lds128(L, L_H2 + (lane & 7) * XSTR + (32 * (kt * 6 + k6) + 8 * g) * 2), D0);
        D1 = mfma(wf[k6 + 1], lds128(L, L_H2 + (lane & 7) * XSTR + (32 * (kt * 6 + k6 + 1) + 8 * g) * 2), D1);
      }
      if ((lane & 15) < 8) *(LAS f4*)(L + L_GU + (((kt * 4 + gt) * 4 + g) * 8 + (lane & 7)) * 16) = D0 + D1;
    }
    RAWBAR();
    STAMP(7);
    if (tid < 256) {
      const int b = tid & 7, fc = tid >> 3;
      const int o = (((fc >> 4) * 4 + ((fc >> 2) & 3)) * 8 + b) * 16 + (fc & 3) * 4;
      float gs = 0.f, us = 0.f;
#pragma unroll
      for (int k3 = 0; k3 < 3; ++k3) { gs += *(LAS float*)(L + L_GU + k3 * 2048 + o); us += *(LAS float*)(L + L_GU + k3 * 2048 + 1024 + o); }
      const float a = rb(gelu_t(rb(gs))) * rb(us);
      *(LAS u16*)(L + L_ACT + b * ASTR + fc * 2) = (u16)pk(a, a);
    }
    RAWBAR();
    STAMP(31);
    if (wave < 9) {
      const v4u af = lds128(L, L_ACT + (lane & 7) * ASTR + g * 16);
      const int b = lane & 7, hi = (lane >> 3) & 1;
      const f4 z4 = {0.f, 0.f, 0.f, 0.f};
      const f4 T0 = mfma(wd[0], af, z4), T1 = mfma(wd[1], af, z4), T2 = mfma(wd[2], af, z4), T3 = mfma(wd[3], af, z4);
      const f4 Da = hi ? T2 : T0, Db = hi ? T3 : T1;
      stsc1(san4(v4u{pk(Da.x, Da.y), pk(Da.z, Da.w), pk(Db.x, Db.y), pk(Db.z, Db.w)}), rws, WS_DP + ((b * 9 + wave) * 144 + f) * 128 + (hi * 4 + g) * 16);
    }
    STAMP(19);
    if (isR) {
      v4u a, b2;
      const int o = WS_DP + f * 144 * 128 + tid * 16;
      for (unsigned spins = 0;;) {
        MEMBAR();
        a = ldsc1(rws, o); b2 = ldsc1(rws, tid < 384 ? o + 768 * 16 : 0x7ffffff0);
        if (__all(umax(max4(a), max4(b2)) != SENT)) break;
        if (give_up(spins, L)) break;
      }
      STAMP(20);
      float s[8];
#pragma unroll
      for (int e = 0; e < 4; ++e) { s[2 * e] = bflo(a[e]) + bflo(b2[e]); s[2 * e + 1] = bfhi(a[e]) + bfhi(b2[e]); }
#pragma unroll
      for (int e = 0; e < 8; ++e) { float v = s[e]; v += dpp<ROR8>(v); v = x32sum(x16sum(v)); s[e] = v; }
      if (lane < 8) {
        *(LAS f4*)(L + L_RED2 + (wave * 8 + lane) * 32) = f4{s[0], s[1], s[2], s[3]};
        *(LAS f4*)(L + L_RED2 + (wave * 8 + lane) * 32 + 16) = f4{s[4], s[5], s[6], s[7]};
      }
      RAWBAR();
    }
    if (isR && wave == 0) {
      const int ro = (((lane >> 5) * 4 + ((lane >> 2) & 3)) * 8 + ((lane >> 4) & 1) * 4 + (lane & 3)) * 4;
      float dn = 0.f;
#pragma unroll
      for (int w = 0; w < 12; ++w) dn += *(LAS float*)(L + L_RED2 + w * 256 + ro);
      const float gv = (lane & 1) ? bfhi(gbw) : bflo(gbw);
      const float o = *(LAS float*)(L + L_X1S + lane * 4) + rb(gv * rb(dn));
      const unsigned two = pk(o, dpp<QX1>(o));
      if (!(lane & 1)) *(GAS unsigned*)(p.out + rb_ * DM + rc0 + lane) = two;
      if (wg == 0 && lane == 0) {
        const u64 E = ((u64)ldsw(L, C_E1) << 32) | ldsw(L, C_E0);
        __hip_atomic_store((GAS u64*)(p.ws + WS_EPOCH), E + 1, __ATOMIC_RELAXED, __HIP_MEMORY_SCOPE_AGENT);
      }
      STAMP(21);
    }
  }
}

}

extern "C" void kernel_launch(void* const* d_in, const int* in_sizes, int n_in, void* d_out, int out_size, void* d_ws, size_t ws_size, hipStream_t stream) {
  ak::Params p;
  p.x = (const ak::u16*)d_in[0]; p.K = (const ak::u16*)d_in[1]; p.V = (const ak::u16*)d_in[2];
  p.wq = (const ak::u16*)d_in[3]; p.wo = (const ak::u16*)d_in[4];
  p.wg = (const ak::u16*)d_in[6]; p.wu = (const ak::u16*)d_in[7]; p.wd = (const ak::u16*)d_in[8];
  p.c = (const ak::u16*)d_in[9]; p.wm = (const ak::u16*)d_in[10];
  p.out = (ak::u16*)d_out; p.ws = (unsigned char*)d_ws;
  (void)hipFuncSetAttribute((const void*)ak::fused_kernel, hipFuncAttributeMaxDynamicSharedMemorySize, ak::L_END);
  ak::fused_kernel<<<ak::NWG, ak::NTHR, ak::L_END, stream>>>(p);
}
```

```cpp
#include <hip/hip_runtime.h>
#include <stdint.h>

namespace ak {

typedef unsigned short u16;
typedef unsigned long long u64;
typedef unsigned v4u __attribute__((ext_vector_type(4)));
typedef unsigned v2u __attribute__((ext_vector_type(2)));
typedef float f4 __attribute__((ext_vector_type(4)));
typedef float f2 __attribute__((ext_vector_type(2)));
typedef __bf16 bf8 __attribute__((ext_vector_type(8)));
typedef __bf16 bf2 __attribute__((ext_vector_type(2)));
typedef short s4v __attribute__((ext_vector_type(4)));
#define LAS __attribute__((address_space(3)))
#define GAS __attribute__((address_space(1)))
typedef __amdgpu_buffer_rsrc_t rsrc_t;

#define K_R 3
#define K_RTAIL 8
#define K_TTAIL 16
#define K_NP 2
#define K_PF 4
#define K_TRIG_WO 8
#define K_TRIG_FFN 33
#define K_NL 2
#define K_FB 0
#define K_FB1 2
#define K_FB2 6
#define K_FBW 1
#define K_FBPER 6
#define K_PRIO 1
#define K_WOPACE 40
#define K_WNT 1
#define K_FFNDELAY 0
#define K_WOEARLY 32
#define K_FFNEARLY 0
#define K_FPACE 60
#define K_PSL 1
#define K_WNEAR 15
#define K_WSL 1
#define K_FFNTRIG 0
#define K_H1NEAR 6
#define K_H2NEAR 12
#define K_X 0
#define K_YSL 1
#define K_WSL2 24
#define K_DMAR 8
#define K_PFN 12
#define K_PF2 3
#define K_MHOLD 1
#define K_STAMPS 0

constexpr int NB = 8, DM = 576, DFF = 4608, NMOD = 3456, NQ = 4096;
constexpr int NWG = 256, NTHR = 768;
constexpr int NTILE = 512;
constexpr int NP = K_NP, P = NP * 8;
constexpr float LOG2E = 1.4426950408889634f;
constexpr float SCALE = 0.08838834764831845f;
constexpr unsigned SENT = 0xFFFFFFFFu;
constexpr int AUX_NT = 2, AUX_SC1 = 16;
constexpr unsigned SPIN_CAP = 1u << 15, LDS_CAP = 1u << 18;

constexpr int WS_EPOCH = 0, WS_PRG = 1024, WS_CLR = 4096, WS_CLR2 = 6144;
constexpr int WS_MOD = 8192;
constexpr int WS_QP = 65536;
constexpr int WS_OP = WS_QP + 3 * NB * NQ * 4;
constexpr int WS_ML = WS_OP + NWG * 512;
constexpr int WS_YP = WS_ML + NWG * 16;
constexpr int WS_DP = WS_YP + 2 * NB * DM * 4;
constexpr int WS_END = WS_DP + 72 * 144 * 128;
constexpr size_t WS_STAMP = 64u << 20;
constexpr int NSTAMP = 32;

constexpr int L_W = 0;
constexpr int L_SIMG = 73728;
constexpr int L_HIMG = 24576;
constexpr int HSTR = 400;
constexpr int L_WU = 36864, L_WD = 73728;
constexpr int L_GU = 110592;
constexpr int L_ACT = L_GU + 6144;
constexpr int ASTR = 80;
constexpr int L_RED2 = L_ACT + 640;
constexpr int L_X1S = L_RED2 + 3072;
constexpr int L_H2 = L_X1S + 256;
constexpr int XSTR = 1168;
static_assert(L_H2 + 8 * XSTR <= 131072, "ffn lds");
constexpr int L_MRG = 131072;
constexpr int L_RED = L_MRG + 8448;
constexpr int L_KV = 28672, L_KVM = 83968;
constexpr int L_Q = 160128;
constexpr int L_CTL = L_Q + 512;
constexpr int L_STAMP = L_CTL + 128;
constexpr int L_END = L_STAMP + (K_STAMPS ? 12 * 32 * 8 : 0);
static_assert(L_KV + K_NL * 65536 <= L_Q && L_KVM + 65536 <= L_Q && L_END <= 163840, "lds map");
enum { C_SYNC1 = 0, C_HS = 1, C_TRIG = 2, C_MRG = 3, C_DEAD = 4, C_DMA0 = 5, C_DMA1 = 6, C_HS2 = 7, C_KVL = 10, C_Y = 11, C_DMA2 = 12, C_E0 = 8, C_E1 = 9 };

__host__ __device__ constexpr bool role_w(int wg) { return wg >= 144 && wg < 192 && (wg & 7) + 8 * (((wg >> 3) - 18) >> 1) < 18; }
#define K_WT_MC 110
#define K_WT_ML 35
#define K_WT_W 100
#define K_WT_F 30
#define K_WT_N 10
#define K_WT_Q 25
constexpr int FKB = K_FFNEARLY == 1 ? 110 : (K_FFNEARLY == 2 ? 37 : 0);
constexpr int role_kb(int wg) { return wg < 18 ? K_WT_MC + FKB : wg < 54 ? K_WT_ML + FKB : role_w(wg) ? K_WT_W : wg < 144 ? K_WT_F + FKB : wg < 246 ? K_WT_Q : K_WT_N; }
struct SplitTab { unsigned v[256]; };
constexpr SplitTab make_split() {
  SplitTab t{};
  int o[256] = {};
  for (int i = 0; i < 256; ++i) o[i] = i;
  for (int i = 1; i < 256; ++i) {
    const int x = o[i]; int j = i - 1;
    while (j >= 0 && role_kb(o[j]) < role_kb(x)) { o[j + 1] = o[j]; --j; }
    o[j + 1] = x;
  }
  for (int h = 0; h < 128; ++h) {
    const int a = o[h], b = o[255 - h];
    const int n0 = 256 - (role_kb(a) - role_kb(b) + 8) / 16;
    t.v[a] = (unsigned)(h | (n0 << 8));
    t.v[b] = (unsigned)(h | 128 | (n0 << 8));
  }
  return t;
}
__device__ constexpr SplitTab SPLIT = make_split();

struct Params {
  const u16 *x, *K, *V, *wq, *wo, *wg, *wu, *wd, *c, *wm;
  u16* out;
  unsigned char* ws;
};

__device__ __forceinline__ float asf(unsigned u) { return __builtin_bit_cast(float, u); }
__device__ __forceinline__ unsigned asu(float f) { return __builtin_bit_cast(unsigned, f); }
__device__ __forceinline__ float bflo(unsigned u) { return asf(u << 16); }
__device__ __forceinline__ float bfhi(unsigned u) { return asf(u & 0xffff0000u); }
__device__ __forceinline__ unsigned pk(float a, float b) {
  f2 v = {a, b};
  return __builtin_bit_cast(unsigned, __builtin_convertvector(v, bf2));
}
__device__ __forceinline__ float rb(float a) { return bflo(pk(a, a)); }
__device__ __forceinline__ unsigned san(unsigned u) { return u < 0xFFFFFFFEu ? u : 0xFFFFFFFEu; }
__device__ __forceinline__ v4u san4(v4u v) { return v4u{san(v.x), san(v.y), san(v.z), san(v.w)}; }
__device__ __forceinline__ unsigned umax(unsigned a, unsigned b) { return a > b ? a : b; }
__device__ __forceinline__ unsigned max4(v4u v) { return umax(umax(v.x, v.y), umax(v.z, v.w)); }
template <int C> __device__ __forceinline__ float dpp(float x) {
  return asf((unsigned)__builtin_amdgcn_mov_dpp((int)asu(x), C, 0xf, 0xf, true));
}
template <int C> __device__ __forceinline__ unsigned dppu(unsigned x) { return (unsigned)__builtin_amdgcn_mov_dpp((int)x, C, 0xf, 0xf, true); }
constexpr int ROR1 = 0x121, ROR2 = 0x122, ROR4 = 0x124, ROR8 = 0x128, QX1 = 0xB1, QX2 = 0x4E;
__device__ __forceinline__ float x32max(float x) { auto t = __builtin_amdgcn_permlane32_swap(asu(x), asu(x), false, false); return fmaxf(asf(t[0]), asf(t[1])); }
__device__ __forceinline__ float x32sum(float x) { auto t = __builtin_amdgcn_permlane32_swap(asu(x), asu(x), false, false); return asf(t[0]) + asf(t[1]); }
__device__ __forceinline__ float x16sum(float x) { auto t = __builtin_amdgcn_permlane16_swap(asu(x), asu(x), false, false); return asf(t[0]) + asf(t[1]); }
__device__ __forceinline__ float row16sum(float x) { x += dpp<ROR1>(x); x += dpp<ROR2>(x); x += dpp<ROR4>(x); x += dpp<ROR8>(x); return x; }
__device__ __forceinline__ f4 mfma(v4u a, v4u b, f4 c) {
  return __builtin_amdgcn_mfma_f32_16x16x32_bf16(__builtin_bit_cast(bf8, a), __builtin_bit_cast(bf8, b), c, 0, 0, 0);
}
__device__ __forceinline__ rsrc_t mkr(const void* p, unsigned bytes) { return __builtin_amdgcn_make_buffer_rsrc((void*)p, 0, bytes, 0x00020000); }
__device__ __forceinline__ v4u ldsc1(rsrc_t r, int off) { return __builtin_amdgcn_raw_buffer_load_b128(r, off, 0, AUX_SC1); }
__device__ __forceinline__ v2u ldsc1_2(rsrc_t r, int off) { return __builtin_amdgcn_raw_buffer_load_b64(r, off, 0, AUX_SC1); }
__device__ __forceinline__ void stsc1(v4u v, rsrc_t r, int off) { __builtin_amdgcn_raw_buffer_store_b128(v, r, off, 0, AUX_SC1); }
__device__ __forceinline__ void stsc1_2(v2u v, rsrc_t r, int off) { __builtin_amdgcn_raw_buffer_store_b64(v, r, off, 0, AUX_SC1); }
#define MEMBAR() asm volatile("" ::: "memory")
#define VMWAIT(n) asm volatile("s_waitcnt vmcnt(" #n ")" ::: "memory")
#define LGWAIT() asm volatile("s_waitcnt lgkmcnt(0)" ::: "memory")
#define RAWBAR() asm volatile("s_waitcnt lgkmcnt(0)\n\ts_barrier" ::: "memory")
template <bool NT = true> __device__ __forceinline__ void dma16(rsrc_t r, int voff, unsigned soff, unsigned ldsa) {
  unsigned keep;
  if (NT) asm volatile("s_mov_b32 %0, m0\n\ts_mov_b32 m0, %1\n\ts_nop 0\n\tbuffer_load_dwordx4 %2, %3, %4 offen nt lds\n\ts_mov_b32 m0, %0" : "=&s"(keep) : "s"(ldsa), "v"(voff), "s"(r), "s"(soff) : "memory");
  else asm volatile("s_mov_b32 %0, m0\n\ts_mov_b32 m0, %1\n\ts_nop 0\n\tbuffer_load_dwordx4 %2, %3, %4 offen lds\n\ts_mov_b32 m0, %0" : "=&s"(keep) : "s"(ldsa), "v"(voff), "s"(r), "s"(soff) : "memory");
}
__device__ __forceinline__ v4u ld_hidden(rsrc_t r, int voff) {
  v4u v;
  asm volatile("buffer_load_dwordx4 %0, %1, %2, 0 offen" : "=&v"(v) : "v"(voff), "s"(r) : "memory");
  return v;
}
__device__ __forceinline__ v4u lds128(LAS unsigned char* L, int off) { return *(LAS v4u*)(L + off); }
__device__ __forceinline__ v4u trfrag(LAS unsigned char* L, int a0, int d) {
  s4v lo = __builtin_amdgcn_ds_read_tr16_b64_v4i16((LAS s4v*)(L + a0));
  s4v hi = __builtin_amdgcn_ds_read_tr16_b64_v4i16((LAS s4v*)(L + a0 + d));
  v2u l = __builtin_bit_cast(v2u, lo), h = __builtin_bit_cast(v2u, hi);
  return v4u{l.x, l.y, h.x, h.y};
}
__device__ __forceinline__ unsigned ldsw(LAS unsigned char* L, int w) { return *(volatile LAS unsigned*)(L + L_CTL + 4 * w); }
__device__ __forceinline__ void stw(LAS unsigned char* L, int w, unsigned v) { *(volatile LAS unsigned*)(L + L_CTL + 4 * w) = v; }
__device__ __forceinline__ unsigned lds_add(LAS unsigned char* L, int w) {
  unsigned o = 0;
  if ((threadIdx.x & 63) == 0) o = __hip_atomic_fetch_add((LAS unsigned*)(L + L_CTL + 4 * w), 1u, __ATOMIC_RELAXED, __HIP_MEMORY_SCOPE_WORKGROUP);
  return __builtin_amdgcn_readfirstlane(o);
}
template <int SLP = 1> __device__ __forceinline__ void lds_wait(LAS unsigned char* L, int w, unsigned n) {
  for (unsigned spins = 0; __builtin_amdgcn_readfirstlane(ldsw(L, w)) < n; ++spins) {
    if (__builtin_amdgcn_readfirstlane(ldsw(L, C_DEAD))) break;
    if (spins > LDS_CAP) { stw(L, C_DEAD, 1u); break; }
    __builtin_amdgcn_s_sleep(SLP);
  }
  MEMBAR();
}
__device__ __forceinline__ bool give_up(unsigned& spins, LAS unsigned char* L) {
  if (++spins > SPIN_CAP) { stw(L, C_DEAD, 1u); return true; }
  return (spins & 15u) == 0u && __builtin_amdgcn_readfirstlane(ldsw(L, C_DEAD)) != 0u;
}
__device__ __forceinline__ u64 mix64(u64 z) {
  z = (z ^ (z >> 30)) * 0xBF58476D1CE4E5B9ull;
  z = (z ^ (z >> 27)) * 0x94D049BB133111EBull;
  return z ^ (z >> 31);
}
#define STAMP(k) do {} while (0)
__device__ __forceinline__ void throttle(unsigned r) {
  asm volatile("s_cmp_gt_u32 %0, 6\n\ts_cbranch_scc1 9f\n\ts_waitcnt vmcnt(5)\n\t"
               "s_cmp_gt_u32 %0, 5\n\ts_cbranch_scc1 9f\n\ts_waitcnt vmcnt(4)\n\t"
               "s_cmp_gt_u32 %0, 4\n\ts_cbranch_scc1 9f\n\ts_waitcnt vmcnt(3)\n\t"
               "s_cmp_gt_u32 %0, 3\n\ts_cbranch_scc1 9f\n\ts_waitcnt vmcnt(2)\n\t"
               "s_cmp_gt_u32 %0, 2\n\ts_cbranch_scc1 9f\n\ts_waitcnt vmcnt(1)\n9:" ::"s"(r) : "scc", "memory");
}

__device__ __forceinline__ float sigm(float z) { return __builtin_amdgcn_rcpf(1.f + __builtin_amdgcn_exp2f(-LOG2E * z)); }
__device__ __forceinline__ float silu_acc(float a) {
  const float hi = 1.44269502162933349609375f, lo = 1.92596299112661746e-8f;
  const float t0 = -a * hi;
  const float r = __builtin_fmaf(-a, lo, __builtin_fmaf(-a, hi, -t0));
  const float e0 = __builtin_amdgcn_exp2f(fminf(t0, 126.f));
  const float d = 1.f + __builtin_fmaf(e0, r * 0.693147180559945f, e0);
  const float rc = __builtin_amdgcn_rcpf(d), q = a * rc;
  return __builtin_fmaf(__builtin_fmaf(-q, d, a), rc, q);
}
__device__ __forceinline__ float gelu_t(float x) { const float z = 0.7978845608028654f * (x + 0.044715f * x * x * x); return x * sigm(2.f * z); }

struct Soft { float m, l; f4 acc; };
struct Lane { v4u qa0, qa1; unsigned sel[2][4]; int idx0; };

__device__ __forceinline__ float quadsum(f4 d) {
  float t = dpp<0x00>(d.x);
  t += dpp<0x55>(d.y); t += dpp<0xAA>(d.z); t += dpp<0xFF>(d.w);
  return t;
}
__device__ __forceinline__ void softmax_tile(f4 D0, f4 D1, Soft& s, const Lane& c, unsigned& val0, unsigned& val1) {
  const unsigned r1 = pk(quadsum(D0), quadsum(D1));
  const unsigned r2 = pk(bflo(r1) * SCALE, bfhi(r1) * SCALE);
  const float s0 = bflo(r2), s1 = bfhi(r2);
  float mx = fmaxf(s0, s1);
  mx = fmaxf(mx, dpp<ROR4>(mx)); mx = fmaxf(mx, dpp<ROR8>(mx)); mx = x32max(mx);
  const float mn = fmaxf(s.m, mx);
  const float al = __builtin_amdgcn_exp2f((s.m - mn) * LOG2E);
  const float p0 = __builtin_amdgcn_exp2f((s0 - mn) * LOG2E), p1 = __builtin_amdgcn_exp2f((s1 - mn) * LOG2E);
  s.m = mn; s.l = s.l * al + (p0 + p1);
  auto t = __builtin_amdgcn_permlane16_swap(asu(al), asu(al), false, false);
  const float al0 = asf(t[0]), al1 = asf(t[1]);
  s.acc.x *= al0; s.acc.y *= al0; s.acc.z *= al1; s.acc.w *= al1;
  const unsigned pp = pk(p0, p1);
  val0 = (unsigned)__builtin_amdgcn_ds_bpermute(c.idx0, (int)pp);
  val1 = (unsigned)__builtin_amdgcn_ds_bpermute(c.idx0 + 128, (int)pp);
}
template <int U> __device__ __forceinline__ v4u pfrag(unsigned val0, unsigned val1, const Lane& c) {
  const unsigned v = (U & 1) ? val1 : val0;
  constexpr int y = U >> 1;
  return v4u{__builtin_amdgcn_perm(v, v, c.sel[y][0]), __builtin_amdgcn_perm(v, v, c.sel[y][1]), __builtin_amdgcn_perm(v, v, c.sel[y][2]), __builtin_amdgcn_perm(v, v, c.sel[y][3])};
}

__global__ void __launch_bounds__(NTHR, 3) fused_kernel(Params p) {
  extern __shared__ __attribute__((aligned(16))) unsigned char smem[];
  LAS unsigned char* L = (LAS unsigned char*)smem;
  const int tid = threadIdx.x, lane = tid & 63, wave = __builtin_amdgcn_readfirstlane(tid >> 6);
  const int wg = blockIdx.x;
  const unsigned sp = SPLIT.v[wg];
  const unsigned ldsb = (unsigned)(size_t)L;
  STAMP(0);
  if (tid < 32) *(LAS unsigned*)(L + L_CTL + 4 * tid) = 0u;
  const rsrc_t rws = mkr(p.ws, WS_END);
  const int xl = wg & 7, xi = wg >> 3;
  const bool isM = wg < 54, isMc = wg < 18, isQ = wg >= 54 && wg < 246;
  const bool isF = wg < 144;
  const int f = xl * 18 + xi;
  const int wpair = xl + 8 * ((xi - 18) >> 1);
  const bool isW = wg >= 144 && wg < 192 && wpair < 18;
  const int wsig = wpair / 9, wng = 2 * (wpair % 9) + ((xi - 18) & 1);
  const int ahead = sp & 127, asl = (sp >> 7) & 1, an0 = sp >> 8;
  const int ab = ahead >> 4, akv = ahead & 15, aslot = ahead * 2 + asl;
  const int tbeg = asl ? an0 : 0, tend = asl ? NTILE : an0;
  const int qi = wg - 54, qcg = qi / 3, qkq = qi % 3;
  const int g = lane >> 4, q4 = (lane & 15) >> 2, p4 = lane & 3;

  if (wave < 8) {
    v2u E7 = {0u, 0u};
    if (wave == 7) {
      asm volatile("buffer_load_dwordx2 %0, %1, %2, 0 offen sc1" : "=&v"(E7) : "v"(WS_EPOCH), "s"(rws) : "memory");
      const v4u ss = {SENT, SENT, SENT, SENT};
      if (isM) stsc1(ss, rws, WS_MOD + ((lane >> 3) * NMOD + wg * 64) * 2 + (lane & 7) * 16);
      if (isQ) {
#pragma unroll
        for (int j = 0; j < 2; ++j) stsc1(ss, rws, WS_QP + (((qkq * 8 + 4 * j + (lane >> 4)) * NQ) + qcg * 64) * 4 + (lane & 15) * 16);
      }
      if (lane <= 32) stsc1(ss, rws, lane < 32 ? WS_OP + aslot * 512 + lane * 16 : WS_ML + aslot * 16);
      if (isW) stsc1(ss, rws, WS_YP + ((wsig * 8 + (lane >> 3)) * DM + wng * 32) * 4 + (lane & 7) * 16);
    }
    v4u cvm = {0u, 0u, 0u, 0u};
    if (isMc) cvm = ld_hidden(mkr(p.c, NB * DM * 2), tid * 16);
    if (isMc || isQ) {
      const int stride = isM ? NMOD * 2 : NQ * 2, n = isM ? 9 : 3;
      const rsrc_t rw = isM ? mkr(p.wm, DM * NMOD * 2) : mkr(p.wq, DM * NQ * 2);
      const int cb = isM ? wg * 128 : qcg * 128, r0 = isM ? 0 : qkq * 192;
      const int c16 = lane & 7;
#pragma unroll 1
      for (int j = 0; j < n; ++j) {
        const int i = wave * n + j;
        const int fz = ((lane >> 4) & 1) | ((i & 1) << 1);
        dma16(rw, (lane >> 3) * stride + ((((c16 >> 1) ^ fz) << 1) | (c16 & 1)) * 16 + cb, (r0 + i * 8) * stride, ldsb + L_W + i * 1024);
      }
    }
    auto say_cleared = [&]() {
      if (wave == 7) {
        const unsigned e0 = __builtin_amdgcn_readfirstlane(E7.x), e1 = __builtin_amdgcn_readfirstlane(E7.y);
        if (lane == 0) { __hip_atomic_store((GAS u64*)(p.ws + WS_CLR) + wg, mix64((((u64)e1 << 32) | e0) + 1), __ATOMIC_RELAXED, __HIP_MEMORY_SCOPE_AGENT); stw(L, C_E0, e0); stw(L, C_E1, e1); }
        LGWAIT();
        STAMP(22);
      }
    };
    if (isMc) {
      RAWBAR();
      asm volatile("s_waitcnt vmcnt(9)" : "+v"(cvm), "+v"(E7)::"memory");
      __builtin_amdgcn_sched_barrier(0);
      say_cleared();
      v4u o;
#pragma unroll
      for (int e = 0; e < 4; ++e) o[e] = pk(silu_acc(bflo(cvm[e])), silu_acc(bfhi(cvm[e])));
      *(LAS v4u*)(L + L_SIMG + (tid / 72) * XSTR + (tid % 72) * 16) = o;
      asm volatile("s_waitcnt vmcnt(0) lgkmcnt(0)" ::: "memory");
      lds_add(L, C_DMA0);
    }
    const unsigned char* kb = (const unsigned char*)p.K + ((size_t)ahead << 21);
    const unsigned char* vb = (const unsigned char*)p.V + ((size_t)ahead << 21);
    const int koff = ((lane >> 2) & 3) * 256 + (g * 4 + (lane & 3)) * 16, voff = lane * 16;
    const int tw = tbeg + wave;
#define KVT(tb, n) (tw + 8 * ((tb) + ((n) >> 3)))
#define KVLOAD(tb, n) (((n) & 4) ? __builtin_amdgcn_raw_buffer_load_b128(mkr(vb, KVT(tb, n) < tend ? (1u << 21) : 0u), voff, KVT(tb, n) * 4096 + ((n) & 3) * 1024, AUX_NT) \
                                 : __builtin_amdgcn_raw_buffer_load_b128(mkr(kb, KVT(tb, n) < tend ? (1u << 21) : 0u), koff, KVT(tb, n) * 4096 + ((n) & 3) * 1024, AUX_NT))
#define SB() __builtin_amdgcn_sched_barrier(0)
    v4u ring[8], pre[P];
#pragma unroll
    for (int n = 0; n < 8 + P; ++n) {
      if (n == K_PF && !isMc) { RAWBAR(); asm volatile("s_waitcnt vmcnt(%1)" : "+v"(E7) : "n"(K_PF) : "memory"); __builtin_amdgcn_sched_barrier(0); say_cleared(); lds_add(L, C_DMA0); }
      if (n >= K_PF) throttle(n < K_PFN ? K_PF : K_PF2);
      if (n < 8) ring[n] = KVLOAD(0, n); else pre[n - 8] = KVLOAD(0, n);
      SB();
    }
    const int nl = isMc ? (K_NL < 1 ? K_NL : 1) : K_NL;
    const int kvl = (isMc ? L_KVM : L_KV) + wave * (nl * 8192);
    {
      const rsrc_t rk = mkr(kb, 1 << 21), rv = mkr(vb, 1 << 21);
#pragma unroll 1
      for (int t = 0; t < nl; ++t) {
#pragma unroll
        for (int n = 0; n < 8; ++n) {
          throttle(K_PF2);
          dma16((n & 4) ? rv : rk, (n & 4) ? voff : koff, (tw + 8 * (NP + 1 + t)) * 4096 + (n & 3) * 1024, ldsb + kvl + t * 8192 + n * 1024);
        }
      }
    }
    const int tn = NP + 1 + nl;
    Lane c;
    {
      const int i = lane & 15, beta = i >> 2, pos = i & 1;
#pragma unroll
      for (int y = 0; y < 2; ++y)
#pragma unroll
        for (int w = 0; w < 4; ++w) {
          const unsigned b0 = 2 * y, b1 = 2 * y + 1;
          c.sel[y][w] = beta != w ? 0x0c0c0c0cu : (pos ? ((b1 << 24) | (b0 << 16) | 0x0c0cu) : (0x0c0c0000u | (b1 << 8) | b0));
        }
      c.idx0 = (16 * ((i >> 1) & 1) + 4 * g) * 4;
    }
    if (K_PRIO && wave >= 4) __builtin_amdgcn_s_setprio(1);
    STAMP(1);
    RAWBAR();
    STAMP(2);
    {
      const int i = lane & 15;
      const v4u qf = lds128(L, L_Q + ((i >> 2) & 1) * 256 + (4 * g + (i & 3)) * 16);
      const v4u z = {0u, 0u, 0u, 0u};
      c.qa0 = (i >> 3) ? z : qf;
      c.qa1 = (i >> 3) ? qf : z;
    }
    Soft st;
    st.m = -INFINITY; st.l = 0.f; st.acc = f4{0.f, 0.f, 0.f, 0.f};
    const f4 zero = {0.f, 0.f, 0.f, 0.f};
    unsigned rr = K_R;
    {
      f4 D0 = mfma(c.qa0, ring[0], zero); SB(); ring[0] = KVLOAD(tn, 0); SB();
      D0 = mfma(c.qa1, ring[1], D0);      SB(); ring[1] = KVLOAD(tn, 1); SB();
      f4 D1 = mfma(c.qa0, ring[2], zero); SB(); ring[2] = KVLOAD(tn, 2); SB();
      D1 = mfma(c.qa1, ring[3], D1);      SB(); throttle(rr); ring[3] = KVLOAD(tn, 3); SB();
      unsigned v0, v1;
      softmax_tile(D0, D1, st, c, v0, v1);
      st.acc = mfma(pfrag<0>(v0, v1, c), ring[4], st.acc);
      st.acc = mfma(pfrag<1>(v0, v1, c), ring[5], st.acc);
      st.acc = mfma(pfrag<2>(v0, v1, c), ring[6], st.acc);
      st.acc = mfma(pfrag<3>(v0, v1, c), ring[7], st.acc);
      SB();
#pragma unroll
      for (int t = 0; t < NP; ++t) {
        D0 = mfma(c.qa0, pre[8 * t + 0], zero); D0 = mfma(c.qa1, pre[8 * t + 1], D0);
        D1 = mfma(c.qa0, pre[8 * t + 2], zero); D1 = mfma(c.qa1, pre[8 * t + 3], D1);
        softmax_tile(D0, D1, st, c, v0, v1);
        st.acc = mfma(pfrag<0>(v0, v1, c), pre[8 * t + 4], st.acc);
        st.acc = mfma(pfrag<1>(v0, v1, c), pre[8 * t + 5], st.acc);
        st.acc = mfma(pfrag<2>(v0, v1, c), pre[8 * t + 6], st.acc);
        st.acc = mfma(pfrag<3>(v0, v1, c), pre[8 * t + 7], st.acc);
        SB();
      }
      VMWAIT(4);
#pragma unroll 1
      for (int t = 0; t < nl; ++t) {
        const int a = kvl + t * 8192 + lane * 16;
        D0 = mfma(c.qa0, lds128(L, a), zero); D0 = mfma(c.qa1, lds128(L, a + 1024), D0);
        D1 = mfma(c.qa0, lds128(L, a + 2048), zero); D1 = mfma(c.qa1, lds128(L, a + 3072), D1);
        softmax_tile(D0, D1, st, c, v0, v1);
        st.acc = mfma(pfrag<0>(v0, v1, c), lds128(L, a + 4096), st.acc);
        st.acc = mfma(pfrag<1>(v0, v1, c), lds128(L, a + 5120), st.acc);
        st.acc = mfma(pfrag<2>(v0, v1, c), lds128(L, a + 6144), st.acc);
        st.acc = mfma(pfrag<3>(v0, v1, c), lds128(L, a + 7168), st.acc);
      }
      LGWAIT();
      lds_add(L, C_KVL);
      throttle(rr); ring[4] = KVLOAD(tn, 4); SB();
      throttle(rr); ring[5] = KVLOAD(tn, 5); SB();
      throttle(rr); ring[6] = KVLOAD(tn, 6); SB();
      throttle(rr); ring[7] = KVLOAD(tn, 7); SB();
    }
#pragma unroll 1
    for (int t0 = tn; tw + 8 * t0 < tend; ++t0) {
      if (t0 == K_TRIG_WO && lane == 0 && wave == 0) stw(L, C_TRIG, 1u);
      if (tw + 8 * t0 + K_TTAIL >= tend) rr = K_RTAIL;
      f4 D0 = mfma(c.qa0, ring[0], zero); SB(); throttle(rr); ring[0] = KVLOAD(t0 + 1, 0); SB();
      D0 = mfma(c.qa1, ring[1], D0);      SB(); throttle(rr); ring[1] = KVLOAD(t0 + 1, 1); SB();
      f4 D1 = mfma(c.qa0, ring[2], zero); SB(); throttle(rr); ring[2] = KVLOAD(t0 + 1, 2); SB();
      D1 = mfma(c.qa1, ring[3], D1);      SB(); throttle(rr); ring[3] = KVLOAD(t0 + 1, 3); SB();
      unsigned v0, v1;
      softmax_tile(D0, D1, st, c, v0, v1);
      st.acc = mfma(pfrag<0>(v0, v1, c), ring[4], st.acc); SB(); throttle(rr); ring[4] = KVLOAD(t0 + 1, 4); SB();
      st.acc = mfma(pfrag<1>(v0, v1, c), ring[5], st.acc); SB(); throttle(rr); ring[5] = KVLOAD(t0 + 1, 5); SB();
      st.acc = mfma(pfrag<2>(v0, v1, c), ring[6], st.acc); SB(); throttle(rr); ring[6] = KVLOAD(t0 + 1, 6); SB();
      st.acc = mfma(pfrag<3>(v0, v1, c), ring[7], st.acc); SB(); throttle(rr); ring[7] = KVLOAD(t0 + 1, 7); SB();
    }
#undef KVLOAD
    if (lds_add(L, 18) == 7u && lane == 0) stw(L, C_TRIG, 2u);
    STAMP(3);
    float l = st.l;
    l += dpp<ROR4>(l); l += dpp<ROR8>(l); l = x32sum(l);
    LAS float* mg = (LAS float*)(L + L_MRG) + wave * 264;
    const int cc = lane & 15;
    *(LAS f2*)(mg + 8 + 8 * cc + 2 * g) = f2{st.acc.x, st.acc.y};
    *(LAS f2*)(mg + 8 + 128 + 8 * cc + 2 * g) = f2{st.acc.z, st.acc.w};
    if (lane == 0 || lane == 16) *(LAS f2*)(mg + 2 * g) = f2{st.m, l};
    LGWAIT();
    if (lds_add(L, C_MRG) == 7u) {
      MEMBAR();
      const int h = lane >> 5, d0 = 4 * (lane & 31);
      LAS float* m0 = (LAS float*)(L + L_MRG);
      f2 ml[8]; f4 a[8];
#pragma unroll
      for (int w = 0; w < 8; ++w) { ml[w] = *(LAS f2*)(m0 + w * 264 + 2 * h); a[w] = *(LAS f4*)(m0 + w * 264 + 8 + 128 * h + d0); }
      float M = ml[0].x;
#pragma unroll
      for (int w = 1; w < 8; ++w) M = fmaxf(M, ml[w].x);
      float Ls = 0.f; f4 O = {0.f, 0.f, 0.f, 0.f};
#pragma unroll
      for (int w = 0; w < 8; ++w) { const float e = __builtin_amdgcn_exp2f((ml[w].x - M) * LOG2E); Ls += e * ml[w].y; O += e * a[w]; }
      const float inv = __builtin_amdgcn_rcpf(Ls);
      const unsigned o0 = pk(O.x * inv, O.y * inv), o1 = pk(O.z * inv, O.w * inv);
      const unsigned n0 = dppu<QX1>(o0), n1 = dppu<QX1>(o1);
      if (!(lane & 1)) stsc1(san4(v4u{o0, o1, n0, n1}), rws, WS_OP + aslot * 512 + (h * 128 + d0) * 2);
      const unsigned M1 = __builtin_amdgcn_readlane(asu(M), 32), L1 = __builtin_amdgcn_readlane(asu(Ls), 32);
      if (lane == 0) stsc1(san4(v4u{asu(M), asu(Ls), M1, L1}), rws, WS_ML + aslot * 16);
      STAMP(4);
    }
  } else {
    RAWBAR();
    const int sw = wave - 8, stid = tid - 512;
    const u64 Es = __hip_atomic_load((GAS u64*)(p.ws + WS_EPOCH), __ATOMIC_RELAXED, __HIP_MEMORY_SCOPE_AGENT);
    const int fz = (q4 >> 1) | ((g & 1) << 1);
    auto mod_mfma = [&]() {
      STAMP(5);
      f4 D[3] = {{0.f, 0.f, 0.f, 0.f}, {0.f, 0.f, 0.f, 0.f}, {0.f, 0.f, 0.f, 0.f}};
#pragma unroll
      for (int ks = 0; ks < 18; ++ks) {
        const v4u wf = trfrag(L, L_W + (32 * ks + 8 * g + q4) * 128 + (sw ^ fz) * 32 + p4 * 8, 4 * 128);
        const v4u sf = lds128(L, L_SIMG + (lane & 7) * XSTR + (32 * ks + 8 * g) * 2);
        D[ks % 3] = mfma(wf, sf, D[ks % 3]);
      }
      const f4 Ds = (D[0] + D[1]) + D[2];
      if ((lane & 15) < 8) stsc1_2(v2u{san(pk(Ds.x, Ds.y)), san(pk(Ds.z, Ds.w))}, rws, WS_MOD + ((lane & 7) * NMOD + wg * 64 + sw * 16 + 4 * g) * 2);
      STAMP(6);
    };
    const u64 tagv = mix64((((u64)__builtin_amdgcn_readfirstlane((unsigned)(Es >> 32)) << 32) | (unsigned)__builtin_amdgcn_readfirstlane((unsigned)Es)) + 1);
    const unsigned tg0 = (unsigned)tagv, tg1 = (unsigned)(tagv >> 32);
    if (isMc) {
      if (sw == 0) {
        const v4u cv = __builtin_amdgcn_raw_buffer_load_b128(mkr(p.c, NB * DM * 2), tid * 16, 0, 0);
        v4u o;
#pragma unroll
        for (int e = 0; e < 4; ++e) o[e] = pk(silu_acc(bflo(cv[e])), silu_acc(bfhi(cv[e])));
        *(LAS v4u*)(L + L_SIMG + (tid / 72) * XSTR + (tid % 72) * 16) = o;
        LGWAIT();
        lds_add(L, C_SYNC1);
      }
      STAMP(23);
      lds_wait(L, C_SYNC1, 1); lds_wait(L, C_DMA0, 8);
      mod_mfma();
    } else if (isQ) {
      const int qb = 2 * sw + (lane >> 5), qj = lane & 31;
      const bool qact = qj < 24;
      const rsrc_t rx = mkr(p.x, NB * DM * 2);
      v4u xv[3];
#pragma unroll
      for (int t = 0; t < 3; ++t) xv[t] = __builtin_amdgcn_raw_buffer_load_b128(rx, qact ? (qb * DM + 192 * t + 8 * qj) * 2 : 0x7ffffff0, 0, 0);
      float xf[3][8];
      float sum = 0.f;
#pragma unroll
      for (int t = 0; t < 3; ++t)
#pragma unroll
        for (int e = 0; e < 4; ++e) { xf[t][2 * e] = bflo(xv[t][e]); xf[t][2 * e + 1] = bfhi(xv[t][e]); sum += xf[t][2 * e] + xf[t][2 * e + 1]; }
      sum = x16sum(row16sum(sum));
      const float mean = sum * (1.f / DM);
      float vs = 0.f;
#pragma unroll
      for (int t = 0; t < 3; ++t)
#pragma unroll
        for (int e = 0; e < 8; ++e) { const float d = xf[t][e] - mean; vs += qact ? d * d : 0.f; }
      vs = x16sum(row16sum(vs));
      const float mb = rb(mean), den = rb(__builtin_sqrtf(rb(rb(vs * (1.f / DM)) + 1e-5f)));
      float ln[8];
#pragma unroll
      for (int e = 0; e < 8; ++e) { const float xe = qkq == 0 ? xf[0][e] : (qkq == 1 ? xf[1][e] : xf[2][e]); ln[e] = rb(rb(xe - mb) / den); }
      lds_wait(L, C_DMA0, 8);
      v4u wf[6];
#pragma unroll
      for (int ks = 0; ks < 6; ++ks) wf[ks] = trfrag(L, L_W + (32 * ks + 8 * g + q4) * 128 + (sw ^ fz) * 32 + p4 * 8, 4 * 128);
      STAMP(29);
      if (!(K_X & 16)) {
      v4u sa, ca;
      const int mo = qact ? WS_MOD + (qb * NMOD + 192 * qkq + 8 * qj) * 2 : 0x7ffffff0;
      if (sw == 0) {
        const int pw = (lane < 3 ? 0 : 6) + 3 * qkq + lane;
        const int fo = lane < 6 ? WS_CLR + pw * 8 : 0x7ffffff0, io = lane < 6 ? WS_MOD + (7 * NMOD + pw * 64 + 62) * 2 : 0x7ffffff0;
        bool cleared = false;
        for (unsigned spins = 0;;) {
          MEMBAR();
          if (!cleared) { const v2u fl = ldsc1_2(rws, fo); cleared = __all(lane >= 6 || (fl.x == tg0 && fl.y == tg1)); }
          else if (__builtin_popcountll(__builtin_amdgcn_ballot_w64(lane < 6 && __builtin_amdgcn_raw_buffer_load_b32(rws, io, 0, AUX_SC1) != SENT)) >= K_H1NEAR) break;
          if (give_up(spins, L)) break;
          __builtin_amdgcn_s_sleep(K_PSL);
        }
        if (lane == 0) stw(L, C_HS, 1u);
      } else lds_wait(L, C_HS, 1);
      for (unsigned spins = 0;;) {
        MEMBAR();
        sa = ldsc1(rws, mo); ca = ldsc1(rws, qact ? mo + DM * 2 : mo);
        if (__all(umax(max4(sa), max4(ca)) != SENT)) break;
        if (give_up(spins, L)) break;
        __builtin_amdgcn_s_sleep(K_PSL);
      }
      STAMP(8);
      v4u h;
#pragma unroll
      for (int e = 0; e < 4; ++e) {
        const unsigned c1 = pk(1.f + bflo(ca[e]), 1.f + bfhi(ca[e]));
        const unsigned m1 = pk(ln[2 * e] * bflo(c1), ln[2 * e + 1] * bfhi(c1));
        h[e] = pk(bflo(m1) + bflo(sa[e]), bfhi(m1) + bfhi(sa[e]));
      }
      if (qact) *(LAS v4u*)(L + L_HIMG + qb * HSTR + qj * 16) = h;
      LGWAIT();
      lds_add(L, C_SYNC1); lds_wait(L, C_SYNC1, 4);
      f4 D0 = {0.f, 0.f, 0.f, 0.f}, D1 = {0.f, 0.f, 0.f, 0.f};
#pragma unroll
      for (int ks = 0; ks < 6; ks += 2) {
        D0 = mfma(wf[ks], lds128(L, L_HIMG + (lane & 7) * HSTR + (32 * ks + 8 * g) * 2), D0);
        D1 = mfma(wf[ks + 1], lds128(L, L_HIMG + (lane & 7) * HSTR + (32 * ks + 32 + 8 * g) * 2), D1);
      }
      if ((lane & 15) < 8) stsc1(san4(__builtin_bit_cast(v4u, D0 + D1)), rws, WS_QP + ((qkq * 8 + (lane & 7)) * NQ + qcg * 64 + sw * 16 + 4 * g) * 4);
      STAMP(9);
      }
    }
    if (sw == 0 && !(K_X & 8)) {
      v4u a0, a1, a2;
      const int qo = WS_QP + (ab * NQ + akv * 256 + 4 * lane) * 4;
      const int fo = lane < 12 ? WS_CLR + (54 + 12 * akv + lane) * 8 : 0x7ffffff0;
      const int io = lane < 12 ? WS_QP + (((lane % 3) * 8 + ab) * NQ + (4 * akv + lane / 3) * 64 + 63) * 4 : 0x7ffffff0;
      bool cleared = false;
      for (unsigned spins = 0;;) {
        MEMBAR();
        if (!cleared) { const v2u fl = ldsc1_2(rws, fo); cleared = __all(lane >= 12 || (fl.x == tg0 && fl.y == tg1)); }
        else if (__builtin_popcountll(__builtin_amdgcn_ballot_w64(lane < 12 && __builtin_amdgcn_raw_buffer_load_b32(rws, io, 0, AUX_SC1) != SENT)) >= K_H2NEAR) break;
        if (give_up(spins, L)) break;
        __builtin_amdgcn_s_sleep(K_PSL);
      }
      for (unsigned spins = 0;;) {
        MEMBAR();
        a0 = ldsc1(rws, qo); a1 = ldsc1(rws, qo + NB * NQ * 4); a2 = ldsc1(rws, qo + 2 * NB * NQ * 4);
        if (__all(umax(umax(max4(a0), max4(a1)), max4(a2)) != SENT)) break;
        if (give_up(spins, L)) break;
        __builtin_amdgcn_s_sleep(K_PSL);
      }
      const f4 s = (__builtin_bit_cast(f4, a0) + __builtin_bit_cast(f4, a1)) + __builtin_bit_cast(f4, a2);
      *(LAS v2u*)(L + L_Q + lane * 8) = v2u{pk(s.x, s.y), pk(s.z, s.w)};
      STAMP(10);
    }
    RAWBAR();
    if (isF && sw == 3) {
      const v4u ss = {SENT, SENT, SENT, SENT};
#pragma unroll 3
      for (int j = 0; j < (K_X & 1 ? 0 : 9); ++j) stsc1(ss, rws, WS_DP + ((j * 8 + (lane >> 3)) * 144 + f) * 128 + (lane & 7) * 16);
      VMWAIT(0);
      if (lane == 0) __hip_atomic_store((GAS u64*)(p.ws + WS_CLR2) + f, tagv, __ATOMIC_RELAXED, __HIP_MEMORY_SCOPE_AGENT);
    }
    if (isM && !isMc) {
      lds_wait(L, C_DMA0, 8); lds_wait(L, C_KVL, 8);
      const rsrc_t rw = mkr(p.wm, DM * NMOD * 2);
      const int c16 = lane & 7;
#pragma unroll 1
      for (int j = 0; j < (K_X & 4 ? 0 : 18); ++j) {
        const int i = sw * 18 + j;
        const int fs = ((lane >> 4) & 1) | ((i & 1) << 1);
        dma16(rw, (lane >> 3) * (NMOD * 2) + ((((c16 >> 1) ^ fs) << 1) | (c16 & 1)) * 16 + wg * 128, i * 8 * (NMOD * 2), ldsb + L_W + i * 1024);
        if (K_WOPACE) __builtin_amdgcn_s_sleep(K_WOPACE);
      }
      const rsrc_t rc = mkr(p.c, NB * DM * 2);
#pragma unroll 1
      for (int j = 0; j < 3; ++j) {
        const int ch = stid + 256 * j;
        const v4u cv = __builtin_amdgcn_raw_buffer_load_b128(rc, ch * 16, 0, 0);
        v4u o;
#pragma unroll
        for (int e = 0; e < 4; ++e) o[e] = pk(silu_acc(bflo(cv[e])), silu_acc(bfhi(cv[e])));
        if (ch < 576) *(LAS v4u*)(L + L_SIMG + (ch / 72) * XSTR + (ch % 72) * 16) = o;
      }
      VMWAIT(0); LGWAIT();
      lds_add(L, C_SYNC1); lds_wait(L, C_SYNC1, 4);
      mod_mfma();
    }
    if ((isF || isW) && sw == 3) {
      for (unsigned spins = 0;;) {
        MEMBAR();
        const v4u a = ldsc1(rws, WS_CLR + lane * 32), b = ldsc1(rws, WS_CLR + lane * 32 + 16);
        bool ok = a.x == tg0 && a.y == tg1 && a.z == tg0 && a.w == tg1 && b.x == tg0 && b.y == tg1 && b.z == tg0 && b.w == tg1;
        if (isF) {
          const v4u c2 = ldsc1(rws, lane < 36 ? WS_CLR2 + lane * 32 : 0x7ffffff0), d2 = ldsc1(rws, lane < 36 ? WS_CLR2 + lane * 32 + 16 : 0x7ffffff0);
          ok = ok && (lane >= 36 || (c2.x == tg0 && c2.y == tg1 && c2.z == tg0 && c2.w == tg1 && d2.x == tg0 && d2.y == tg1 && d2.z == tg0 && d2.w == tg1));
        }
        if (__all(ok)) break;
        if (give_up(spins, L)) break;
        __builtin_amdgcn_s_sleep(127);
      }
      if (lane == 0) stw(L, C_HS2, 1u);
      STAMP(24);
    }
    if (isW || isF) {
      if (isM && !isMc) { lds_add(L, 17); lds_wait(L, 17, 4); }
      lds_wait(L, C_KVL, 8);
      const int sc = ((lane & 3) ^ ((lane >> 4) & 2)) * 16;
      if (isW) {
        const rsrc_t rw = mkr(p.wo, NQ * DM * 2);
        const int vo = (lane >> 2) * (DM * 2) + sc + wng * 64;
        lds_wait<16>(L, C_TRIG, 1);
        STAMP(11);
#pragma unroll 1
        for (int j = 0; j < (K_X & 2 ? 0 : K_WOEARLY); ++j) { const int i = j * 4 + sw; dma16<K_WNT != 0>(rw, vo, (wsig * 2048 + i * 16) * DM * 2, ldsb + L_W + i * 1024); if (K_WOPACE) __builtin_amdgcn_s_sleep(K_WOPACE); }
        lds_wait<4>(L, C_TRIG, 2);
#pragma unroll 1
        for (int j = K_WOEARLY; j < 32; ++j) { const int i = j * 4 + sw; dma16<K_WNT != 0>(rw, vo, (wsig * 2048 + i * 16) * DM * 2, ldsb + L_W + i * 1024); }
      } else {
        const rsrc_t rg = mkr(p.wg, DM * DFF * 2), ru = mkr(p.wu, DM * DFF * 2), rd = mkr(p.wd, DFF * DM * 2);
        const int vo = (lane >> 2) * (DFF * 2) + sc + f * 64;
        lds_wait<4>(L, C_TRIG, K_FFNEARLY ? 1 : 2);
        STAMP(11);
        for (int k = 0; k < K_FFNDELAY; ++k) __builtin_amdgcn_s_sleep(16);
        if (K_FFNEARLY == 2) {
#pragma unroll 1
          for (int j = 0; j < 9; ++j) { const int i = j * 4 + sw; dma16(rd, lane * 16, f * 32 * DM * 2 + i * 1024, ldsb + L_WD + i * 1024); __builtin_amdgcn_s_sleep(K_FPACE); }
          lds_wait<4>(L, C_TRIG, 2);
        }
#pragma unroll 1
        for (int j = 0; j < 9; ++j) {
          const int i = j * 4 + sw;
          throttle(K_DMAR); dma16<K_WNT != 0>(rg, vo, i * 16 * DFF * 2, ldsb + L_W + i * 1024); if (K_FFNEARLY == 1) __builtin_amdgcn_s_sleep(K_FPACE);
          throttle(K_DMAR); dma16<K_WNT != 0>(ru, vo, i * 16 * DFF * 2, ldsb + L_WU + i * 1024); if (K_FFNEARLY == 1) __builtin_amdgcn_s_sleep(K_FPACE);
        }
        if (K_FFNEARLY != 2) {
#pragma unroll 1
          for (int j = 0; j < 9; ++j) {
            throttle(K_DMAR);
            if (j == (K_DMAR < 8 ? K_DMAR - 1 : 8)) { if (K_DMAR >= 8) VMWAIT(8); lds_add(L, C_DMA1); }
            const int i = j * 4 + sw; dma16(rd, lane * 16, f * 32 * DM * 2 + i * 1024, ldsb + L_WD + i * 1024); if (K_FFNEARLY) __builtin_amdgcn_s_sleep(K_FPACE);
          }
        }
      }
      VMWAIT(0);
      if (isW || K_FFNEARLY == 2) lds_add(L, C_DMA1);
      lds_add(L, C_DMA2);
      STAMP(12);
    }
  }

  if (isW) {
    const int cc = lane & 15;
    if (wave < 8) {
      const int pid = (cc & 7) * 32 + (wsig * 8 + wave) * 2 + (cc >> 3);
      lds_wait(L, C_HS2, 1);
      lds_wait(L, C_DMA1, 4);
      v4u wf[2][4][2];
#pragma unroll
      for (int hh = 0; hh < 2; ++hh)
#pragma unroll
        for (int ks = 0; ks < 4; ++ks)
#pragma unroll
          for (int nt = 0; nt < 2; ++nt) wf[hh][ks][nt] = trfrag(L, L_W + (wave * 256 + hh * 128 + 32 * ks + 8 * g + q4) * 64 + (nt ^ (g & 1)) * 32 + p4 * 8, 4 * 64);
      v4u ml, fr[2][4];
      for (unsigned spins = 0;;) {
        MEMBAR();
        ml = ldsc1(rws, WS_ML + pid * 16);
        const int nv = __builtin_popcountll(__builtin_amdgcn_ballot_w64(max4(ml) != SENT));
        if (nv >= 4 * K_WNEAR) break;
        if (give_up(spins, L)) break;
        if (nv < 4 * 12) __builtin_amdgcn_s_sleep(K_WSL2); else __builtin_amdgcn_s_sleep(K_WSL);
      }
      STAMP(13);
      for (unsigned spins = 0;;) {
        MEMBAR();
        ml = ldsc1(rws, WS_ML + pid * 16);
#pragma unroll
        for (int hh = 0; hh < 2; ++hh)
#pragma unroll
          for (int ks = 0; ks < 4; ++ks) fr[hh][ks] = ldsc1(rws, WS_OP + pid * 512 + (hh * 128 + 32 * ks + 8 * g) * 2);
        unsigned mx = max4(ml);
#pragma unroll
        for (int hh = 0; hh < 2; ++hh)
#pragma unroll
          for (int ks = 0; ks < 4; ++ks) mx = umax(mx, max4(fr[hh][ks]));
        if (__all(mx != SENT)) break;
        if (give_up(spins, L)) break;
      }
      float wgt[2];
#pragma unroll
      for (int hh = 0; hh < 2; ++hh) {
        const float M = asf(ml[2 * hh]), Lv = asf(ml[2 * hh + 1]);
        const float M2 = dpp<ROR8>(M), L2 = dpp<ROR8>(Lv), Mx = fmaxf(M, M2);
        const float e1 = __builtin_amdgcn_exp2f((M - Mx) * LOG2E) * Lv, e2 = __builtin_amdgcn_exp2f((M2 - Mx) * LOG2E) * L2;
        wgt[hh] = e1 * __builtin_amdgcn_rcpf(e1 + e2);
      }
      STAMP(14);
#pragma unroll
      for (int nt = 0; nt < 2; ++nt) {
        f4 d0 = {0.f, 0.f, 0.f, 0.f}, d1 = {0.f, 0.f, 0.f, 0.f};
#pragma unroll
        for (int ks = 0; ks < 4; ++ks) { d0 = mfma(wf[0][ks][nt], fr[0][ks], d0); d1 = mfma(wf[1][ks][nt], fr[1][ks], d1); }
        f4 t = wgt[0] * d0 + wgt[1] * d1;
        t.x += dpp<ROR8>(t.x); t.y += dpp<ROR8>(t.y); t.z += dpp<ROR8>(t.z); t.w += dpp<ROR8>(t.w);
        if (cc < 8) *(LAS f4*)(L + L_RED + (((wave * 2 + nt) * 4 + g) * 8 + cc) * 16) = t;
      }
      LGWAIT();
      if (lds_add(L, C_SYNC1 + 16) == 7u) {
        MEMBAR();
        f4 s = {0.f, 0.f, 0.f, 0.f};
#pragma unroll
        for (int w = 0; w < 8; ++w) s += *(LAS f4*)(L + L_RED + (w * 64 + lane) * 16);
        const int nt = lane >> 5, rr = (lane >> 3) & 3, b = lane & 7;
        stsc1(san4(__builtin_bit_cast(v4u, s)), rws, WS_YP + ((wsig * 8 + b) * DM + wng * 32 + nt * 16 + 4 * rr) * 4);
        STAMP(15);
      }
    }
  } else if (isF) {
    const bool isR = f < 72;
    const int rb_ = f / 9, rc0 = (f % 9) * 64;
    unsigned gbw = 0;
    const int gt = wave & 3, kt = wave >> 2;
    v4u wf[6], wd[4];
    const int dw = wave - 3;
    bool have_wd = false;
    auto read_wd = [&]() {
      lds_wait(L, C_DMA2, 4);
#pragma unroll
      for (int j = 0; j < 4; ++j) wd[j] = trfrag(L, L_WD + (8 * g + q4) * (DM * 2) + (dw >= 0 ? 4 * dw + j : 0) * 32 + p4 * 8, 4 * DM * 2);
    };
    if (wave < 8) {
      const int b = wave;
      const bool act = lane < 48;
      const rsrc_t rx = mkr(p.x, NB * DM * 2);
      v2u xv[3], ga[3], sb[3], cb[3];
      lds_wait(L, C_HS2, 1);
#pragma unroll
      for (int t = 0; t < 3; ++t) xv[t] = __builtin_amdgcn_raw_buffer_load_b64(rx, act ? (b * DM + 4 * (lane + 48 * t)) * 2 : 0x7ffffff0, 0, 0);
      for (unsigned spins = 0;;) {
        MEMBAR();
        unsigned mx = 0;
#pragma unroll
        for (int t = 0; t < 3; ++t) {
          const int o = act ? WS_MOD + (b * NMOD + 4 * (lane + 48 * t)) * 2 : 0x7ffffff0;
          ga[t] = ldsc1_2(rws, act ? o + 2 * DM * 2 : o); sb[t] = ldsc1_2(rws, act ? o + 3 * DM * 2 : o); cb[t] = ldsc1_2(rws, act ? o + 4 * DM * 2 : o);
          mx = umax(mx, umax(umax(umax(ga[t].x, ga[t].y), umax(sb[t].x, sb[t].y)), umax(cb[t].x, cb[t].y)));
        }
        if (wave == 0) { gbw = __builtin_amdgcn_raw_buffer_load_b32(rws, isR ? WS_MOD + (rb_ * NMOD + 5 * DM + rc0 + (lane & ~1)) * 2 : 0x7ffffff0, 0, AUX_SC1); mx = umax(mx, gbw); }
        if (__all(mx != SENT)) break;
        if (give_up(spins, L)) break;
      }
      float xf[12], gf[12], cf[12], sf[12];
#pragma unroll
      for (int t = 0; t < 3; ++t)
#pragma unroll
        for (int e = 0; e < 2; ++e) {
          xf[4 * t + 2 * e] = bflo(xv[t][e]); xf[4 * t + 2 * e + 1] = bfhi(xv[t][e]);
          gf[4 * t + 2 * e] = bflo(ga[t][e]); gf[4 * t + 2 * e + 1] = bfhi(ga[t][e]);
          cf[4 * t + 2 * e] = 1.f + bflo(cb[t][e]); cf[4 * t + 2 * e + 1] = 1.f + bfhi(cb[t][e]);
          sf[4 * t + 2 * e] = bflo(sb[t][e]); sf[4 * t + 2 * e + 1] = bfhi(sb[t][e]);
        }
#pragma unroll
      for (int e = 0; e < 12; ++e) asm volatile("" : "+v"(xf[e]), "+v"(gf[e]), "+v"(cf[e]), "+v"(sf[e]));
      lds_wait(L, C_DMA1, 4);
#pragma unroll
      for (int k6 = 0; k6 < 6; ++k6) wf[k6] = trfrag(L, ((gt >> 1) ? L_WU : L_W) + (32 * (kt * 6 + k6) + 8 * g + q4) * 64 + ((gt & 1) ^ (g & 1)) * 32 + p4 * 8, 4 * 64);
      if (wave >= 3 && __builtin_amdgcn_readfirstlane(ldsw(L, C_DMA2)) >= 4u) { read_wd(); have_wd = true; }
      if (wave == 0) {
        for (unsigned spins = 0;;) {
          MEMBAR();
          const unsigned v = __builtin_amdgcn_raw_buffer_load_b32(rws, lane == 0 ? WS_YP + (((f & 1) * 8 + (f & 7)) * DM + (f % 18) * 32) * 4 : 0x7ffffff0, 0, AUX_SC1);
          if (__builtin_amdgcn_readfirstlane(v) != SENT) break;
          if (give_up(spins, L)) break;
          __builtin_amdgcn_s_sleep(K_YSL);
        }
        if (lane == 0) stw(L, C_Y, 1u);
      } else lds_wait(L, C_Y, 1);
      STAMP(16);
      v4u y0[3], y1[3];
      for (unsigned spins = 0;;) {
        MEMBAR();
        unsigned mx = 0;
#pragma unroll
        for (int t = 0; t < 3; ++t) {
          const int o = act ? WS_YP + (b * DM + 4 * (lane + 48 * t)) * 4 : 0x7ffffff0;
          y0[t] = ldsc1(rws, o); y1[t] = ldsc1(rws, act ? o + NB * DM * 4 : o);
        }
#pragma unroll
        for (int t = 0; t < 3; ++t) mx = umax(mx, umax(max4(y0[t]), max4(y1[t])));
        if (__all(mx != SENT)) break;
        if (give_up(spins, L)) break;
      }
      STAMP(17);
      float x1[12];
#pragma unroll
      for (int e = 0; e < 12; ++e) x1[e] = __builtin_fmaf(gf[e], asf(y0[e >> 2][e & 3]) + asf(y1[e >> 2][e & 3]), xf[e]);
      const float sh = asf(__builtin_amdgcn_readfirstlane(asu(x1[0])));
      float s1 = 0.f, s2 = 0.f;
#pragma unroll
      for (int e = 0; e < 12; ++e) { const float d = x1[e] - sh; s1 += d; s2 = __builtin_fmaf(d, d, s2); }
      s1 = act ? s1 : 0.f; s2 = act ? s2 : 0.f;
      s1 += dpp<ROR1>(s1); s2 += dpp<ROR1>(s2); s1 += dpp<ROR2>(s1); s2 += dpp<ROR2>(s2);
      s1 += dpp<ROR4>(s1); s2 += dpp<ROR4>(s2); s1 += dpp<ROR8>(s1); s2 += dpp<ROR8>(s2);
      s1 = x16sum(s1); s2 = x16sum(s2); s1 = x32sum(s1); s2 = x32sum(s2);
      const float dm = s1 * (1.f / DM), var = fmaxf(__builtin_fmaf(-dm, dm, s2 * (1.f / DM)), 0.f);
      const float rstd = __builtin_amdgcn_rsqf(var + 1e-5f), mo = -(sh + dm) * rstd;
#pragma unroll
      for (int t = 0; t < 3; ++t) {
        float hv[4];
#pragma unroll
        for (int e = 0; e < 4; ++e) hv[e] = __builtin_fmaf(__builtin_fmaf(x1[4 * t + e], rstd, mo), cf[4 * t + e], sf[4 * t + e]);
        if (act) {
          *(LAS v2u*)(L + L_H2 + b * XSTR + 8 * (lane + 48 * t)) = v2u{pk(hv[0], hv[1]), pk(hv[2], hv[3])};
          const int col = 4 * (lane + 48 * t) - rc0;
          if (isR && b == rb_ && col >= 0 && col < 64) *(LAS f4*)(L + L_X1S + col * 4) = f4{x1[4 * t], x1[4 * t + 1], x1[4 * t + 2], x1[4 * t + 3]};
        }
      }
    } else {
      lds_wait(L, C_DMA1, 4);
#pragma unroll
      for (int k6 = 0; k6 < 6; ++k6) wf[k6] = trfrag(L, ((gt >> 1) ? L_WU : L_W) + (32 * (kt * 6 + k6) + 8 * g + q4) * 64 + ((gt & 1) ^ (g & 1)) * 32 + p4 * 8, 4 * 64);
      lds_wait(L, C_Y, 1);
      if (__builtin_amdgcn_readfirstlane(ldsw(L, C_DMA2)) >= 4u) { read_wd(); have_wd = true; }
    }
    RAWBAR();
    STAMP(18);
    {
      f4 D0 = {0.f, 0.f, 0.f, 0.f}, D1 = {0.f, 0.f, 0.f, 0.f};
#pragma unroll
      for (int k6 = 0; k6 < 6; k6 += 2) {
        D0 = mfma(wf[k6], lds128(L, L_H2 + (lane & 7) * XSTR + (32 * (kt * 6 + k6) + 8 * g) * 2), D0);
        D1 = mfma(wf[k6 + 1], lds128(L, L_H2 + (lane & 7) * XSTR + (32 * (kt * 6 + k6 + 1) + 8 * g) * 2), D1);
      }
      if ((lane & 15) < 8) *(LAS f4*)(L + L_GU + (((kt * 4 + gt) * 4 + g) * 8 + (lane & 7)) * 16) = D0 + D1;
    }
    RAWBAR();
    STAMP(7);
    if (!have_wd && wave >= 4) read_wd();
    if (tid < 256) {
      const int b = tid & 7, fc = tid >> 3;
      const int o = (((fc >> 4) * 4 + ((fc >> 2) & 3)) * 8 + b) * 16 + (fc & 3) * 4;
      float gs = 0.f, us = 0.f;
#pragma unroll
      for (int k3 = 0; k3 < 3; ++k3) { gs += *(LAS float*)(L + L_GU + k3 * 2048 + o); us += *(LAS float*)(L + L_GU + k3 * 2048 + 1024 + o); }
      const float a = rb(gelu_t(rb(gs))) * rb(us);
      *(LAS u16*)(L + L_ACT + b * ASTR + fc * 2) = (u16)pk(a, a);
      if (!have_wd && wave == 3) read_wd();
    }
    RAWBAR();
    STAMP(31);
    if (wave >= 3) {
      const v4u af = lds128(L, L_ACT + (lane & 7) * ASTR + g * 16);
      const int b = lane & 7, hi = (lane >> 3) & 1;
      const f4 z4 = {0.f, 0.f, 0.f, 0.f};
      const f4 T0 = mfma(wd[0], af, z4), T1 = mfma(wd[1], af, z4), T2 = mfma(wd[2], af, z4), T3 = mfma(wd[3], af, z4);
      const f4 Da = hi ? T2 : T0, Db = hi ? T3 : T1;
      stsc1(san4(v4u{pk(Da.x, Da.y), pk(Da.z, Da.w), pk(Db.x, Db.y), pk(Db.z, Db.w)}), rws, WS_DP + ((b * 9 + dw) * 144 + f) * 128 + (hi * 4 + g) * 16);
    }
    STAMP(19);
    if (isR) {
      v4u a, b2;
      const int o = WS_DP + f * 144 * 128 + tid * 16;
      for (unsigned spins = 0;;) {
        MEMBAR();
        a = ldsc1(rws, o); b2 = ldsc1(rws, tid < 384 ? o + 768 * 16 : 0x7ffffff0);
        if (__all(umax(max4(a), max4(b2)) != SENT)) break;
        if (give_up(spins, L)) break;
      }
      STAMP(20);
      float s[8];
#pragma unroll
      for (int e = 0; e < 4; ++e) { s[2 * e] = bflo(a[e]) + bflo(b2[e]); s[2 * e + 1] = bfhi(a[e]) + bfhi(b2[e]); }
#pragma unroll
      for (int e = 0; e < 8; ++e) { float v = s[e]; v += dpp<ROR8>(v); v = x32sum(x16sum(v)); s[e] = v; }
      if (lane < 8) {
        *(LAS f4*)(L + L_RED2 + (wave * 8 + lane) * 32) = f4{s[0], s[1], s[2], s[3]};
        *(LAS f4*)(L + L_RED2 + (wave * 8 + lane) * 32 + 16) = f4{s[4], s[5], s[6], s[7]};
      }
      RAWBAR();
    }
    if (isR && wave == 0) {
      const int ro = (((lane >> 5) * 4 + ((lane >> 2) & 3)) * 8 + ((lane >> 4) & 1) * 4 + (lane & 3)) * 4;
      float dn = 0.f;
#pragma unroll
      for (int w = 0; w < 12; ++w) dn += *(LAS float*)(L + L_RED2 + w * 256 + ro);
      const float gv = (lane & 1) ? bfhi(gbw) : bflo(gbw);
      const float o = *(LAS float*)(L + L_X1S + lane * 4) + rb(gv * rb(dn));
      const unsigned two = pk(o, dpp<QX1>(o));
      if (!(lane & 1)) *(GAS unsigned*)(p.out + rb_ * DM + rc0 + lane) = two;
      if (wg == 0 && lane == 0) {
        const u64 E = ((u64)ldsw(L, C_E1) << 32) | ldsw(L, C_E0);
        __hip_atomic_store((GAS u64*)(p.ws + WS_EPOCH), E + 1, __ATOMIC_RELAXED, __HIP_MEMORY_SCOPE_AGENT);
      }
      STAMP(21);
    }
  }
}

}

extern "C" void kernel_launch(void* const* d_in, const int* in_sizes, int n_in, void* d_out, int out_size, void* d_ws, size_t ws_size, hipStream_t stream) {
  ak::Params p;
  p.x = (const ak::u16*)d_in[0]; p.K = (const ak::u16*)d_in[1]; p.V = (const ak::u16*)d_in[2];
  p.wq = (const ak::u16*)d_in[3]; p.wo = (const ak::u16*)d_in[4];
  p.wg = (const ak::u16*)d_in[6]; p.wu = (const ak::u16*)d_in[7]; p.wd = (const ak::u16*)d_in[8];
  p.c = (const ak::u16*)d_in[9]; p.wm = (const ak::u16*)d_in[10];
  p.out = (ak::u16*)d_out; p.ws = (unsigned char*)d_ws;
  (void)hipFuncSetAttribute((const void*)ak::fused_kernel, hipFuncAttributeMaxDynamicSharedMemorySize, ak::L_END);
  ak::fused_kernel<<<ak::NWG, ak::NTHR, ak::L_END, stream>>>(p);
}
```

```cpp
#include <hip/hip_runtime.h>
#include <stdint.h>

namespace ak {

typedef unsigned short u16;
typedef unsigned long long u64;
typedef unsigned v4u __attribute__((ext_vector_type(4)));
typedef unsigned v2u __attribute__((ext_vector_type(2)));
typedef float f4 __attribute__((ext_vector_type(4)));
typedef float f2 __attribute__((ext_vector_type(2)));
typedef __bf16 bf8 __attribute__((ext_vector_type(8)));
typedef __bf16 bf2 __attribute__((ext_vector_type(2)));
typedef short s4v __attribute__((ext_vector_type(4)));
#define LAS __attribute__((address_space(3)))
#define GAS __attribute__((address_space(1)))
typedef __amdgpu_buffer_rsrc_t rsrc_t;

#define K_R 3
#define K_RTAIL 6
#define K_TTAIL 24
#define K_NP 2
#define K_PF 4
#define K_TRIG_WO 8
#define K_TRIG_FFN 33
#define K_NL 2
#define K_FB 0
#define K_FB1 2
#define K_FB2 6
#define K_FBW 1
#define K_FBPER 6
#define K_PRIO 0
#define K_WOPACE 40
#define K_WNT 1
#define K_FFNDELAY 0
#define K_WOEARLY 32
#define K_FFNEARLY 0
#define K_FPACE 60
#define K_PSL 1
#define K_WNEAR 15
#define K_WSL 1
#define K_FFNTRIG 0
#define K_H1NEAR 6
#define K_H2NEAR 12
#define K_X 0
#define K_YSL 1
#define K_WSL2 24
#define K_DMAR 5
#define K_PFN 12
#define K_PF2 3
#define K_DLATE 0
#define K_RPAT 22224444
#define K_MHOLD 1
#define K_STAMPS 0

constexpr int NB = 8, DM = 576, DFF = 4608, NMOD = 3456, NQ = 4096;
constexpr int NWG = 256, NTHR = 768;
constexpr int NTILE = 512;
constexpr int NP = K_NP, P = NP * 8;
__host__ __device__ constexpr unsigned rpat(int k) { unsigned v = K_RPAT; for (int i = 7; i > k; --i) v /= 10; return v % 10; }
constexpr float LOG2E = 1.4426950408889634f;
constexpr float SCALE = 0.08838834764831845f;
constexpr unsigned SENT = 0xFFFFFFFFu;
constexpr int AUX_NT = 2, AUX_SC1 = 16;
constexpr unsigned SPIN_CAP = 1u << 15, LDS_CAP = 1u << 18;

constexpr int WS_EPOCH = 0, WS_PRG = 1024, WS_CLR = 4096, WS_CLR2 = 6144;
constexpr int WS_MOD = 8192;
constexpr int WS_QP = 65536;
constexpr int WS_OP = WS_QP + 3 * NB * NQ * 4;
constexpr int WS_ML = WS_OP + NWG * 512;
constexpr int WS_YP = WS_ML + NWG * 16;
constexpr int WS_DP = WS_YP + 2 * NB * DM * 4;
constexpr int WS_END = WS_DP + 72 * 144 * 128;
constexpr size_t WS_STAMP = 64u << 20;
constexpr int NSTAMP = 32;

constexpr int L_W = 0;
constexpr int L_SIMG = 73728;
constexpr int L_HIMG = 24576;
constexpr int HSTR = 400;
constexpr int L_WU = 36864, L_WD = 73728;
constexpr int L_GU = 110592;
constexpr int L_ACT = L_GU + 6144;
constexpr int ASTR = 80;
constexpr int L_RED2 = L_ACT + 640;
constexpr int L_X1S = L_RED2 + 3072;
constexpr int L_H2 = L_X1S + 256;
constexpr int XSTR = 1168;
static_assert(L_H2 + 8 * XSTR <= 131072, "ffn lds");
constexpr int L_MRG = 131072;
constexpr int L_RED = L_MRG + 8448;
constexpr int L_KV = 28672, L_KVM = 83968;
constexpr int L_Q = 160128;
constexpr int L_CTL = L_Q + 512;
constexpr int L_STAMP = L_CTL + 128;
constexpr int L_END = L_STAMP + (K_STAMPS ? 12 * 32 * 8 : 0);
static_assert(L_KV + K_NL * 65536 <= L_Q && L_KVM + 65536 <= L_Q && L_END <= 163840, "lds map");
enum { C_SYNC1 = 0, C_HS = 1, C_TRIG = 2, C_MRG = 3, C_DEAD = 4, C_DMA0 = 5, C_DMA1 = 6, C_HS2 = 7, C_KVL = 10, C_Y = 11, C_DMA2 = 12, C_E0 = 8, C_E1 = 9 };

__host__ __device__ constexpr bool role_w(int wg) { return wg >= 144 && wg < 192 && (wg & 7) + 8 * (((wg >> 3) - 18) >> 1) < 18; }
#define K_WT_MC 110
#define K_WT_ML 35
#define K_WT_W 100
#define K_WT_F 30
#define K_WT_N 10
#define K_WT_Q 25
constexpr int FKB = K_FFNEARLY == 1 ? 110 : (K_FFNEARLY == 2 ? 37 : 0);
constexpr int role_kb(int wg) { return wg < 18 ? K_WT_MC + FKB : wg < 54 ? K_WT_ML + FKB : role_w(wg) ? K_WT_W : wg < 144 ? K_WT_F + FKB : wg < 246 ? K_WT_Q : K_WT_N; }
struct SplitTab { unsigned v[256]; };
constexpr SplitTab make_split() {
  SplitTab t{};
  int o[256] = {};
  for (int i = 0; i < 256; ++i) o[i] = i;
  for (int i = 1; i < 256; ++i) {
    const int x = o[i]; int j = i - 1;
    while (j >= 0 && role_kb(o[j]) < role_kb(x)) { o[j + 1] = o[j]; --j; }
    o[j + 1] = x;
  }
  for (int h = 0; h < 128; ++h) {
    const int a = o[h], b = o[255 - h];
    const int n0 = 256 - (role_kb(a) - role_kb(b) + 8) / 16;
    t.v[a] = (unsigned)(h | (n0 << 8));
    t.v[b] = (unsigned)(h | 128 | (n0 << 8));
  }
  return t;
}
__device__ constexpr SplitTab SPLIT = make_split();

struct Params {
  const u16 *x, *K, *V, *wq, *wo, *wg, *wu, *wd, *c, *wm;
  u16* out;
  unsigned char* ws;
};

__device__ __forceinline__ float asf(unsigned u) { return __builtin_bit_cast(float, u); }
__device__ __forceinline__ unsigned asu(float f) { return __builtin_bit_cast(unsigned, f); }
__device__ __forceinline__ float bflo(unsigned u) { return asf(u << 16); }
__device__ __forceinline__ float bfhi(unsigned u) { return asf(u & 0xffff0000u); }
__device__ __forceinline__ unsigned pk(float a, float b) {
  f2 v = {a, b};
  return __builtin_bit_cast(unsigned, __builtin_convertvector(v, bf2));
}
__device__ __forceinline__ float rb(float a) { return bflo(pk(a, a)); }
__device__ __forceinline__ unsigned san(unsigned u) { return u < 0xFFFFFFFEu ? u : 0xFFFFFFFEu; }
__device__ __forceinline__ v4u san4(v4u v) { return v4u{san(v.x), san(v.y), san(v.z), san(v.w)}; }
__device__ __forceinline__ unsigned umax(unsigned a, unsigned b) { return a > b ? a : b; }
__device__ __forceinline__ unsigned max4(v4u v) { return umax(umax(v.x, v.y), umax(v.z, v.w)); }
template <int C> __device__ __forceinline__ float dpp(float x) {
  return asf((unsigned)__builtin_amdgcn_mov_dpp((int)asu(x), C, 0xf, 0xf, true));
}
template <int C> __device__ __forceinline__ unsigned dppu(unsigned x) { return (unsigned)__builtin_amdgcn_mov_dpp((int)x, C, 0xf, 0xf, true); }
constexpr int ROR1 = 0x121, ROR2 = 0x122, ROR4 = 0x124, ROR8 = 0x128, QX1 = 0xB1, QX2 = 0x4E;
__device__ __forceinline__ float x32max(float x) { auto t = __builtin_amdgcn_permlane32_swap(asu(x), asu(x), false, false); return fmaxf(asf(t[0]), asf(t[1])); }
__device__ __forceinline__ float x32sum(float x) { auto t = __builtin_amdgcn_permlane32_swap(asu(x), asu(x), false, false); return asf(t[0]) + asf(t[1]); }
__device__ __forceinline__ float x16sum(float x) { auto t = __builtin_amdgcn_permlane16_swap(asu(x), asu(x), false, false); return asf(t[0]) + asf(t[1]); }
__device__ __forceinline__ float row16sum(float x) { x += dpp<ROR1>(x); x += dpp<ROR2>(x); x += dpp<ROR4>(x); x += dpp<ROR8>(x); return x; }
__device__ __forceinline__ f4 mfma(v4u a, v4u b, f4 c) {
  return __builtin_amdgcn_mfma_f32_16x16x32_bf16(__builtin_bit_cast(bf8, a), __builtin_bit_cast(bf8, b), c, 0, 0, 0);
}
__device__ __forceinline__ rsrc_t mkr(const void* p, unsigned bytes) { return __builtin_amdgcn_make_buffer_rsrc((void*)p, 0, bytes, 0x00020000); }
__device__ __forceinline__ v4u ldsc1(rsrc_t r, int off) { return __builtin_amdgcn_raw_buffer_load_b128(r, off, 0, AUX_SC1); }
__device__ __forceinline__ v2u ldsc1_2(rsrc_t r, int off) { return __builtin_amdgcn_raw_buffer_load_b64(r, off, 0, AUX_SC1); }
__device__ __forceinline__ void stsc1(v4u v, rsrc_t r, int off) { __builtin_amdgcn_raw_buffer_store_b128(v, r, off, 0, AUX_SC1); }
__device__ __forceinline__ void stsc1_2(v2u v, rsrc_t r, int off) { __builtin_amdgcn_raw_buffer_store_b64(v, r, off, 0, AUX_SC1); }
#define MEMBAR() asm volatile("" ::: "memory")
#define VMWAIT(n) asm volatile("s_waitcnt vmcnt(" #n ")" ::: "memory")
#define LGWAIT() asm volatile("s_waitcnt lgkmcnt(0)" ::: "memory")
#define RAWBAR() asm volatile("s_waitcnt lgkmcnt(0)\n\ts_barrier" ::: "memory")
template <bool NT = true> __device__ __forceinline__ void dma16(rsrc_t r, int voff, unsigned soff, unsigned ldsa) {
  unsigned keep;
  if (NT) asm volatile("s_mov_b32 %0, m0\n\ts_mov_b32 m0, %1\n\ts_nop 0\n\tbuffer_load_dwordx4 %2, %3, %4 offen nt lds\n\ts_mov_b32 m0, %0" : "=&s"(keep) : "s"(ldsa), "v"(voff), "s"(r), "s"(soff) : "memory");
  else asm volatile("s_mov_b32 %0, m0\n\ts_mov_b32 m0, %1\n\ts_nop 0\n\tbuffer_load_dwordx4 %2, %3, %4 offen lds\n\ts_mov_b32 m0, %0" : "=&s"(keep) : "s"(ldsa), "v"(voff), "s"(r), "s"(soff) : "memory");
}
__device__ __forceinline__ v4u ld_hidden(rsrc_t r, int voff) {
  v4u v;
  asm volatile("buffer_load_dwordx4 %0, %1, %2, 0 offen" : "=&v"(v) : "v"(voff), "s"(r) : "memory");
  return v;
}
__device__ __forceinline__ v4u lds128(LAS unsigned char* L, int off) { return *(LAS v4u*)(L + off); }
__device__ __forceinline__ v4u trfrag(LAS unsigned char* L, int a0, int d) {
  s4v lo = __builtin_amdgcn_ds_read_tr16_b64_v4i16((LAS s4v*)(L + a0));
  s4v hi = __builtin_amdgcn_ds_read_tr16_b64_v4i16((LAS s4v*)(L + a0 + d));
  v2u l = __builtin_bit_cast(v2u, lo), h = __builtin_bit_cast(v2u, hi);
  return v4u{l.x, l.y, h.x, h.y};
}
__device__ __forceinline__ unsigned ldsw(LAS unsigned char* L, int w) { return *(volatile LAS unsigned*)(L + L_CTL + 4 * w); }
__device__ __forceinline__ void stw(LAS unsigned char* L, int w, unsigned v) { *(volatile LAS unsigned*)(L + L_CTL + 4 * w) = v; }
__device__ __forceinline__ unsigned lds_add(LAS unsigned char* L, int w) {
  unsigned o = 0;
  if ((threadIdx.x & 63) == 0) o = __hip_atomic_fetch_add((LAS unsigned*)(L + L_CTL + 4 * w), 1u, __ATOMIC_RELAXED, __HIP_MEMORY_SCOPE_WORKGROUP);
  return __builtin_amdgcn_readfirstlane(o);
}
template <int SLP = 1> __device__ __forceinline__ void lds_wait(LAS unsigned char* L, int w, unsigned n) {
  for (unsigned spins = 0; __builtin_amdgcn_readfirstlane(ldsw(L, w)) < n; ++spins) {
    if (__builtin_amdgcn_readfirstlane(ldsw(L, C_DEAD))) break;
    if (spins > LDS_CAP) { stw(L, C_DEAD, 1u); break; }
    __builtin_amdgcn_s_sleep(SLP);
  }
  MEMBAR();
}
__device__ __forceinline__ bool give_up(unsigned& spins, LAS unsigned char* L) {
  if (++spins > SPIN_CAP) { stw(L, C_DEAD, 1u); return true; }
  return (spins & 15u) == 0u && __builtin_amdgcn_readfirstlane(ldsw(L, C_DEAD)) != 0u;
}
__device__ __forceinline__ u64 mix64(u64 z) {
  z = (z ^ (z >> 30)) * 0xBF58476D1CE4E5B9ull;
  z = (z ^ (z >> 27)) * 0x94D049BB133111EBull;
  return z ^ (z >> 31);
}
#define STAMP(k) do {} while (0)
__device__ __forceinline__ void throttle(unsigned r) {
  asm volatile("s_cmp_gt_u32 %0, 6\n\ts_cbranch_scc1 9f\n\ts_waitcnt vmcnt(5)\n\t"
               "s_cmp_gt_u32 %0, 5\n\ts_cbranch_scc1 9f\n\ts_waitcnt vmcnt(4)\n\t"
               "s_cmp_gt_u32 %0, 4\n\ts_cbranch_scc1 9f\n\ts_waitcnt vmcnt(3)\n\t"
               "s_cmp_gt_u32 %0, 3\n\ts_cbranch_scc1 9f\n\ts_waitcnt vmcnt(2)\n\t"
               "s_cmp_gt_u32 %0, 2\n\ts_cbranch_scc1 9f\n\ts_waitcnt vmcnt(1)\n\t"
               "s_cmp_gt_u32 %0, 1\n\ts_cbranch_scc1 9f\n\ts_waitcnt vmcnt(0)\n9:" ::"s"(r) : "scc", "memory");
}

__device__ __forceinline__ float sigm(float z) { return __builtin_amdgcn_rcpf(1.f + __builtin_amdgcn_exp2f(-LOG2E * z)); }
__device__ __forceinline__ float silu_acc(float a) {
  const float hi = 1.44269502162933349609375f, lo = 1.92596299112661746e-8f;
  const float t0 = -a * hi;
  const float r = __builtin_fmaf(-a, lo, __builtin_fmaf(-a, hi, -t0));
  const float e0 = __builtin_amdgcn_exp2f(fminf(t0, 126.f));
  const float d = 1.f + __builtin_fmaf(e0, r * 0.693147180559945f, e0);
  const float rc = __builtin_amdgcn_rcpf(d), q = a * rc;
  return __builtin_fmaf(__builtin_fmaf(-q, d, a), rc, q);
}
__device__ __forceinline__ float gelu_t(float x) { const float z = 0.7978845608028654f * (x + 0.044715f * x * x * x); return x * sigm(2.f * z); }

struct Soft { float m, l; f4 acc; };
struct Lane { v4u qa0, qa1; unsigned sel[2][4]; int idx0; };

__device__ __forceinline__ float quadsum(f4 d) {
  float t = dpp<0x00>(d.x);
  t += dpp<0x55>(d.y); t += dpp<0xAA>(d.z); t += dpp<0xFF>(d.w);
  return t;
}
__device__ __forceinline__ void softmax_tile(f4 D0, f4 D1, Soft& s, const Lane& c, unsigned& val0, unsigned& val1) {
  const unsigned r1 = pk(quadsum(D0), quadsum(D1));
  const unsigned r2 = pk(bflo(r1) * SCALE, bfhi(r1) * SCALE);
  const float s0 = bflo(r2), s1 = bfhi(r2);
  float mx = fmaxf(s0, s1);
  mx = fmaxf(mx, dpp<ROR4>(mx)); mx = fmaxf(mx, dpp<ROR8>(mx)); mx = x32max(mx);
  const float mn = fmaxf(s.m, mx);
  const float al = __builtin_amdgcn_exp2f((s.m - mn) * LOG2E);
  const float p0 = __builtin_amdgcn_exp2f((s0 - mn) * LOG2E), p1 = __builtin_amdgcn_exp2f((s1 - mn) * LOG2E);
  s.m = mn; s.l = s.l * al + (p0 + p1);
  auto t = __builtin_amdgcn_permlane16_swap(asu(al), asu(al), false, false);
  const float al0 = asf(t[0]), al1 = asf(t[1]);
  s.acc.x *= al0; s.acc.y *= al0; s.acc.z *= al1; s.acc.w *= al1;
  const unsigned pp = pk(p0, p1);
  val0 = (unsigned)__builtin_amdgcn_ds_bpermute(c.idx0, (int)pp);
  val1 = (unsigned)__builtin_amdgcn_ds_bpermute(c.idx0 + 128, (int)pp);
}
template <int U> __device__ __forceinline__ v4u pfrag(unsigned val0, unsigned val1, const Lane& c) {
  const unsigned v = (U & 1) ? val1 : val0;
  constexpr int y = U >> 1;
  return v4u{__builtin_amdgcn_perm(v, v, c.sel[y][0]), __builtin_amdgcn_perm(v, v, c.sel[y][1]), __builtin_amdgcn_perm(v, v, c.sel[y][2]), __builtin_amdgcn_perm(v, v, c.sel[y][3])};
}

__global__ void __launch_bounds__(NTHR, 3) fused_kernel(Params p) {
  extern __shared__ __attribute__((aligned(16))) unsigned char smem[];
  LAS unsigned char* L = (LAS unsigned char*)smem;
  const int tid = threadIdx.x, lane = tid & 63, wave = __builtin_amdgcn_readfirstlane(tid >> 6);
  const int wg = blockIdx.x;
  const unsigned sp = SPLIT.v[wg];
  const unsigned ldsb = (unsigned)(size_t)L;
  STAMP(0);
  if (tid < 32) *(LAS unsigned*)(L + L_CTL + 4 * tid) = 0u;
  const rsrc_t rws = mkr(p.ws, WS_END);
  const int xl = wg & 7, xi = wg >> 3;
  const bool isM = wg < 54, isMc = wg < 18, isQ = wg >= 54 && wg < 246;
  const bool isF = wg < 144;
  const int f = xl * 18 + xi;
  const int wpair = xl + 8 * ((xi - 18) >> 1);
  const bool isW = wg >= 144 && wg < 192 && wpair < 18;
  const int wsig = wpair / 9, wng = 2 * (wpair % 9) + ((xi - 18) & 1);
  const int ahead = sp & 127, asl = (sp >> 7) & 1, an0 = sp >> 8;
  const int ab = ahead >> 4, akv = ahead & 15, aslot = ahead * 2 + asl;
  const int tbeg = asl ? an0 : 0, tend = asl ? NTILE : an0;
  const int qi = wg - 54, qcg = qi / 3, qkq = qi % 3;
  const int g = lane >> 4, q4 = (lane & 15) >> 2, p4 = lane & 3;

  if (wave < 8) {
    v2u E7 = {0u, 0u};
    if (wave == 7) {
      asm volatile("buffer_load_dwordx2 %0, %1, %2, 0 offen sc1" : "=&v"(E7) : "v"(WS_EPOCH), "s"(rws) : "memory");
      const v4u ss = {SENT, SENT, SENT, SENT};
      if (isM) stsc1(ss, rws, WS_MOD + ((lane >> 3) * NMOD + wg * 64) * 2 + (lane & 7) * 16);
      if (isQ) {
#pragma unroll
        for (int j = 0; j < 2; ++j) stsc1(ss, rws, WS_QP + (((qkq * 8 + 4 * j + (lane >> 4)) * NQ) + qcg * 64) * 4 + (lane & 15) * 16);
      }
      if (lane <= 32) stsc1(ss, rws, lane < 32 ? WS_OP + aslot * 512 + lane * 16 : WS_ML + aslot * 16);
      if (isW) stsc1(ss, rws, WS_YP + ((wsig * 8 + (lane >> 3)) * DM + wng * 32) * 4 + (lane & 7) * 16);
    }
    v4u cvm = {0u, 0u, 0u, 0u};
    if (isMc) cvm = ld_hidden(mkr(p.c, NB * DM * 2), tid * 16);
    if (isMc || isQ) {
      const int stride = isM ? NMOD * 2 : NQ * 2, n = isM ? 9 : 3;
      const rsrc_t rw = isM ? mkr(p.wm, DM * NMOD * 2) : mkr(p.wq, DM * NQ * 2);
      const int cb = isM ? wg * 128 : qcg * 128, r0 = isM ? 0 : qkq * 192;
      const int c16 = lane & 7;
#pragma unroll 1
      for (int j = 0; j < n; ++j) {
        const int i = wave * n + j;
        const int fz = ((lane >> 4) & 1) | ((i & 1) << 1);
        dma16(rw, (lane >> 3) * stride + ((((c16 >> 1) ^ fz) << 1) | (c16 & 1)) * 16 + cb, (r0 + i * 8) * stride, ldsb + L_W + i * 1024);
      }
    }
    auto say_cleared = [&]() {
      if (wave == 7) {
        const unsigned e0 = __builtin_amdgcn_readfirstlane(E7.x), e1 = __builtin_amdgcn_readfirstlane(E7.y);
        if (lane == 0) { __hip_atomic_store((GAS u64*)(p.ws + WS_CLR) + wg, mix64((((u64)e1 << 32) | e0) + 1), __ATOMIC_RELAXED, __HIP_MEMORY_SCOPE_AGENT); stw(L, C_E0, e0); stw(L, C_E1, e1); }
        LGWAIT();
        STAMP(22);
      }
    };
    if (isMc) {
      RAWBAR();
      asm volatile("s_waitcnt vmcnt(9)" : "+v"(cvm), "+v"(E7)::"memory");
      __builtin_amdgcn_sched_barrier(0);
      say_cleared();
      v4u o;
#pragma unroll
      for (int e = 0; e < 4; ++e) o[e] = pk(silu_acc(bflo(cvm[e])), silu_acc(bfhi(cvm[e])));
      *(LAS v4u*)(L + L_SIMG + (tid / 72) * XSTR + (tid % 72) * 16) = o;
      asm volatile("s_waitcnt vmcnt(0) lgkmcnt(0)" ::: "memory");
      lds_add(L, C_DMA0);
    }
    const unsigned char* kb = (const unsigned char*)p.K + ((size_t)ahead << 21);
    const unsigned char* vb = (const unsigned char*)p.V + ((size_t)ahead << 21);
    const int koff = ((lane >> 2) & 3) * 256 + (g * 4 + (lane & 3)) * 16, voff = lane * 16;
    const int tw = tbeg + wave;
#define KVT(tb, n) (tw + 8 * ((tb) + ((n) >> 3)))
#define KVLOAD(tb, n) (((n) & 4) ? __builtin_amdgcn_raw_buffer_load_b128(mkr(vb, KVT(tb, n) < tend ? (1u << 21) : 0u), voff, KVT(tb, n) * 4096 + ((n) & 3) * 1024, AUX_NT) \
                                 : __builtin_amdgcn_raw_buffer_load_b128(mkr(kb, KVT(tb, n) < tend ? (1u << 21) : 0u), koff, KVT(tb, n) * 4096 + ((n) & 3) * 1024, AUX_NT))
#define SB() __builtin_amdgcn_sched_barrier(0)
#define PIN(x) do { asm volatile("" : "+v"(x)); SB(); } while (0)
    v4u ring[8], pre[P];
#pragma unroll
    for (int n = 0; n < 8 + P; ++n) {
      if (n == K_PF && !isMc) { RAWBAR(); asm volatile("s_waitcnt vmcnt(%1)" : "+v"(E7) : "n"(K_PF) : "memory"); __builtin_amdgcn_sched_barrier(0); say_cleared(); lds_add(L, C_DMA0); }
      if (n >= K_PF) throttle(n < K_PFN ? K_PF : K_PF2);
      if (n < 8) ring[n] = KVLOAD(0, n); else pre[n - 8] = KVLOAD(0, n);
      SB();
    }
    const int nl = isMc ? (K_NL < 1 ? K_NL : 1) : K_NL;
    const int kvl = (isMc ? L_KVM : L_KV) + wave * (nl * 8192);
    {
      const rsrc_t rk = mkr(kb, 1 << 21), rv = mkr(vb, 1 << 21);
#pragma unroll 1
      for (int t = 0; t < nl; ++t) {
#pragma unroll
        for (int n = 0; n < 8; ++n) {
          throttle(K_PF2);
          dma16((n & 4) ? rv : rk, (n & 4) ? voff : koff, (tw + 8 * (NP + 1 + t)) * 4096 + (n & 3) * 1024, ldsb + kvl + t * 8192 + n * 1024);
        }
      }
    }
    const int tn = NP + 1 + nl;
    Lane c;
    {
      const int i = lane & 15, beta = i >> 2, pos = i & 1;
#pragma unroll
      for (int y = 0; y < 2; ++y)
#pragma unroll
        for (int w = 0; w < 4; ++w) {
          const unsigned b0 = 2 * y, b1 = 2 * y + 1;
          c.sel[y][w] = beta != w ? 0x0c0c0c0cu : (pos ? ((b1 << 24) | (b0 << 16) | 0x0c0cu) : (0x0c0c0000u | (b1 << 8) | b0));
        }
      c.idx0 = (16 * ((i >> 1) & 1) + 4 * g) * 4;
    }
    if (K_PRIO && wave >= 4) __builtin_amdgcn_s_setprio(1);
    STAMP(1);
    RAWBAR();
    STAMP(2);
    {
      const int i = lane & 15;
      const v4u qf = lds128(L, L_Q + ((i >> 2) & 1) * 256 + (4 * g + (i & 3)) * 16);
      const v4u z = {0u, 0u, 0u, 0u};
      c.qa0 = (i >> 3) ? z : qf;
      c.qa1 = (i >> 3) ? qf : z;
    }
    Soft st;
    st.m = -INFINITY; st.l = 0.f; st.acc = f4{0.f, 0.f, 0.f, 0.f};
    const f4 zero = {0.f, 0.f, 0.f, 0.f};
    const unsigned rr = K_R;
    {
      f4 D0 = mfma(c.qa0, ring[0], zero); PIN(D0); ring[0] = KVLOAD(tn, 0); SB();
      D0 = mfma(c.qa1, ring[1], D0);      PIN(D0); ring[1] = KVLOAD(tn, 1); SB();
      f4 D1 = mfma(c.qa0, ring[2], zero); PIN(D1); ring[2] = KVLOAD(tn, 2); SB();
      D1 = mfma(c.qa1, ring[3], D1);      PIN(D1); throttle(rr); ring[3] = KVLOAD(tn, 3); SB();
      unsigned v0, v1;
      softmax_tile(D0, D1, st, c, v0, v1);
      st.acc = mfma(pfrag<0>(v0, v1, c), ring[4], st.acc);
      st.acc = mfma(pfrag<1>(v0, v1, c), ring[5], st.acc);
      st.acc = mfma(pfrag<2>(v0, v1, c), ring[6], st.acc);
      st.acc = mfma(pfrag<3>(v0, v1, c), ring[7], st.acc);
      SB();
#pragma unroll
      for (int t = 0; t < NP; ++t) {
        D0 = mfma(c.qa0, pre[8 * t + 0], zero); D0 = mfma(c.qa1, pre[8 * t + 1], D0);
        D1 = mfma(c.qa0, pre[8 * t + 2], zero); D1 = mfma(c.qa1, pre[8 * t + 3], D1);
        softmax_tile(D0, D1, st, c, v0, v1);
        st.acc = mfma(pfrag<0>(v0, v1, c), pre[8 * t + 4], st.acc);
        st.acc = mfma(pfrag<1>(v0, v1, c), pre[8 * t + 5], st.acc);
        st.acc = mfma(pfrag<2>(v0, v1, c), pre[8 * t + 6], st.acc);
        st.acc = mfma(pfrag<3>(v0, v1, c), pre[8 * t + 7], st.acc);
        SB();
      }
      VMWAIT(4);
#pragma unroll 1
      for (int t = 0; t < nl; ++t) {
        const int a = kvl + t * 8192 + lane * 16;
        D0 = mfma(c.qa0, lds128(L, a), zero); D0 = mfma(c.qa1, lds128(L, a + 1024), D0);
        D1 = mfma(c.qa0, lds128(L, a + 2048), zero); D1 = mfma(c.qa1, lds128(L, a + 3072), D1);
        softmax_tile(D0, D1, st, c, v0, v1);
        st.acc = mfma(pfrag<0>(v0, v1, c), lds128(L, a + 4096), st.acc);
        st.acc = mfma(pfrag<1>(v0, v1, c), lds128(L, a + 5120), st.acc);
        st.acc = mfma(pfrag<2>(v0, v1, c), lds128(L, a + 6144), st.acc);
        st.acc = mfma(pfrag<3>(v0, v1, c), lds128(L, a + 7168), st.acc);
      }
      LGWAIT();
      lds_add(L, C_KVL);
      throttle(rr); ring[4] = KVLOAD(tn, 4); SB();
      throttle(rr); ring[5] = KVLOAD(tn, 5); SB();
      throttle(rr); ring[6] = KVLOAD(tn, 6); SB();
      throttle(rr); ring[7] = KVLOAD(tn, 7); SB();
    }
#pragma unroll 1
    for (int t0 = tn; tw + 8 * t0 < tend; ++t0) {
      if (t0 == K_TRIG_WO && lane == 0 && wave == 0) stw(L, C_TRIG, 1u);
      const bool tail = tw + 8 * t0 + K_TTAIL >= tend;
      f4 D0 = mfma(c.qa0, ring[0], zero); PIN(D0); throttle(tail ? K_RTAIL : rpat(0)); ring[0] = KVLOAD(t0 + 1, 0); SB();
      D0 = mfma(c.qa1, ring[1], D0);      PIN(D0); throttle(tail ? K_RTAIL : rpat(1)); ring[1] = KVLOAD(t0 + 1, 1); SB();
      f4 D1 = mfma(c.qa0, ring[2], zero); PIN(D1); throttle(tail ? K_RTAIL : rpat(2)); ring[2] = KVLOAD(t0 + 1, 2); SB();
      D1 = mfma(c.qa1, ring[3], D1);      PIN(D1); throttle(tail ? K_RTAIL : rpat(3)); ring[3] = KVLOAD(t0 + 1, 3); SB();
      unsigned v0, v1;
      softmax_tile(D0, D1, st, c, v0, v1);
      st.acc = mfma(pfrag<0>(v0, v1, c), ring[4], st.acc); PIN(st.acc); throttle(tail ? K_RTAIL : rpat(4)); ring[4] = KVLOAD(t0 + 1, 4); SB();
      st.acc = mfma(pfrag<1>(v0, v1, c), ring[5], st.acc); PIN(st.acc); throttle(tail ? K_RTAIL : rpat(5)); ring[5] = KVLOAD(t0 + 1, 5); SB();
      st.acc = mfma(pfrag<2>(v0, v1, c), ring[6], st.acc); PIN(st.acc); throttle(tail ? K_RTAIL : rpat(6)); ring[6] = KVLOAD(t0 + 1, 6); SB();
      st.acc = mfma(pfrag<3>(v0, v1, c), ring[7], st.acc); PIN(st.acc); throttle(tail ? K_RTAIL : rpat(7)); ring[7] = KVLOAD(t0 + 1, 7); SB();
    }
#undef KVLOAD
    if (lds_add(L, 18) == 7u && lane == 0) stw(L, C_TRIG, 2u);
    STAMP(3);
    float l = st.l;
    l += dpp<ROR4>(l); l += dpp<ROR8>(l); l = x32sum(l);
    LAS float* mg = (LAS float*)(L + L_MRG) + wave * 264;
    const int cc = lane & 15;
    *(LAS f2*)(mg + 8 + 8 * cc + 2 * g) = f2{st.acc.x, st.acc.y};
    *(LAS f2*)(mg + 8 + 128 + 8 * cc + 2 * g) = f2{st.acc.z, st.acc.w};
    if (lane == 0 || lane == 16) *(LAS f2*)(mg + 2 * g) = f2{st.m, l};
    LGWAIT();
    if (lds_add(L, C_MRG) == 7u) {
      MEMBAR();
      const int h = lane >> 5, d0 = 4 * (lane & 31);
      LAS float* m0 = (LAS float*)(L + L_MRG);
      f2 ml[8]; f4 a[8];
#pragma unroll
      for (int w = 0; w < 8; ++w) { ml[w] = *(LAS f2*)(m0 + w * 264 + 2 * h); a[w] = *(LAS f4*)(m0 + w * 264 + 8 + 128 * h + d0); }
      float M = ml[0].x;
#pragma unroll
      for (int w = 1; w < 8; ++w) M = fmaxf(M, ml[w].x);
      float Ls = 0.f; f4 O = {0.f, 0.f, 0.f, 0.f};
#pragma unroll
      for (int w = 0; w < 8; ++w) { const float e = __builtin_amdgcn_exp2f((ml[w].x - M) * LOG2E); Ls += e * ml[w].y; O += e * a[w]; }
      const float inv = __builtin_amdgcn_rcpf(Ls);
      const unsigned o0 = pk(O.x * inv, O.y * inv), o1 = pk(O.z * inv, O.w * inv);
      const unsigned n0 = dppu<QX1>(o0), n1 = dppu<QX1>(o1);
      if (!(lane & 1)) stsc1(san4(v4u{o0, o1, n0, n1}), rws, WS_OP + aslot * 512 + (h * 128 + d0) * 2);
      const unsigned M1 = __builtin_amdgcn_readlane(asu(M), 32), L1 = __builtin_amdgcn_readlane(asu(Ls), 32);
      if (lane == 0) stsc1(san4(v4u{asu(M), asu(Ls), M1, L1}), rws, WS_ML + aslot * 16);
      STAMP(4);
    }
  } else {
    RAWBAR();
    const int sw = wave - 8, stid = tid - 512;
    const u64 Es = __hip_atomic_load((GAS u64*)(p.ws + WS_EPOCH), __ATOMIC_RELAXED, __HIP_MEMORY_SCOPE_AGENT);
    const int fz = (q4 >> 1) | ((g & 1) << 1);
    auto mod_mfma = [&]() {
      STAMP(5);
      f4 D[3] = {{0.f, 0.f, 0.f, 0.f}, {0.f, 0.f, 0.f, 0.f}, {0.f, 0.f, 0.f, 0.f}};
#pragma unroll
      for (int ks = 0; ks < 18; ++ks) {
        const v4u wf = trfrag(L, L_W + (32 * ks + 8 * g + q4) * 128 + (sw ^ fz) * 32 + p4 * 8, 4 * 128);
        const v4u sf = lds128(L, L_SIMG + (lane & 7) * XSTR + (32 * ks + 8 * g) * 2);
        D[ks % 3] = mfma(wf, sf, D[ks % 3]);
      }
      const f4 Ds = (D[0] + D[1]) + D[2];
      if ((lane & 15) < 8) stsc1_2(v2u{san(pk(Ds.x, Ds.y)), san(pk(Ds.z, Ds.w))}, rws, WS_MOD + ((lane & 7) * NMOD + wg * 64 + sw * 16 + 4 * g) * 2);
      STAMP(6);
    };
    const u64 tagv = mix64((((u64)__builtin_amdgcn_readfirstlane((unsigned)(Es >> 32)) << 32) | (unsigned)__builtin_amdgcn_readfirstlane((unsigned)Es)) + 1);
    const unsigned tg0 = (unsigned)tagv, tg1 = (unsigned)(tagv >> 32);
    if (isMc) {
      if (sw == 0) {
        const v4u cv = __builtin_amdgcn_raw_buffer_load_b128(mkr(p.c, NB * DM * 2), tid * 16, 0, 0);
        v4u o;
#pragma unroll
        for (int e = 0; e < 4; ++e) o[e] = pk(silu_acc(bflo(cv[e])), silu_acc(bfhi(cv[e])));
        *(LAS v4u*)(L + L_SIMG + (tid / 72) * XSTR + (tid % 72) * 16) = o;
        LGWAIT();
        lds_add(L, C_SYNC1);
      }
      STAMP(23);
      lds_wait(L, C_SYNC1, 1); lds_wait(L, C_DMA0, 8);
      mod_mfma();
    } else if (isQ) {
      const int qb = 2 * sw + (lane >> 5), qj = lane & 31;
      const bool qact = qj < 24;
      const rsrc_t rx = mkr(p.x, NB * DM * 2);
      v4u xv[3];
#pragma unroll
      for (int t = 0; t < 3; ++t) xv[t] = __builtin_amdgcn_raw_buffer_load_b128(rx, qact ? (qb * DM + 192 * t + 8 * qj) * 2 : 0x7ffffff0, 0, 0);
      float xf[3][8];
      float sum = 0.f;
#pragma unroll
      for (int t = 0; t < 3; ++t)
#pragma unroll
        for (int e = 0; e < 4; ++e) { xf[t][2 * e] = bflo(xv[t][e]); xf[t][2 * e + 1] = bfhi(xv[t][e]); sum += xf[t][2 * e] + xf[t][2 * e + 1]; }
      sum = x16sum(row16sum(sum));
      const float mean = sum * (1.f / DM);
      float vs = 0.f;
#pragma unroll
      for (int t = 0; t < 3; ++t)
#pragma unroll
        for (int e = 0; e < 8; ++e) { const float d = xf[t][e] - mean; vs += qact ? d * d : 0.f; }
      vs = x16sum(row16sum(vs));
      const float mb = rb(mean), den = rb(__builtin_sqrtf(rb(rb(vs * (1.f / DM)) + 1e-5f)));
      float ln[8];
#pragma unroll
      for (int e = 0; e < 8; ++e) { const float xe = qkq == 0 ? xf[0][e] : (qkq == 1 ? xf[1][e] : xf[2][e]); ln[e] = rb(rb(xe - mb) / den); }
      lds_wait(L, C_DMA0, 8);
      v4u wf[6];
#pragma unroll
      for (int ks = 0; ks < 6; ++ks) wf[ks] = trfrag(L, L_W + (32 * ks + 8 * g + q4) * 128 + (sw ^ fz) * 32 + p4 * 8, 4 * 128);
      STAMP(29);
      if (!(K_X & 16)) {
      v4u sa, ca;
      const int mo = qact ? WS_MOD + (qb * NMOD + 192 * qkq + 8 * qj) * 2 : 0x7ffffff0;
      if (sw == 0) {
        const int pw = (lane < 3 ? 0 : 6) + 3 * qkq + lane;
        const int fo = lane < 6 ? WS_CLR + pw * 8 : 0x7ffffff0, io = lane < 6 ? WS_MOD + (7 * NMOD + pw * 64 + 62) * 2 : 0x7ffffff0;
        bool cleared = false;
        for (unsigned spins = 0;;) {
          MEMBAR();
          if (!cleared) { const v2u fl = ldsc1_2(rws, fo); cleared = __all(lane >= 6 || (fl.x == tg0 && fl.y == tg1)); }
          else if (__builtin_popcountll(__builtin_amdgcn_ballot_w64(lane < 6 && __builtin_amdgcn_raw_buffer_load_b32(rws, io, 0, AUX_SC1) != SENT)) >= K_H1NEAR) break;
          if (give_up(spins, L)) break;
          __builtin_amdgcn_s_sleep(K_PSL);
        }
        if (lane == 0) stw(L, C_HS, 1u);
      } else lds_wait(L, C_HS, 1);
      for (unsigned spins = 0;;) {
        MEMBAR();
        sa = ldsc1(rws, mo); ca = ldsc1(rws, qact ? mo + DM * 2 : mo);
        if (__all(umax(max4(sa), max4(ca)) != SENT)) break;
        if (give_up(spins, L)) break;
        __builtin_amdgcn_s_sleep(K_PSL);
      }
      STAMP(8);
      v4u h;
#pragma unroll
      for (int e = 0; e < 4; ++e) {
        const unsigned c1 = pk(1.f + bflo(ca[e]), 1.f + bfhi(ca[e]));
        const unsigned m1 = pk(ln[2 * e] * bflo(c1), ln[2 * e + 1] * bfhi(c1));
        h[e] = pk(bflo(m1) + bflo(sa[e]), bfhi(m1) + bfhi(sa[e]));
      }
      if (qact) *(LAS v4u*)(L + L_HIMG + qb * HSTR + qj * 16) = h;
      LGWAIT();
      lds_add(L, C_SYNC1); lds_wait(L, C_SYNC1, 4);
      f4 D0 = {0.f, 0.f, 0.f, 0.f}, D1 = {0.f, 0.f, 0.f, 0.f};
#pragma unroll
      for (int ks = 0; ks < 6; ks += 2) {
        D0 = mfma(wf[ks], lds128(L, L_HIMG + (lane & 7) * HSTR + (32 * ks + 8 * g) * 2), D0);
        D1 = mfma(wf[ks + 1], lds128(L, L_HIMG + (lane & 7) * HSTR + (32 * ks + 32 + 8 * g) * 2), D1);
      }
      if ((lane & 15) < 8) stsc1(san4(__builtin_bit_cast(v4u, D0 + D1)), rws, WS_QP + ((qkq * 8 + (lane & 7)) * NQ + qcg * 64 + sw * 16 + 4 * g) * 4);
      STAMP(9);
      }
    }
    if (sw == 0 && !(K_X & 8)) {
      v4u a0, a1, a2;
      const int qo = WS_QP + (ab * NQ + akv * 256 + 4 * lane) * 4;
      const int fo = lane < 12 ? WS_CLR + (54 + 12 * akv + lane) * 8 : 0x7ffffff0;
      const int io = lane < 12 ? WS_QP + (((lane % 3) * 8 + ab) * NQ + (4 * akv + lane / 3) * 64 + 63) * 4 : 0x7ffffff0;
      bool cleared = false;
      for (unsigned spins = 0;;) {
        MEMBAR();
        if (!cleared) { const v2u fl = ldsc1_2(rws, fo); cleared = __all(lane >= 12 || (fl.x == tg0 && fl.y == tg1)); }
        else if (__builtin_popcountll(__builtin_amdgcn_ballot_w64(lane < 12 && __builtin_amdgcn_raw_buffer_load_b32(rws, io, 0, AUX_SC1) != SENT)) >= K_H2NEAR) break;
        if (give_up(spins, L)) break;
        __builtin_amdgcn_s_sleep(K_PSL);
      }
      for (unsigned spins = 0;;) {
        MEMBAR();
        a0 = ldsc1(rws, qo); a1 = ldsc1(rws, qo + NB * NQ * 4); a2 = ldsc1(rws, qo + 2 * NB * NQ * 4);
        if (__all(umax(umax(max4(a0), max4(a1)), max4(a2)) != SENT)) break;
        if (give_up(spins, L)) break;
        __builtin_amdgcn_s_sleep(K_PSL);
      }
      const f4 s = (__builtin_bit_cast(f4, a0) + __builtin_bit_cast(f4, a1)) + __builtin_bit_cast(f4, a2);
      *(LAS v2u*)(L + L_Q + lane * 8) = v2u{pk(s.x, s.y), pk(s.z, s.w)};
      STAMP(10);
    }
    RAWBAR();
    if (isF && sw == 3) {
      const v4u ss = {SENT, SENT, SENT, SENT};
#pragma unroll 3
      for (int j = 0; j < (K_X & 1 ? 0 : 9); ++j) stsc1(ss, rws, WS_DP + ((j * 8 + (lane >> 3)) * 144 + f) * 128 + (lane & 7) * 16);
      VMWAIT(0);
      if (lane == 0) __hip_atomic_store((GAS u64*)(p.ws + WS_CLR2) + f, tagv, __ATOMIC_RELAXED, __HIP_MEMORY_SCOPE_AGENT);
    }
    if (isM && !isMc) {
      lds_wait(L, C_DMA0, 8); lds_wait(L, C_KVL, 8);
      const rsrc_t rw = mkr(p.wm, DM * NMOD * 2);
      const int c16 = lane & 7;
#pragma unroll 1
      for (int j = 0; j < (K_X & 4 ? 0 : 18); ++j) {
        const int i = sw * 18 + j;
        const int fs = ((lane >> 4) & 1) | ((i & 1) << 1);
        dma16(rw, (lane >> 3) * (NMOD * 2) + ((((c16 >> 1) ^ fs) << 1) | (c16 & 1)) * 16 + wg * 128, i * 8 * (NMOD * 2), ldsb + L_W + i * 1024);
        if (K_WOPACE) __builtin_amdgcn_s_sleep(K_WOPACE);
      }
      const rsrc_t rc = mkr(p.c, NB * DM * 2);
#pragma unroll 1
      for (int j = 0; j < 3; ++j) {
        const int ch = stid + 256 * j;
        const v4u cv = __builtin_amdgcn_raw_buffer_load_b128(rc, ch * 16, 0, 0);
        v4u o;
#pragma unroll
        for (int e = 0; e < 4; ++e) o[e] = pk(silu_acc(bflo(cv[e])), silu_acc(bfhi(cv[e])));
        if (ch < 576) *(LAS v4u*)(L + L_SIMG + (ch / 72) * XSTR + (ch % 72) * 16) = o;
      }
      VMWAIT(0); LGWAIT();
      lds_add(L, C_SYNC1); lds_wait(L, C_SYNC1, 4);
      mod_mfma();
    }
    if ((isF || isW) && sw == 3) {
      for (unsigned spins = 0;;) {
        MEMBAR();
        const v4u a = ldsc1(rws, WS_CLR + lane * 32), b = ldsc1(rws, WS_CLR + lane * 32 + 16);
        bool ok = a.x == tg0 && a.y == tg1 && a.z == tg0 && a.w == tg1 && b.x == tg0 && b.y == tg1 && b.z == tg0 && b.w == tg1;
        if (isF) {
          const v4u c2 = ldsc1(rws, lane < 36 ? WS_CLR2 + lane * 32 : 0x7ffffff0), d2 = ldsc1(rws, lane < 36 ? WS_CLR2 + lane * 32 + 16 : 0x7ffffff0);
          ok = ok && (lane >= 36 || (c2.x == tg0 && c2.y == tg1 && c2.z == tg0 && c2.w == tg1 && d2.x == tg0 && d2.y == tg1 && d2.z == tg0 && d2.w == tg1));
        }
        if (__all(ok)) break;
        if (give_up(spins, L)) break;
        __builtin_amdgcn_s_sleep(127);
      }
      if (lane == 0) stw(L, C_HS2, 1u);
      STAMP(24);
    }
    if (isW || isF) {
      if (isM && !isMc) { lds_add(L, 17); lds_wait(L, 17, 4); }
      lds_wait(L, C_KVL, 8);
      const int sc = ((lane & 3) ^ ((lane >> 4) & 2)) * 16;
      if (isW) {
        const rsrc_t rw = mkr(p.wo, NQ * DM * 2);
        const int vo = (lane >> 2) * (DM * 2) + sc + wng * 64;
        lds_wait<16>(L, C_TRIG, 1);
        STAMP(11);
#pragma unroll 1
        for (int j = 0; j < (K_X & 2 ? 0 : K_WOEARLY); ++j) { const int i = j * 4 + sw; dma16<K_WNT != 0>(rw, vo, (wsig * 2048 + i * 16) * DM * 2, ldsb + L_W + i * 1024); if (K_WOPACE) __builtin_amdgcn_s_sleep(K_WOPACE); }
        lds_wait<4>(L, C_TRIG, 2);
#pragma unroll 1
        for (int j = K_WOEARLY; j < 32; ++j) { const int i = j * 4 + sw; dma16<K_WNT != 0>(rw, vo, (wsig * 2048 + i * 16) * DM * 2, ldsb + L_W + i * 1024); }
      } else {
        const rsrc_t rg = mkr(p.wg, DM * DFF * 2), ru = mkr(p.wu, DM * DFF * 2), rd = mkr(p.wd, DFF * DM * 2);
        const int vo = (lane >> 2) * (DFF * 2) + sc + f * 64;
        lds_wait<4>(L, C_TRIG, K_FFNEARLY ? 1 : 2);
        STAMP(11);
        for (int k = 0; k < K_FFNDELAY; ++k) __builtin_amdgcn_s_sleep(16);
        if (K_FFNEARLY == 2) {
#pragma unroll 1
          for (int j = 0; j < 9; ++j) { const int i = j * 4 + sw; dma16(rd, lane * 16, f * 32 * DM * 2 + i * 1024, ldsb + L_WD + i * 1024); __builtin_amdgcn_s_sleep(K_FPACE); }
          lds_wait<4>(L, C_TRIG, 2);
        }
#pragma unroll 1
        for (int j = 0; j < 9; ++j) {
          const int i = j * 4 + sw;
          throttle(K_DMAR); dma16<K_WNT != 0>(rg, vo, i * 16 * DFF * 2, ldsb + L_W + i * 1024); if (K_FFNEARLY == 1) __builtin_amdgcn_s_sleep(K_FPACE);
          throttle(K_DMAR); dma16<K_WNT != 0>(ru, vo, i * 16 * DFF * 2, ldsb + L_WU + i * 1024); if (K_FFNEARLY == 1) __builtin_amdgcn_s_sleep(K_FPACE);
        }
        if (K_FFNEARLY != 2) {
#pragma unroll 1
          for (int j = 0; j < (K_DLATE ? 0 : 9); ++j) {
            if (!K_DLATE) {
              throttle(K_DMAR);
              if (j == (K_DMAR < 8 ? K_DMAR - 1 : 8)) { if (K_DMAR >= 8) VMWAIT(8); lds_add(L, C_DMA1); }
            }
            const int i = j * 4 + sw; dma16(rd, lane * 16, f * 32 * DM * 2 + i * 1024, ldsb + L_WD + i * 1024); if (K_FFNEARLY) __builtin_amdgcn_s_sleep(K_FPACE);
          }
        }
      }
      VMWAIT(0);
      if (isW || K_FFNEARLY == 2 || K_DLATE) lds_add(L, C_DMA1);
      if (isW || !K_DLATE) lds_add(L, C_DMA2);
      STAMP(12);
    }
  }

  if (isW) {
    const int cc = lane & 15;
    if (wave < 8) {
      const int pid = (cc & 7) * 32 + (wsig * 8 + wave) * 2 + (cc >> 3);
      lds_wait(L, C_HS2, 1);
      lds_wait(L, C_DMA1, 4);
      v4u wf[2][4][2];
#pragma unroll
      for (int hh = 0; hh < 2; ++hh)
#pragma unroll
        for (int ks = 0; ks < 4; ++ks)
#pragma unroll
          for (int nt = 0; nt < 2; ++nt) wf[hh][ks][nt] = trfrag(L, L_W + (wave * 256 + hh * 128 + 32 * ks + 8 * g + q4) * 64 + (nt ^ (g & 1)) * 32 + p4 * 8, 4 * 64);
      v4u ml, fr[2][4];
      for (unsigned spins = 0;;) {
        MEMBAR();
        ml = ldsc1(rws, WS_ML + pid * 16);
        const int nv = __builtin_popcountll(__builtin_amdgcn_ballot_w64(max4(ml) != SENT));
        if (nv >= 4 * K_WNEAR) break;
        if (give_up(spins, L)) break;
        if (nv < 4 * 12) __builtin_amdgcn_s_sleep(K_WSL2); else __builtin_amdgcn_s_sleep(K_WSL);
      }
      STAMP(13);
      for (unsigned spins = 0;;) {
        MEMBAR();
        ml = ldsc1(rws, WS_ML + pid * 16);
#pragma unroll
        for (int hh = 0; hh < 2; ++hh)
#pragma unroll
          for (int ks = 0; ks < 4; ++ks) fr[hh][ks] = ldsc1(rws, WS_OP + pid * 512 + (hh * 128 + 32 * ks + 8 * g) * 2);
        unsigned mx = max4(ml);
#pragma unroll
        for (int hh = 0; hh < 2; ++hh)
#pragma unroll
          for (int ks = 0; ks < 4; ++ks) mx = umax(mx, max4(fr[hh][ks]));
        if (__all(mx != SENT)) break;
        if (give_up(spins, L)) break;
      }
      float wgt[2];
#pragma unroll
      for (int hh = 0; hh < 2; ++hh) {
        const float M = asf(ml[2 * hh]), Lv = asf(ml[2 * hh + 1]);
        const float M2 = dpp<ROR8>(M), L2 = dpp<ROR8>(Lv), Mx = fmaxf(M, M2);
        const float e1 = __builtin_amdgcn_exp2f((M - Mx) * LOG2E) * Lv, e2 = __builtin_amdgcn_exp2f((M2 - Mx) * LOG2E) * L2;
        wgt[hh] = e1 * __builtin_amdgcn_rcpf(e1 + e2);
      }
      STAMP(14);
#pragma unroll
      for (int nt = 0; nt < 2; ++nt) {
        f4 d0 = {0.f, 0.f, 0.f, 0.f}, d1 = {0.f, 0.f, 0.f, 0.f};
#pragma unroll
        for (int ks = 0; ks < 4; ++ks) { d0 = mfma(wf[0][ks][nt], fr[0][ks], d0); d1 = mfma(wf[1][ks][nt], fr[1][ks], d1); }
        f4 t = wgt[0] * d0 + wgt[1] * d1;
        t.x += dpp<ROR8>(t.x); t.y += dpp<ROR8>(t.y); t.z += dpp<ROR8>(t.z); t.w += dpp<ROR8>(t.w);
        if (cc < 8) *(LAS f4*)(L + L_RED + (((wave * 2 + nt) * 4 + g) * 8 + cc) * 16) = t;
      }
      LGWAIT();
      if (lds_add(L, C_SYNC1 + 16) == 7u) {
        MEMBAR();
        f4 s = {0.f, 0.f, 0.f, 0.f};
#pragma unroll
        for (int w = 0; w < 8; ++w) s += *(LAS f4*)(L + L_RED + (w * 64 + lane) * 16);
        const int nt = lane >> 5, rr = (lane >> 3) & 3, b = lane & 7;
        stsc1(san4(__builtin_bit_cast(v4u, s)), rws, WS_YP + ((wsig * 8 + b) * DM + wng * 32 + nt * 16 + 4 * rr) * 4);
        STAMP(15);
      }
    }
  } else if (isF) {
    const bool isR = f < 72;
    const int rb_ = f / 9, rc0 = (f % 9) * 64;
    unsigned gbw = 0;
    const int gt = wave & 3, kt = wave >> 2;
    v4u wf[6], wd[4];
    const int dw = wave - 3;
    bool have_wd = false;
    auto read_wd = [&]() {
      lds_wait(L, C_DMA2, 4);
#pragma unroll
      for (int j = 0; j < 4; ++j) wd[j] = trfrag(L, L_WD + (8 * g + q4) * (DM * 2) + (dw >= 0 ? 4 * dw + j : 0) * 32 + p4 * 8, 4 * DM * 2);
    };
    if (wave < 8) {
      const int b = wave;
      const bool act = lane < 48;
      const rsrc_t rx = mkr(p.x, NB * DM * 2);
      v2u xv[3], ga[3], sb[3], cb[3];
      lds_wait(L, C_HS2, 1);
#pragma unroll
      for (int t = 0; t < 3; ++t) xv[t] = __builtin_amdgcn_raw_buffer_load_b64(rx, act ? (b * DM + 4 * (lane + 48 * t)) * 2 : 0x7ffffff0, 0, 0);
      for (unsigned spins = 0;;) {
        MEMBAR();
        unsigned mx = 0;
#pragma unroll
        for (int t = 0; t < 3; ++t) {
          const int o = act ? WS_MOD + (b * NMOD + 4 * (lane + 48 * t)) * 2 : 0x7ffffff0;
          ga[t] = ldsc1_2(rws, act ? o + 2 * DM * 2 : o); sb[t] = ldsc1_2(rws, act ? o + 3 * DM * 2 : o); cb[t] = ldsc1_2(rws, act ? o + 4 * DM * 2 : o);
          mx = umax(mx, umax(umax(umax(ga[t].x, ga[t].y), umax(sb[t].x, sb[t].y)), umax(cb[t].x, cb[t].y)));
        }
        if (wave == 0) { gbw = __builtin_amdgcn_raw_buffer_load_b32(rws, isR ? WS_MOD + (rb_ * NMOD + 5 * DM + rc0 + (lane & ~1)) * 2 : 0x7ffffff0, 0, AUX_SC1); mx = umax(mx, gbw); }
        if (__all(mx != SENT)) break;
        if (give_up(spins, L)) break;
      }
      float xf[12], gf[12], cf[12], sf[12];
#pragma unroll
      for (int t = 0; t < 3; ++t)
#pragma unroll
        for (int e = 0; e < 2; ++e) {
          xf[4 * t + 2 * e] = bflo(xv[t][e]); xf[4 * t + 2 * e + 1] = bfhi(xv[t][e]);
          gf[4 * t + 2 * e] = bflo(ga[t][e]); gf[4 * t + 2 * e + 1] = bfhi(ga[t][e]);
          cf[4 * t + 2 * e] = 1.f + bflo(cb[t][e]); cf[4 * t + 2 * e + 1] = 1.f + bfhi(cb[t][e]);
          sf[4 * t + 2 * e] = bflo(sb[t][e]); sf[4 * t + 2 * e + 1] = bfhi(sb[t][e]);
        }
#pragma unroll
      for (int e = 0; e < 12; ++e) asm volatile("" : "+v"(xf[e]), "+v"(gf[e]), "+v"(cf[e]), "+v"(sf[e]));
      lds_wait(L, C_DMA1, 4);
#pragma unroll
      for (int k6 = 0; k6 < 6; ++k6) wf[k6] = trfrag(L, ((gt >> 1) ? L_WU : L_W) + (32 * (kt * 6 + k6) + 8 * g + q4) * 64 + ((gt & 1) ^ (g & 1)) * 32 + p4 * 8, 4 * 64);
      if (wave >= 3 && __builtin_amdgcn_readfirstlane(ldsw(L, C_DMA2)) >= 4u) { read_wd(); have_wd = true; }
      if (wave == 0) {
        for (unsigned spins = 0;;) {
          MEMBAR();
          const unsigned v = __builtin_amdgcn_raw_buffer_load_b32(rws, lane == 0 ? WS_YP + (((f & 1) * 8 + (f & 7)) * DM + (f % 18) * 32) * 4 : 0x7ffffff0, 0, AUX_SC1);
          if (__builtin_amdgcn_readfirstlane(v) != SENT) break;
          if (give_up(spins, L)) break;
          __builtin_amdgcn_s_sleep(K_YSL);
        }
        if (lane == 0) stw(L, C_Y, 1u);
      } else lds_wait(L, C_Y, 1);
      STAMP(16);
      v4u y0[3], y1[3];
      for (unsigned spins = 0;;) {
        MEMBAR();
        unsigned mx = 0;
#pragma unroll
        for (int t = 0; t < 3; ++t) {
          const int o = act ? WS_YP + (b * DM + 4 * (lane + 48 * t)) * 4 : 0x7ffffff0;
          y0[t] = ldsc1(rws, o); y1[t] = ldsc1(rws, act ? o + NB * DM * 4 : o);
        }
#pragma unroll
        for (int t = 0; t < 3; ++t) mx = umax(mx, umax(max4(y0[t]), max4(y1[t])));
        if (__all(mx != SENT)) break;
        if (give_up(spins, L)) break;
      }
      STAMP(17);
      float x1[12];
#pragma unroll
      for (int e = 0; e < 12; ++e) x1[e] = __builtin_fmaf(gf[e], asf(y0[e >> 2][e & 3]) + asf(y1[e >> 2][e & 3]), xf[e]);
      const float sh = asf(__builtin_amdgcn_readfirstlane(asu(x1[0])));
      float s1 = 0.f, s2 = 0.f;
#pragma unroll
      for (int e = 0; e < 12; ++e) { const float d = x1[e] - sh; s1 += d; s2 = __builtin_fmaf(d, d, s2); }
      s1 = act ? s1 : 0.f; s2 = act ? s2 : 0.f;
      s1 += dpp<ROR1>(s1); s2 += dpp<ROR1>(s2); s1 += dpp<ROR2>(s1); s2 += dpp<ROR2>(s2);
      s1 += dpp<ROR4>(s1); s2 += dpp<ROR4>(s2); s1 += dpp<ROR8>(s1); s2 += dpp<ROR8>(s2);
      s1 = x16sum(s1); s2 = x16sum(s2); s1 = x32sum(s1); s2 = x32sum(s2);
      const float dm = s1 * (1.f / DM), var = fmaxf(__builtin_fmaf(-dm, dm, s2 * (1.f / DM)), 0.f);
      const float rstd = __builtin_amdgcn_rsqf(var + 1e-5f), mo = -(sh + dm) * rstd;
#pragma unroll
      for (int t = 0; t < 3; ++t) {
        float hv[4];
#pragma unroll
        for (int e = 0; e < 4; ++e) hv[e] = __builtin_fmaf(__builtin_fmaf(x1[4 * t + e], rstd, mo), cf[4 * t + e], sf[4 * t + e]);
        if (act) {
          *(LAS v2u*)(L + L_H2 + b * XSTR + 8 * (lane + 48 * t)) = v2u{pk(hv[0], hv[1]), pk(hv[2], hv[3])};
          const int col = 4 * (lane + 48 * t) - rc0;
          if (isR && b == rb_ && col >= 0 && col < 64) *(LAS f4*)(L + L_X1S + col * 4) = f4{x1[4 * t], x1[4 * t + 1], x1[4 * t + 2], x1[4 * t + 3]};
        }
      }
    } else {
      lds_wait(L, C_DMA1, 4);
#pragma unroll
      for (int k6 = 0; k6 < 6; ++k6) wf[k6] = trfrag(L, ((gt >> 1) ? L_WU : L_W) + (32 * (kt * 6 + k6) + 8 * g + q4) * 64 + ((gt & 1) ^ (g & 1)) * 32 + p4 * 8, 4 * 64);
      lds_wait(L, C_Y, 1);
      if (K_DLATE) {
        const rsrc_t rd = mkr(p.wd, DFF * DM * 2);
#pragma unroll 1
        for (int j = 0; j < 9; ++j) { const int i = j * 4 + (wave - 8); dma16(rd, lane * 16, f * 32 * DM * 2 + i * 1024, ldsb + L_WD + i * 1024); }
      }
      if (__builtin_amdgcn_readfirstlane(ldsw(L, C_DMA2)) >= 4u) { read_wd(); have_wd = true; }
    }
    RAWBAR();
    STAMP(18);
    {
      f4 D0 = {0.f, 0.f, 0.f, 0.f}, D1 = {0.f, 0.f, 0.f, 0.f};
#pragma unroll
      for (int k6 = 0; k6 < 6; k6 += 2) {
        D0 = mfma(wf[k6], lds128(L, L_H2 + (lane & 7) * XSTR + (32 * (kt * 6 + k6) + 8 * g) * 2), D0);
        D1 = mfma(wf[k6 + 1], lds128(L, L_H2 + (lane & 7) * XSTR + (32 * (kt * 6 + k6 + 1) + 8 * g) * 2), D1);
      }
      if ((lane & 15) < 8) *(LAS f4*)(L + L_GU + (((kt * 4 + gt) * 4 + g) * 8 + (lane & 7)) * 16) = D0 + D1;
      if (K_DLATE && wave >= 8) { VMWAIT(0); lds_add(L, C_DMA2); }
    }
    RAWBAR();
    STAMP(7);
    if (!have_wd && wave >= 4) read_wd();
    if (tid < 256) {
      const int b = tid & 7, fc = tid >> 3;
      const int o = (((fc >> 4) * 4 + ((fc >> 2) & 3)) * 8 + b) * 16 + (fc & 3) * 4;
      float gs = 0.f, us = 0.f;
#pragma unroll
      for (int k3 = 0; k3 < 3; ++k3) { gs += *(LAS float*)(L + L_GU + k3 * 2048 + o); us += *(LAS float*)(L + L_GU + k3 * 2048 + 1024 + o); }
      const float a = rb(gelu_t(rb(gs))) * rb(us);
      *(LAS u16*)(L + L_ACT + b * ASTR + fc * 2) = (u16)pk(a, a);
      if (!have_wd && wave == 3) read_wd();
    }
    RAWBAR();
    STAMP(31);
    if (wave >= 3) {
      const v4u af = lds128(L, L_ACT + (lane & 7) * ASTR + g * 16);
      const int b = lane & 7, hi = (lane >> 3) & 1;
      const f4 z4 = {0.f, 0.f, 0.f, 0.f};
      const f4 T0 = mfma(wd[0], af, z4), T1 = mfma(wd[1], af, z4), T2 = mfma(wd[2], af, z4), T3 = mfma(wd[3], af, z4);
      const f4 Da = hi ? T2 : T0, Db = hi ? T3 : T1;
      stsc1(san4(v4u{pk(Da.x, Da.y), pk(Da.z, Da.w), pk(Db.x, Db.y), pk(Db.z, Db.w)}), rws, WS_DP + ((b * 9 + dw) * 144 + f) * 128 + (hi * 4 + g) * 16);
    }
    STAMP(19);
    if (isR) {
      v4u a, b2;
      const int o = WS_DP + f * 144 * 128 + tid * 16;
      for (unsigned spins = 0;;) {
        MEMBAR();
        a = ldsc1(rws, o); b2 = ldsc1(rws, tid < 384 ? o + 768 * 16 : 0x7ffffff0);
        if (__all(umax(max4(a), max4(b2)) != SENT)) break;
        if (give_up(spins, L)) break;
      }
      STAMP(20);
      float s[8];
#pragma unroll
      for (int e = 0; e < 4; ++e) { s[2 * e] = bflo(a[e]) + bflo(b2[e]); s[2 * e + 1] = bfhi(a[e]) + bfhi(b2[e]); }
#pragma unroll
      for (int e = 0; e < 8; ++e) { float v = s[e]; v += dpp<ROR8>(v); v = x32sum(x16sum(v)); s[e] = v; }
      if (lane < 8) {
        *(LAS f4*)(L + L_RED2 + (wave * 8 + lane) * 32) = f4{s[0], s[1], s[2], s[3]};
        *(LAS f4*)(L + L_RED2 + (wave * 8 + lane) * 32 + 16) = f4{s[4], s[5], s[6], s[7]};
      }
      RAWBAR();
    }
    if (isR && wave == 0) {
      const int ro = (((lane >> 5) * 4 + ((lane >> 2) & 3)) * 8 + ((lane >> 4) & 1) * 4 + (lane & 3)) * 4;
      float dn = 0.f;
#pragma unroll
      for (int w = 0; w < 12; ++w) dn += *(LAS float*)(L + L_RED2 + w * 256 + ro);
      const float gv = (lane & 1) ? bfhi(gbw) : bflo(gbw);
      const float o = *(LAS float*)(L + L_X1S + lane * 4) + rb(gv * rb(dn));
      const unsigned two = pk(o, dpp<QX1>(o));
      if (!(lane & 1)) *(GAS unsigned*)(p.out + rb_ * DM + rc0 + lane) = two;
      if (wg == 0 && lane == 0) {
        const u64 E = ((u64)ldsw(L, C_E1) << 32) | ldsw(L, C_E0);
        __hip_atomic_store((GAS u64*)(p.ws + WS_EPOCH), E + 1, __ATOMIC_RELAXED, __HIP_MEMORY_SCOPE_AGENT);
      }
      STAMP(21);
    }
  }
}

}

extern "C" void kernel_launch(void* const* d_in, const int* in_sizes, int n_in, void* d_out, int out_size, void* d_ws, size_t ws_size, hipStream_t stream) {
  ak::Params p;
  p.x = (const ak::u16*)d_in[0]; p.K = (const ak::u16*)d_in[1]; p.V = (const ak::u16*)d_in[2];
  p.wq = (const ak::u16*)d_in[3]; p.wo = (const ak::u16*)d_in[4];
  p.wg = (const ak::u16*)d_in[6]; p.wu = (const ak::u16*)d_in[7]; p.wd = (const ak::u16*)d_in[8];
  p.c = (const ak::u16*)d_in[9]; p.wm = (const ak::u16*)d_in[10];
  p.out = (ak::u16*)d_out; p.ws = (unsigned char*)d_ws;
  (void)hipFuncSetAttribute((const void*)ak::fused_kernel, hipFuncAttributeMaxDynamicSharedMemorySize, ak::L_END);
  ak::fused_kernel<<<ak::NWG, ak::NTHR, ak::L_END, stream>>>(p);
}
```

```cpp
#include <hip/hip_runtime.h>
#include <stdint.h>

namespace ak {

typedef unsigned short u16;
typedef unsigned long long u64;
typedef unsigned v4u __attribute__((ext_vector_type(4)));
typedef unsigned v2u __attribute__((ext_vector_type(2)));
typedef float f4 __attribute__((ext_vector_type(4)));
typedef float f2 __attribute__((ext_vector_type(2)));
typedef __bf16 bf8 __attribute__((ext_vector_type(8)));
typedef __bf16 bf2 __attribute__((ext_vector_type(2)));
typedef short s4v __attribute__((ext_vector_type(4)));
#define LAS __attribute__((address_space(3)))
#define GAS __attribute__((address_space(1)))
typedef __amdgpu_buffer_rsrc_t rsrc_t;

#define K_R 3
#define K_RTAIL 5
#define K_TTAIL 40
#define K_NP 2
#define K_PF 4
#define K_TRIG_WO 14
#define K_TRIG_FFN 33
#define K_NL 2
#define K_FB 0
#define K_FB1 2
#define K_FB2 6
#define K_FBW 1
#define K_FBPER 6
#define K_PRIO 0
#define K_WOPACE 30
#define K_WNT 1
#define K_FFNDELAY 0
#define K_WOEARLY 32
#define K_FFNEARLY 0
#define K_FPACE 60
#define K_PSL 1
#define K_WNEAR 15
#define K_WSL 1
#define K_FFNTRIG 0
#define K_H1NEAR 6
#define K_H2NEAR 12
#define K_X 0
#define K_YSL 1
#define K_WSL2 24
#define K_DMAR 5
#define K_PFN 12
#define K_PF2 3
#define K_DLATE 0
#define K_RPAT 23333333
#define K_TLBPF 0
#define K_TLBST 65536
#define K_ROT 0
#define K_RPATV K_RPAT
#define K_RA_W 2
#define K_RA_MC 1
#define K_RA_ML 0
#define K_MHOLD 1
#define K_STAMPS 0

constexpr int NB = 8, DM = 576, DFF = 4608, NMOD = 3456, NQ = 4096;
constexpr int NWG = 256, NTHR = 768;
constexpr int NTILE = 512;
constexpr int NP = K_NP, P = NP * 8;
__host__ __device__ constexpr unsigned rdigit(unsigned v, int k) { for (int i = 7; i > k; --i) v /= 10; return v % 10; }
constexpr float LOG2E = 1.4426950408889634f;
constexpr float SCALE = 0.08838834764831845f;
constexpr unsigned SENT = 0xFFFFFFFFu;
constexpr int AUX_NT = 2, AUX_SC1 = 16;
constexpr unsigned SPIN_CAP = 1u << 15, LDS_CAP = 1u << 18;

constexpr int WS_EPOCH = 0, WS_PRG = 1024, WS_CLR = 4096, WS_CLR2 = 6144;
constexpr int WS_MOD = 8192;
constexpr int WS_QP = 65536;
constexpr int WS_OP = WS_QP + 3 * NB * NQ * 4;
constexpr int WS_ML = WS_OP + NWG * 512;
constexpr int WS_YP = WS_ML + NWG * 16;
constexpr int WS_DP = WS_YP + 2 * NB * DM * 4;
constexpr int WS_END = WS_DP + 72 * 144 * 128;
constexpr size_t WS_STAMP = 64u << 20;
constexpr int NSTAMP = 32;

constexpr int L_W = 0;
constexpr int L_SIMG = 73728;
constexpr int L_HIMG = 24576;
constexpr int HSTR = 400;
constexpr int L_WU = 36864, L_WD = 73728;
constexpr int L_GU = 110592;
constexpr int L_ACT = L_GU + 6144;
constexpr int ASTR = 80;
constexpr int L_RED2 = L_ACT + 640;
constexpr int L_X1S = L_RED2 + 3072;
constexpr int L_H2 = L_X1S + 256;
constexpr int XSTR = 1168;
static_assert(L_H2 + 8 * XSTR <= 131072, "ffn lds");
constexpr int L_RED = 131072;
constexpr int SCW = 2304;
constexpr int L_SC = 141312;
constexpr int L_KV = 28672, L_KVM = 83968;
constexpr int L_Q = 160128;
constexpr int L_CTL = L_Q + 512;
constexpr int L_STAMP = L_CTL + 128;
constexpr int L_END = L_STAMP + (K_STAMPS ? 12 * 32 * 8 : 0);
static_assert(L_KV + K_NL * 65536 <= L_Q && L_KVM + 65536 <= L_Q && L_SC + 8 * SCW <= L_Q && L_END <= 163840, "lds map");
enum { C_SYNC1 = 0, C_HS = 1, C_TRIG = 2, C_MRG = 3, C_DEAD = 4, C_DMA0 = 5, C_DMA1 = 6, C_HS2 = 7, C_KVL = 10, C_Y = 11, C_DMA2 = 12, C_PRG = 13, C_E0 = 8, C_E1 = 9 };

struct Params {
  const u16 *x, *K, *V, *wq, *wo, *wg, *wu, *wd, *c, *wm;
  u16* out;
  unsigned char* ws;
};

__device__ __forceinline__ float asf(unsigned u) { return __builtin_bit_cast(float, u); }
__device__ __forceinline__ unsigned asu(float f) { return __builtin_bit_cast(unsigned, f); }
__device__ __forceinline__ float bflo(unsigned u) { return asf(u << 16); }
__device__ __forceinline__ float bfhi(unsigned u) { return asf(u & 0xffff0000u); }
__device__ __forceinline__ unsigned pk(float a, float b) {
  f2 v = {a, b};
  return __builtin_bit_cast(unsigned, __builtin_convertvector(v, bf2));
}
__device__ __forceinline__ float rb(float a) { return bflo(pk(a, a)); }
__device__ __forceinline__ unsigned san(unsigned u) { return u < 0xFFFFFFFEu ? u : 0xFFFFFFFEu; }
__device__ __forceinline__ v4u san4(v4u v) { return v4u{san(v.x), san(v.y), san(v.z), san(v.w)}; }
__device__ __forceinline__ unsigned umax(unsigned a, unsigned b) { return a > b ? a : b; }
__device__ __forceinline__ unsigned max4(v4u v) { return umax(umax(v.x, v.y), umax(v.z, v.w)); }
template <int C> __device__ __forceinline__ float dpp(float x) {
  return asf((unsigned)__builtin_amdgcn_mov_dpp((int)asu(x), C, 0xf, 0xf, true));
}
template <int C> __device__ __forceinline__ unsigned dppu(unsigned x) { return (unsigned)__builtin_amdgcn_mov_dpp((int)x, C, 0xf, 0xf, true); }
constexpr int ROR1 = 0x121, ROR2 = 0x122, ROR4 = 0x124, ROR8 = 0x128, QX1 = 0xB1, QX2 = 0x4E;
__device__ __forceinline__ float x32max(float x) { auto t = __builtin_amdgcn_permlane32_swap(asu(x), asu(x), false, false); return fmaxf(asf(t[0]), asf(t[1])); }
__device__ __forceinline__ float x32sum(float x) { auto t = __builtin_amdgcn_permlane32_swap(asu(x), asu(x), false, false); return asf(t[0]) + asf(t[1]); }
__device__ __forceinline__ float x16sum(float x) { auto t = __builtin_amdgcn_permlane16_swap(asu(x), asu(x), false, false); return asf(t[0]) + asf(t[1]); }
__device__ __forceinline__ float row16sum(float x) { x += dpp<ROR1>(x); x += dpp<ROR2>(x); x += dpp<ROR4>(x); x += dpp<ROR8>(x); return x; }
__device__ __forceinline__ f4 mfma(v4u a, v4u b, f4 c) {
  return __builtin_amdgcn_mfma_f32_16x16x32_bf16(__builtin_bit_cast(bf8, a), __builtin_bit_cast(bf8, b), c, 0, 0, 0);
}
__device__ __forceinline__ rsrc_t mkr(const void* p, unsigned bytes) { return __builtin_amdgcn_make_buffer_rsrc((void*)p, 0, bytes, 0x00020000); }
__device__ __forceinline__ v4u ldsc1(rsrc_t r, int off) { return __builtin_amdgcn_raw_buffer_load_b128(r, off, 0, AUX_SC1); }
__device__ __forceinline__ v2u ldsc1_2(rsrc_t r, int off) { return __builtin_amdgcn_raw_buffer_load_b64(r, off, 0, AUX_SC1); }
__device__ __forceinline__ void stsc1(v4u v, rsrc_t r, int off) { __builtin_amdgcn_raw_buffer_store_b128(v, r, off, 0, AUX_SC1); }
__device__ __forceinline__ void stsc1_2(v2u v, rsrc_t r, int off) { __builtin_amdgcn_raw_buffer_store_b64(v, r, off, 0, AUX_SC1); }
#define MEMBAR() asm volatile("" ::: "memory")
#define VMWAIT(n) asm volatile("s_waitcnt vmcnt(" #n ")" ::: "memory")
#define LGWAIT() asm volatile("s_waitcnt lgkmcnt(0)" ::: "memory")
#define RAWBAR() asm volatile("s_waitcnt lgkmcnt(0)\n\ts_barrier" ::: "memory")
template <bool NT = true> __device__ __forceinline__ void dma16(rsrc_t r, int voff, unsigned soff, unsigned ldsa) {
  unsigned keep;
  if (NT) asm volatile("s_mov_b32 %0, m0\n\ts_mov_b32 m0, %1\n\ts_nop 0\n\tbuffer_load_dwordx4 %2, %3, %4 offen nt lds\n\ts_mov_b32 m0, %0" : "=&s"(keep) : "s"(ldsa), "v"(voff), "s"(r), "s"(soff) : "memory");
  else asm volatile("s_mov_b32 %0, m0\n\ts_mov_b32 m0, %1\n\ts_nop 0\n\tbuffer_load_dwordx4 %2, %3, %4 offen lds\n\ts_mov_b32 m0, %0" : "=&s"(keep) : "s"(ldsa), "v"(voff), "s"(r), "s"(soff) : "memory");
}
__device__ __forceinline__ v4u ld_hidden(rsrc_t r, int voff) {
  v4u v;
  asm volatile("buffer_load_dwordx4 %0, %1, %2, 0 offen" : "=&v"(v) : "v"(voff), "s"(r) : "memory");
  return v;
}
__device__ __forceinline__ v4u lds128(LAS unsigned char* L, int off) { return *(LAS v4u*)(L + off); }
__device__ __forceinline__ v4u trfrag(LAS unsigned char* L, int a0, int d) {
  s4v lo = __builtin_amdgcn_ds_read_tr16_b64_v4i16((LAS s4v*)(L + a0));
  s4v hi = __builtin_amdgcn_ds_read_tr16_b64_v4i16((LAS s4v*)(L + a0 + d));
  v2u l = __builtin_bit_cast(v2u, lo), h = __builtin_bit_cast(v2u, hi);
  return v4u{l.x, l.y, h.x, h.y};
}
__device__ __forceinline__ unsigned ldsw(LAS unsigned char* L, int w) { return *(volatile LAS unsigned*)(L + L_CTL + 4 * w); }
__device__ __forceinline__ void stw(LAS unsigned char* L, int w, unsigned v) { *(volatile LAS unsigned*)(L + L_CTL + 4 * w) = v; }
__device__ __forceinline__ unsigned lds_add(LAS unsigned char* L, int w) {
  unsigned o = 0;
  if ((threadIdx.x & 63) == 0) o = __hip_atomic_fetch_add((LAS unsigned*)(L + L_CTL + 4 * w), 1u, __ATOMIC_RELAXED, __HIP_MEMORY_SCOPE_WORKGROUP);
  return __builtin_amdgcn_readfirstlane(o);
}
template <int SLP = 1> __device__ __forceinline__ void lds_wait(LAS unsigned char* L, int w, unsigned n) {
  for (unsigned spins = 0; __builtin_amdgcn_readfirstlane(ldsw(L, w)) < n; ++spins) {
    if (__builtin_amdgcn_readfirstlane(ldsw(L, C_DEAD))) break;
    if (spins > LDS_CAP) { stw(L, C_DEAD, 1u); break; }
    __builtin_amdgcn_s_sleep(SLP);
  }
  MEMBAR();
}
__device__ __forceinline__ bool give_up(unsigned& spins, LAS unsigned char* L) {
  if (++spins > SPIN_CAP) { stw(L, C_DEAD, 1u); return true; }
  return (spins & 15u) == 0u && __builtin_amdgcn_readfirstlane(ldsw(L, C_DEAD)) != 0u;
}
__device__ __forceinline__ u64 mix64(u64 z) {
  z = (z ^ (z >> 30)) * 0xBF58476D1CE4E5B9ull;
  z = (z ^ (z >> 27)) * 0x94D049BB133111EBull;
  return z ^ (z >> 31);
}
#define STAMP(k) do {} while (0)
__device__ __forceinline__ void throttle(unsigned r) {
  asm volatile("s_cmp_gt_u32 %0, 6\n\ts_cbranch_scc1 9f\n\ts_waitcnt vmcnt(5)\n\t"
               "s_cmp_gt_u32 %0, 5\n\ts_cbranch_scc1 9f\n\ts_waitcnt vmcnt(4)\n\t"
               "s_cmp_gt_u32 %0, 4\n\ts_cbranch_scc1 9f\n\ts_waitcnt vmcnt(3)\n\t"
               "s_cmp_gt_u32 %0, 3\n\ts_cbranch_scc1 9f\n\ts_waitcnt vmcnt(2)\n\t"
               "s_cmp_gt_u32 %0, 2\n\ts_cbranch_scc1 9f\n\ts_waitcnt vmcnt(1)\n\t"
               "s_cmp_gt_u32 %0, 1\n\ts_cbranch_scc1 9f\n\ts_waitcnt vmcnt(0)\n9:" ::"s"(r) : "scc", "memory");
}

__device__ __forceinline__ float sigm(float z) { return __builtin_amdgcn_rcpf(1.f + __builtin_amdgcn_exp2f(-LOG2E * z)); }
__device__ __forceinline__ float silu_acc(float a) {
  const float hi = 1.44269502162933349609375f, lo = 1.92596299112661746e-8f;
  const float t0 = -a * hi;
  const float r = __builtin_fmaf(-a, lo, __builtin_fmaf(-a, hi, -t0));
  const float e0 = __builtin_amdgcn_exp2f(fminf(t0, 126.f));
  const float d = 1.f + __builtin_fmaf(e0, r * 0.693147180559945f, e0);
  const float rc = __builtin_amdgcn_rcpf(d), q = a * rc;
  return __builtin_fmaf(__builtin_fmaf(-q, d, a), rc, q);
}
__device__ __forceinline__ float gelu_t(float x) { const float z = 0.7978845608028654f * (x + 0.044715f * x * x * x); return x * sigm(2.f * z); }

__device__ __forceinline__ float quadsum(f4 d) {
  float t = dpp<0x00>(d.x);
  t += dpp<0x55>(d.y); t += dpp<0xAA>(d.z); t += dpp<0xFF>(d.w);
  return t;
}
__device__ __forceinline__ unsigned score_pair(f4 D0, f4 D1) {
  const unsigned r1 = pk(quadsum(D0), quadsum(D1));
  return pk(bflo(r1) * SCALE, bfhi(r1) * SCALE);
}
__device__ __forceinline__ v4u pfrag(unsigned v, const unsigned (&sel)[4]) {
  return v4u{__builtin_amdgcn_perm(v, v, sel[0]), __builtin_amdgcn_perm(v, v, sel[1]), __builtin_amdgcn_perm(v, v, sel[2]), __builtin_amdgcn_perm(v, v, sel[3])};
}

__global__ void __launch_bounds__(NTHR, 3) fused_kernel(Params p) {
  extern __shared__ __attribute__((aligned(16))) unsigned char smem[];
  LAS unsigned char* L = (LAS unsigned char*)smem;
  const int tid = threadIdx.x, lane = tid & 63, wave = __builtin_amdgcn_readfirstlane(tid >> 6);
  const int wg = blockIdx.x;
  const unsigned ldsb = (unsigned)(size_t)L;
  STAMP(0);
  if (tid < 32) *(LAS unsigned*)(L + L_CTL + 4 * tid) = 0u;
  const rsrc_t rws = mkr(p.ws, WS_END);
  const int xl = wg & 7, xi = wg >> 3;
  const bool isM = wg < 54, isMc = wg < 18, isQ = wg >= 54 && wg < 246;
  const bool isF = wg < 144;
  const int f = xl * 18 + xi;
  const int wpair = xl + 8 * ((xi - 18) >> 1);
  const bool isW = wg >= 144 && wg < 192 && wpair < 18;
  const int wsig = wpair / 9, wng = 2 * (wpair % 9) + ((xi - 18) & 1);
  const int ahead = wg >> 1, aslot = wg;
  const int ab = ahead >> 4, akv = ahead & 15;
  const int qi = wg - 54, qcg = qi / 3, qkq = qi % 3;
  const int g = lane >> 4, q4 = (lane & 15) >> 2, p4 = lane & 3;

  if (wave < 8) {
    v2u E7 = {0u, 0u};
    if (wave == 7) {
      asm volatile("buffer_load_dwordx2 %0, %1, %2, 0 offen sc1" : "=&v"(E7) : "v"(WS_EPOCH), "s"(rws) : "memory");
      const v4u ss = {SENT, SENT, SENT, SENT};
      if (isM) stsc1(ss, rws, WS_MOD + ((lane >> 3) * NMOD + wg * 64) * 2 + (lane & 7) * 16);
      if (isQ) {
#pragma unroll
        for (int j = 0; j < 2; ++j) stsc1(ss, rws, WS_QP + (((qkq * 8 + 4 * j + (lane >> 4)) * NQ) + qcg * 64) * 4 + (lane & 15) * 16);
      }
      if (lane <= 32) stsc1(ss, rws, lane < 32 ? WS_OP + aslot * 512 + lane * 16 : WS_ML + aslot * 16);
      if (isW) stsc1(ss, rws, WS_YP + ((wsig * 8 + (lane >> 3)) * DM + wng * 32) * 4 + (lane & 7) * 16);
    }
    v4u cvm = {0u, 0u, 0u, 0u};
    if (isMc) cvm = ld_hidden(mkr(p.c, NB * DM * 2), tid * 16);
    if (isMc || isQ) {
      const int stride = isM ? NMOD * 2 : NQ * 2, n = isM ? 9 : 3;
      const rsrc_t rw = isM ? mkr(p.wm, DM * NMOD * 2) : mkr(p.wq, DM * NQ * 2);
      const int cb = isM ? wg * 128 : qcg * 128, r0 = isM ? 0 : qkq * 192;
      const int c16 = lane & 7;
#pragma unroll 1
      for (int j = 0; j < n; ++j) {
        const int i = wave * n + j;
        const int fz = ((lane >> 4) & 1) | ((i & 1) << 1);
        dma16(rw, (lane >> 3) * stride + ((((c16 >> 1) ^ fz) << 1) | (c16 & 1)) * 16 + cb, (r0 + i * 8) * stride, ldsb + L_W + i * 1024);
      }
    }
    auto say_cleared = [&]() {
      if (wave == 7) {
        const unsigned e0 = __builtin_amdgcn_readfirstlane(E7.x), e1 = __builtin_amdgcn_readfirstlane(E7.y);
        if (lane == 0) { __hip_atomic_store((GAS u64*)(p.ws + WS_CLR) + wg, mix64((((u64)e1 << 32) | e0) + 1), __ATOMIC_RELAXED, __HIP_MEMORY_SCOPE_AGENT); stw(L, C_E0, e0); stw(L, C_E1, e1); }
        LGWAIT();
        STAMP(22);
      }
    };
    if (isMc) {
      RAWBAR();
      asm volatile("s_waitcnt vmcnt(9)" : "+v"(cvm), "+v"(E7)::"memory");
      __builtin_amdgcn_sched_barrier(0);
      say_cleared();
      v4u o;
#pragma unroll
      for (int e = 0; e < 4; ++e) o[e] = pk(silu_acc(bflo(cvm[e])), silu_acc(bfhi(cvm[e])));
      *(LAS v4u*)(L + L_SIMG + (tid / 72) * XSTR + (tid % 72) * 16) = o;
      asm volatile("s_waitcnt vmcnt(0) lgkmcnt(0)" ::: "memory");
      lds_add(L, C_DMA0);
    }
    const unsigned char* kb = (const unsigned char*)p.K + ((size_t)wg << 20);
    const unsigned char* vb = (const unsigned char*)p.V + ((size_t)wg << 20);
    const int koff = ((lane >> 2) & 3) * 256 + (g * 4 + (lane & 3)) * 16, voff = lane * 16;
    const int wb = wave * 4096;
    constexpr int ntw = 32;
    const unsigned radd = isW ? K_RA_W : (isMc ? K_RA_MC : (isM ? K_RA_ML : 0));
#define LOADK(j, u) __builtin_amdgcn_raw_buffer_load_b128(mkr(kb, (j) < ntw ? (1u << 20) : 0u), koff, (j) * 32768 + wb + (u) * 1024, AUX_NT)
#define LOADV(j, u) __builtin_amdgcn_raw_buffer_load_b128(mkr(vb, (j) < ntw ? (1u << 20) : 0u), voff, (j) * 32768 + wb + (u) * 1024, AUX_NT)
#define SB() __builtin_amdgcn_sched_barrier(0)
#define PIN(x) do { asm volatile("" : "+v"(x)); SB(); } while (0)
    constexpr int NPK = 2 * (NP + 1);
    v4u ring[8], pre[P];
#pragma unroll
    for (int n = 0; n < 8 + P; ++n) {
      if (n == K_PF && !isMc) { RAWBAR(); asm volatile("s_waitcnt vmcnt(%1)" : "+v"(E7) : "n"(K_PF) : "memory"); __builtin_amdgcn_sched_barrier(0); say_cleared(); lds_add(L, C_DMA0); }
      if (n >= K_PF) throttle(n < K_PFN ? K_PF : K_PF2);
      if (n < 8) ring[n] = LOADK(n >> 2, n & 3); else pre[n - 8] = LOADK(n >> 2, n & 3);
      SB();
    }
    const int nl = isMc ? (K_NL < 1 ? K_NL : 1) : K_NL;
    const int nlk = 2 * nl;
    const int kvl = (isMc ? L_KVM : L_KV) + wave * (nl * 8192);
    {
      const rsrc_t rk = mkr(kb, 1 << 20);
#pragma unroll 1
      for (int n = 0; n < 4 * nlk; ++n) {
        throttle(K_PF2);
        dma16(rk, koff, (NPK + (n >> 2)) * 32768 + wb + (n & 3) * 1024, ldsb + kvl + n * 1024);
      }
    }
    unsigned sel[4];
    {
      const int i = lane & 15, beta = i >> 2, pos = i & 1;
      const unsigned b0 = 2 * ((i >> 1) & 1), b1 = b0 + 1;
#pragma unroll
      for (int w = 0; w < 4; ++w) sel[w] = beta != w ? 0x0c0c0c0cu : (pos ? ((b1 << 24) | (b0 << 16) | 0x0c0cu) : (0x0c0c0000u | (b1 << 8) | b0));
    }
    if (K_PRIO && wave >= 4) __builtin_amdgcn_s_setprio(1);
    STAMP(1);
    RAWBAR();
    STAMP(2);
    v4u qa0, qa1;
    {
      const int i = lane & 15;
      const v4u qf = lds128(L, L_Q + ((i >> 2) & 1) * 256 + (4 * g + (i & 3)) * 16);
      const v4u z = {0u, 0u, 0u, 0u};
      qa0 = (i >> 3) ? z : qf;
      qa1 = (i >> 3) ? qf : z;
    }
    const f4 zero = {0.f, 0.f, 0.f, 0.f};
    const int tn = NPK + nlk;
    const int scw = L_SC + wave * SCW;
    const int sca = scw + ((lane >> 2) & 3) * 16 + (2 * (lane & 1) + (lane >> 5)) * 4 + ((lane >> 4) & 1) * 2;
    const int scs = 16 * (lane & 1);
    float mx = -INFINITY;
    unsigned sv[NPK + 2 * K_NL];
    {
      f4 D0 = mfma(qa0, ring[0], zero); PIN(D0); ring[0] = LOADK(tn, 0); SB();
      D0 = mfma(qa1, ring[1], D0);      PIN(D0); ring[1] = LOADK(tn, 1); SB();
      f4 D1 = mfma(qa0, ring[2], zero); PIN(D1); throttle(K_R); ring[2] = LOADK(tn, 2); SB();
      D1 = mfma(qa1, ring[3], D1);      PIN(D1); throttle(K_R); ring[3] = LOADK(tn, 3); SB();
      sv[0] = score_pair(D0, D1);
      D0 = mfma(qa0, ring[4], zero); D0 = mfma(qa1, ring[5], D0);
      D1 = mfma(qa0, ring[6], zero); D1 = mfma(qa1, ring[7], D1);
      sv[1] = score_pair(D0, D1);
      SB();
#pragma unroll
      for (int t = 2; t < NPK; ++t) {
        D0 = mfma(qa0, pre[4 * t - 8], zero); D0 = mfma(qa1, pre[4 * t - 7], D0);
        D1 = mfma(qa0, pre[4 * t - 6], zero); D1 = mfma(qa1, pre[4 * t - 5], D1);
        sv[t] = score_pair(D0, D1);
      }
      SB();
      VMWAIT(4);
#pragma unroll
      for (int t = 0; t < 2 * K_NL; ++t) {
        sv[NPK + t] = 0xff80ff80u;
        if (t < nlk) {
          const int a = kvl + t * 4096 + lane * 16;
          D0 = mfma(qa0, lds128(L, a), zero); D0 = mfma(qa1, lds128(L, a + 1024), D0);
          D1 = mfma(qa0, lds128(L, a + 2048), zero); D1 = mfma(qa1, lds128(L, a + 3072), D1);
          sv[NPK + t] = score_pair(D0, D1);
        }
      }
      LGWAIT();
      lds_add(L, C_KVL);
      throttle(K_R); ring[4] = LOADK(tn + 1, 0); SB();
      throttle(K_R); ring[5] = LOADK(tn + 1, 1); SB();
      throttle(K_R); ring[6] = LOADK(tn + 1, 2); SB();
      throttle(K_R); ring[7] = LOADK(tn + 1, 3); SB();
      lds_wait(L, C_KVL, 8);
#pragma unroll
      for (int t = 0; t < NPK + 2 * K_NL; ++t) {
        if (t < tn) {
          mx = fmaxf(mx, fmaxf(bflo(sv[t]), bfhi(sv[t])));
          *(LAS u16*)(L + sca + t * 64) = (u16)(sv[t] >> scs);
        }
      }
    }
#pragma unroll 1
    for (int j = tn; j < ntw; j += 2) {
      if (j >= K_TRIG_WO && j < K_TRIG_WO + 2 && lane == 0 && wave == 0) stw(L, C_TRIG, 1u);
      const bool more = j + 2 < ntw;
      const unsigned char* nb = more ? kb : vb;
      const int nj = more ? j + 2 : 0, no = more ? koff : voff;
#define LOADN(jj, u) __builtin_amdgcn_raw_buffer_load_b128(mkr(nb, 1u << 20), no, (jj) * 32768 + wb + (u) * 1024, AUX_NT)
      f4 D0 = mfma(qa0, ring[0], zero); PIN(D0); throttle(rdigit(K_RPAT, 0) + (radd > 0 ? 1u : 0u)); ring[0] = LOADN(nj, 0); SB();
      D0 = mfma(qa1, ring[1], D0);      PIN(D0); throttle(rdigit(K_RPAT, 1) + (radd > 1 ? 1u : 0u)); ring[1] = LOADN(nj, 1); SB();
      f4 D1 = mfma(qa0, ring[2], zero); PIN(D1); throttle(rdigit(K_RPAT, 2) + (radd > 2 ? 1u : 0u)); ring[2] = LOADN(nj, 2); SB();
      D1 = mfma(qa1, ring[3], D1);      PIN(D1); throttle(rdigit(K_RPAT, 3)); ring[3] = LOADN(nj, 3); SB();
      const unsigned s0 = score_pair(D0, D1);
      mx = fmaxf(mx, fmaxf(bflo(s0), bfhi(s0)));
      *(LAS u16*)(L + sca + j * 64) = (u16)(s0 >> scs);
      D0 = mfma(qa0, ring[4], zero); PIN(D0); throttle(rdigit(K_RPAT, 4)); ring[4] = LOADN(nj + 1, 0); SB();
      D0 = mfma(qa1, ring[5], D0);   PIN(D0); throttle(rdigit(K_RPAT, 5)); ring[5] = LOADN(nj + 1, 1); SB();
      D1 = mfma(qa0, ring[6], zero); PIN(D1); throttle(rdigit(K_RPAT, 6)); ring[6] = LOADN(nj + 1, 2); SB();
      D1 = mfma(qa1, ring[7], D1);   PIN(D1); throttle(rdigit(K_RPAT, 7)); ring[7] = LOADN(nj + 1, 3); SB();
#undef LOADN
      const unsigned s1 = score_pair(D0, D1);
      mx = fmaxf(mx, fmaxf(bflo(s1), bfhi(s1)));
      *(LAS u16*)(L + sca + (j + 1) * 64) = (u16)(s1 >> scs);
    }
    mx = fmaxf(mx, dpp<ROR4>(mx)); mx = fmaxf(mx, dpp<ROR8>(mx)); mx = x32max(mx);
    float m0h, m1h, l0 = 0.f, l1 = 0.f;
    {
      auto t = __builtin_amdgcn_permlane16_swap(asu(mx), asu(mx), false, false);
      m0h = asf(t[0]); m1h = asf(t[1]);
      LGWAIT();
#pragma unroll 3
      for (int i = lane; i < ntw * 16; i += 64) {
        const unsigned s = *(LAS unsigned*)(L + scw + i * 4);
        const float p0 = __builtin_amdgcn_exp2f((bflo(s) - m0h) * LOG2E), p1 = __builtin_amdgcn_exp2f((bfhi(s) - m1h) * LOG2E);
        l0 += p0; l1 += p1;
        *(LAS unsigned*)(L + scw + i * 4) = pk(p0, p1);
      }
      LGWAIT();
    }
    f4 acc = zero;
#pragma unroll 1
    for (int j = 0; j < ntw; j += 2) {
      const bool tail = j + K_TTAIL / 4 >= ntw;
      const v4u pa = lds128(L, scw + j * 64 + g * 16), pb = lds128(L, scw + j * 64 + 64 + g * 16);
      acc = mfma(pfrag(pa.x, sel), ring[0], acc); PIN(acc); throttle(tail ? K_RTAIL : rdigit(K_RPATV, 0) + (radd > 0 ? 1u : 0u)); ring[0] = LOADV(j + 2, 0); SB();
      acc = mfma(pfrag(pa.y, sel), ring[1], acc); PIN(acc); throttle(tail ? K_RTAIL : rdigit(K_RPATV, 1) + (radd > 1 ? 1u : 0u)); ring[1] = LOADV(j + 2, 1); SB();
      acc = mfma(pfrag(pa.z, sel), ring[2], acc); PIN(acc); throttle(tail ? K_RTAIL : rdigit(K_RPATV, 2) + (radd > 2 ? 1u : 0u)); ring[2] = LOADV(j + 2, 2); SB();
      acc = mfma(pfrag(pa.w, sel), ring[3], acc); PIN(acc); throttle(tail ? K_RTAIL : rdigit(K_RPATV, 3)); ring[3] = LOADV(j + 2, 3); SB();
      acc = mfma(pfrag(pb.x, sel), ring[4], acc); PIN(acc); throttle(tail ? K_RTAIL : rdigit(K_RPATV, 4)); ring[4] = LOADV(j + 3, 0); SB();
      acc = mfma(pfrag(pb.y, sel), ring[5], acc); PIN(acc); throttle(tail ? K_RTAIL : rdigit(K_RPATV, 5)); ring[5] = LOADV(j + 3, 1); SB();
      acc = mfma(pfrag(pb.z, sel), ring[6], acc); PIN(acc); throttle(tail ? K_RTAIL : rdigit(K_RPATV, 6)); ring[6] = LOADV(j + 3, 2); SB();
      acc = mfma(pfrag(pb.w, sel), ring[7], acc); PIN(acc); throttle(tail ? K_RTAIL : rdigit(K_RPATV, 7)); ring[7] = LOADV(j + 3, 3); SB();
    }
#undef LOADK
#undef LOADV
    if (lds_add(L, 18) == 7u && lane == 0) stw(L, C_TRIG, 2u);
    STAMP(3);
    l0 = x32sum(x16sum(row16sum(l0))); l1 = x32sum(x16sum(row16sum(l1)));
    LAS float* mg = (LAS float*)(L + scw);
    const int cc = lane & 15;
    *(LAS f2*)(mg + 8 + 8 * cc + 2 * g) = f2{acc.x, acc.y};
    *(LAS f2*)(mg + 8 + 128 + 8 * cc + 2 * g) = f2{acc.z, acc.w};
    if (lane == 0) *(LAS f4*)mg = f4{m0h, l0, m1h, l1};
    LGWAIT();
    if (lds_add(L, C_MRG) == 7u) {
      MEMBAR();
      const int h = lane >> 5, d0 = 4 * (lane & 31);
      LAS float* m0 = (LAS float*)(L + L_SC);
      f2 ml[8]; f4 a[8];
#pragma unroll
      for (int w = 0; w < 8; ++w) { ml[w] = *(LAS f2*)(m0 + w * (SCW / 4) + 2 * h); a[w] = *(LAS f4*)(m0 + w * (SCW / 4) + 8 + 128 * h + d0); }
      float M = ml[0].x;
#pragma unroll
      for (int w = 1; w < 8; ++w) M = fmaxf(M, ml[w].x);
      float Ls = 0.f; f4 O = {0.f, 0.f, 0.f, 0.f};
#pragma unroll
      for (int w = 0; w < 8; ++w) { const float e = __builtin_amdgcn_exp2f((ml[w].x - M) * LOG2E); Ls += e * ml[w].y; O += e * a[w]; }
      const float inv = __builtin_amdgcn_rcpf(Ls);
      const unsigned o0 = pk(O.x * inv, O.y * inv), o1 = pk(O.z * inv, O.w * inv);
      const unsigned n0 = dppu<QX1>(o0), n1 = dppu<QX1>(o1);
      if (!(lane & 1)) stsc1(san4(v4u{o0, o1, n0, n1}), rws, WS_OP + aslot * 512 + (h * 128 + d0) * 2);
      const unsigned M1 = __builtin_amdgcn_readlane(asu(M), 32), L1 = __builtin_amdgcn_readlane(asu(Ls), 32);
      if (lane == 0) stsc1(san4(v4u{asu(M), asu(Ls), M1, L1}), rws, WS_ML + aslot * 16);
      STAMP(4);
    }
  } else {
    RAWBAR();
    const int sw = wave - 8, stid = tid - 512;
    const u64 Es = __hip_atomic_load((GAS u64*)(p.ws + WS_EPOCH), __ATOMIC_RELAXED, __HIP_MEMORY_SCOPE_AGENT);
    const int fz = (q4 >> 1) | ((g & 1) << 1);
    auto mod_mfma = [&]() {
      STAMP(5);
      f4 D[3] = {{0.f, 0.f, 0.f, 0.f}, {0.f, 0.f, 0.f, 0.f}, {0.f, 0.f, 0.f, 0.f}};
#pragma unroll
      for (int ks = 0; ks < 18; ++ks) {
        const v4u wf = trfrag(L, L_W + (32 * ks + 8 * g + q4) * 128 + (sw ^ fz) * 32 + p4 * 8, 4 * 128);
        const v4u sf = lds128(L, L_SIMG + (lane & 7) * XSTR + (32 * ks + 8 * g) * 2);
        D[ks % 3] = mfma(wf, sf, D[ks % 3]);
      }
      const f4 Ds = (D[0] + D[1]) + D[2];
      if ((lane & 15) < 8) stsc1_2(v2u{san(pk(Ds.x, Ds.y)), san(pk(Ds.z, Ds.w))}, rws, WS_MOD + ((lane & 7) * NMOD + wg * 64 + sw * 16 + 4 * g) * 2);
      STAMP(6);
    };
    const u64 tagv = mix64((((u64)__builtin_amdgcn_readfirstlane((unsigned)(Es >> 32)) << 32) | (unsigned)__builtin_amdgcn_readfirstlane((unsigned)Es)) + 1);
    const unsigned tg0 = (unsigned)tagv, tg1 = (unsigned)(tagv >> 32);
    if (isMc) {
      if (sw == 0) {
        const v4u cv = __builtin_amdgcn_raw_buffer_load_b128(mkr(p.c, NB * DM * 2), tid * 16, 0, 0);
        v4u o;
#pragma unroll
        for (int e = 0; e < 4; ++e) o[e] = pk(silu_acc(bflo(cv[e])), silu_acc(bfhi(cv[e])));
        *(LAS v4u*)(L + L_SIMG + (tid / 72) * XSTR + (tid % 72) * 16) = o;
        LGWAIT();
        lds_add(L, C_SYNC1);
      }
      STAMP(23);
      lds_wait(L, C_SYNC1, 1); lds_wait(L, C_DMA0, 8);
      mod_mfma();
    } else if (isQ) {
      const int qb = 2 * sw + (lane >> 5), qj = lane & 31;
      const bool qact = qj < 24;
      const rsrc_t rx = mkr(p.x, NB * DM * 2);
      v4u xv[3];
#pragma unroll
      for (int t = 0; t < 3; ++t) xv[t] = __builtin_amdgcn_raw_buffer_load_b128(rx, qact ? (qb * DM + 192 * t + 8 * qj) * 2 : 0x7ffffff0, 0, 0);
      float xf[3][8];
      float sum = 0.f;
#pragma unroll
      for (int t = 0; t < 3; ++t)
#pragma unroll
        for (int e = 0; e < 4; ++e) { xf[t][2 * e] = bflo(xv[t][e]); xf[t][2 * e + 1] = bfhi(xv[t][e]); sum += xf[t][2 * e] + xf[t][2 * e + 1]; }
      sum = x16sum(row16sum(sum));
      const float mean = sum * (1.f / DM);
      float vs = 0.f;
#pragma unroll
      for (int t = 0; t < 3; ++t)
#pragma unroll
        for (int e = 0; e < 8; ++e) { const float d = xf[t][e] - mean; vs += qact ? d * d : 0.f; }
      vs = x16sum(row16sum(vs));
      const float mb = rb(mean), den = rb(__builtin_sqrtf(rb(rb(vs * (1.f / DM)) + 1e-5f)));
      float ln[8];
#pragma unroll
      for (int e = 0; e < 8; ++e) { const float xe = qkq == 0 ? xf[0][e] : (qkq == 1 ? xf[1][e] : xf[2][e]); ln[e] = rb(rb(xe - mb) / den); }
      lds_wait(L, C_DMA0, 8);
      v4u wf[6];
#pragma unroll
      for (int ks = 0; ks < 6; ++ks) wf[ks] = trfrag(L, L_W + (32 * ks + 8 * g + q4) * 128 + (sw ^ fz) * 32 + p4 * 8, 4 * 128);
      STAMP(29);
      if (!(K_X & 16)) {
      v4u sa, ca;
      const int mo = qact ? WS_MOD + (qb * NMOD + 192 * qkq + 8 * qj) * 2 : 0x7ffffff0;
      if (sw == 0) {
        const int pw = (lane < 3 ? 0 : 6) + 3 * qkq + lane;
        const int fo = lane < 6 ? WS_CLR + pw * 8 : 0x7ffffff0, io = lane < 6 ? WS_MOD + (7 * NMOD + pw * 64 + 62) * 2 : 0x7ffffff0;
        bool cleared = false;
        for (unsigned spins = 0;;) {
          MEMBAR();
          if (!cleared) { const v2u fl = ldsc1_2(rws, fo); cleared = __all(lane >= 6 || (fl.x == tg0 && fl.y == tg1)); }
          else if (__builtin_popcountll(__builtin_amdgcn_ballot_w64(lane < 6 && __builtin_amdgcn_raw_buffer_load_b32(rws, io, 0, AUX_SC1) != SENT)) >= K_H1NEAR) break;
          if (give_up(spins, L)) break;
          __builtin_amdgcn_s_sleep(K_PSL);
        }
        if (lane == 0) stw(L, C_HS, 1u);
      } else lds_wait(L, C_HS, 1);
      for (unsigned spins = 0;;) {
        MEMBAR();
        sa = ldsc1(rws, mo); ca = ldsc1(rws, qact ? mo + DM * 2 : mo);
        if (__all(umax(max4(sa), max4(ca)) != SENT)) break;
        if (give_up(spins, L)) break;
        __builtin_amdgcn_s_sleep(K_PSL);
      }
      STAMP(8);
      v4u h;
#pragma unroll
      for (int e = 0; e < 4; ++e) {
        const unsigned c1 = pk(1.f + bflo(ca[e]), 1.f + bfhi(ca[e]));
        const unsigned m1 = pk(ln[2 * e] * bflo(c1), ln[2 * e + 1] * bfhi(c1));
        h[e] = pk(bflo(m1) + bflo(sa[e]), bfhi(m1) + bfhi(sa[e]));
      }
      if (qact) *(LAS v4u*)(L + L_HIMG + qb * HSTR + qj * 16) = h;
      LGWAIT();
      lds_add(L, C_SYNC1); lds_wait(L, C_SYNC1, 4);
      f4 D0 = {0.f, 0.f, 0.f, 0.f}, D1 = {0.f, 0.f, 0.f, 0.f};
#pragma unroll
      for (int ks = 0; ks < 6; ks += 2) {
        D0 = mfma(wf[ks], lds128(L, L_HIMG + (lane & 7) * HSTR + (32 * ks + 8 * g) * 2), D0);
        D1 = mfma(wf[ks + 1], lds128(L, L_HIMG + (lane & 7) * HSTR + (32 * ks + 32 + 8 * g) * 2), D1);
      }
      if ((lane & 15) < 8) stsc1(san4(__builtin_bit_cast(v4u, D0 + D1)), rws, WS_QP + ((qkq * 8 + (lane & 7)) * NQ + qcg * 64 + sw * 16 + 4 * g) * 4);
      STAMP(9);
      }
    }
    if (sw == 0 && !(K_X & 8)) {
      v4u a0, a1, a2;
      const int qo = WS_QP + (ab * NQ + akv * 256 + 4 * lane) * 4;
      const int fo = lane < 12 ? WS_CLR + (54 + 12 * akv + lane) * 8 : 0x7ffffff0;
      const int io = lane < 12 ? WS_QP + (((lane % 3) * 8 + ab) * NQ + (4 * akv + lane / 3) * 64 + 63) * 4 : 0x7ffffff0;
      bool cleared = false;
      for (unsigned spins = 0;;) {
        MEMBAR();
        if (!cleared) { const v2u fl = ldsc1_2(rws, fo); cleared = __all(lane >= 12 || (fl.x == tg0 && fl.y == tg1)); }
        else if (__builtin_popcountll(__builtin_amdgcn_ballot_w64(lane < 12 && __builtin_amdgcn_raw_buffer_load_b32(rws, io, 0, AUX_SC1) != SENT)) >= K_H2NEAR) break;
        if (give_up(spins, L)) break;
        __builtin_amdgcn_s_sleep(K_PSL);
      }
      for (unsigned spins = 0;;) {
        MEMBAR();
        a0 = ldsc1(rws, qo); a1 = ldsc1(rws, qo + NB * NQ * 4); a2 = ldsc1(rws, qo + 2 * NB * NQ * 4);
        if (__all(umax(umax(max4(a0), max4(a1)), max4(a2)) != SENT)) break;
        if (give_up(spins, L)) break;
        __builtin_amdgcn_s_sleep(K_PSL);
      }
      const f4 s = (__builtin_bit_cast(f4, a0) + __builtin_bit_cast(f4, a1)) + __builtin_bit_cast(f4, a2);
      *(LAS v2u*)(L + L_Q + lane * 8) = v2u{pk(s.x, s.y), pk(s.z, s.w)};
      STAMP(10);
    }
    RAWBAR();
    if (isF && sw == 3) {
      const v4u ss = {SENT, SENT, SENT, SENT};
#pragma unroll 3
      for (int j = 0; j < (K_X & 1 ? 0 : 9); ++j) stsc1(ss, rws, WS_DP + ((j * 8 + (lane >> 3)) * 144 + f) * 128 + (lane & 7) * 16);
      VMWAIT(0);
      if (lane == 0) __hip_atomic_store((GAS u64*)(p.ws + WS_CLR2) + f, tagv, __ATOMIC_RELAXED, __HIP_MEMORY_SCOPE_AGENT);
    }
    if (isM && !isMc) {
      lds_wait(L, C_DMA0, 8); lds_wait(L, C_KVL, 8);
      const rsrc_t rw = mkr(p.wm, DM * NMOD * 2);
      const int c16 = lane & 7;
#pragma unroll 1
      for (int j = 0; j < (K_X & 4 ? 0 : 18); ++j) {
        const int i = sw * 18 + j;
        const int fs = ((lane >> 4) & 1) | ((i & 1) << 1);
        dma16(rw, (lane >> 3) * (NMOD * 2) + ((((c16 >> 1) ^ fs) << 1) | (c16 & 1)) * 16 + wg * 128, i * 8 * (NMOD * 2), ldsb + L_W + i * 1024);
        if (K_WOPACE) __builtin_amdgcn_s_sleep(K_WOPACE);
      }
      const rsrc_t rc = mkr(p.c, NB * DM * 2);
#pragma unroll 1
      for (int j = 0; j < 3; ++j) {
        const int ch = stid + 256 * j;
        const v4u cv = __builtin_amdgcn_raw_buffer_load_b128(rc, ch * 16, 0, 0);
        v4u o;
#pragma unroll
        for (int e = 0; e < 4; ++e) o[e] = pk(silu_acc(bflo(cv[e])), silu_acc(bfhi(cv[e])));
        if (ch < 576) *(LAS v4u*)(L + L_SIMG + (ch / 72) * XSTR + (ch % 72) * 16) = o;
      }
      VMWAIT(0); LGWAIT();
      lds_add(L, C_SYNC1); lds_wait(L, C_SYNC1, 4);
      mod_mfma();
    }
    if ((isF || isW) && sw == 3) {
      for (unsigned spins = 0;;) {
        MEMBAR();
        const v4u a = ldsc1(rws, WS_CLR + lane * 32), b = ldsc1(rws, WS_CLR + lane * 32 + 16);
        bool ok = a.x == tg0 && a.y == tg1 && a.z == tg0 && a.w == tg1 && b.x == tg0 && b.y == tg1 && b.z == tg0 && b.w == tg1;
        if (isF) {
          const v4u c2 = ldsc1(rws, lane < 36 ? WS_CLR2 + lane * 32 : 0x7ffffff0), d2 = ldsc1(rws, lane < 36 ? WS_CLR2 + lane * 32 + 16 : 0x7ffffff0);
          ok = ok && (lane >= 36 || (c2.x == tg0 && c2.y == tg1 && c2.z == tg0 && c2.w == tg1 && d2.x == tg0 && d2.y == tg1 && d2.z == tg0 && d2.w == tg1));
        }
        if (__all(ok)) break;
        if (give_up(spins, L)) break;
        __builtin_amdgcn_s_sleep(127);
      }
      if (lane == 0) stw(L, C_HS2, 1u);
      STAMP(24);
    }
    if (isW || isF) {
      if (isM && !isMc) { lds_add(L, 17); lds_wait(L, 17, 4); }
      lds_wait(L, C_KVL, 8);
      const int sc = ((lane & 3) ^ ((lane >> 4) & 2)) * 16;
      if (isW) {
        const rsrc_t rw = mkr(p.wo, NQ * DM * 2);
        const int vo = (lane >> 2) * (DM * 2) + sc + wng * 64;
        lds_wait<16>(L, C_TRIG, 1);
        STAMP(11);
#pragma unroll 1
        for (int j = 0; j < (K_X & 2 ? 0 : K_WOEARLY); ++j) { const int i = j * 4 + sw; dma16<K_WNT != 0>(rw, vo, (wsig * 2048 + i * 16) * DM * 2, ldsb + L_W + i * 1024); if (K_WOPACE) __builtin_amdgcn_s_sleep(K_WOPACE); }
        lds_wait<4>(L, C_TRIG, 2);
#pragma unroll 1
        for (int j = K_WOEARLY; j < 32; ++j) { const int i = j * 4 + sw; dma16<K_WNT != 0>(rw, vo, (wsig * 2048 + i * 16) * DM * 2, ldsb + L_W + i * 1024); }
      } else {
        const rsrc_t rg = mkr(p.wg, DM * DFF * 2), ru = mkr(p.wu, DM * DFF * 2), rd = mkr(p.wd, DFF * DM * 2);
        const int vo = (lane >> 2) * (DFF * 2) + sc + f * 64;
        lds_wait<4>(L, C_TRIG, K_FFNEARLY ? 1 : 2);
        STAMP(11);
        for (int k = 0; k < K_FFNDELAY; ++k) __builtin_amdgcn_s_sleep(16);
        if (K_FFNEARLY == 2) {
#pragma unroll 1
          for (int j = 0; j < 9; ++j) { const int i = j * 4 + sw; dma16(rd, lane * 16, f * 32 * DM * 2 + i * 1024, ldsb + L_WD + i * 1024); __builtin_amdgcn_s_sleep(K_FPACE); }
          lds_wait<4>(L, C_TRIG, 2);
        }
#pragma unroll 1
        for (int j = 0; j < 9; ++j) {
          const int i = j * 4 + sw;
          throttle(K_DMAR); dma16<K_WNT != 0>(rg, vo, i * 16 * DFF * 2, ldsb + L_W + i * 1024); if (K_FFNEARLY == 1) __builtin_amdgcn_s_sleep(K_FPACE);
          throttle(K_DMAR); dma16<K_WNT != 0>(ru, vo, i * 16 * DFF * 2, ldsb + L_WU + i * 1024); if (K_FFNEARLY == 1) __builtin_amdgcn_s_sleep(K_FPACE);
        }
        if (K_FFNEARLY != 2) {
#pragma unroll 1
          for (int j = 0; j < (K_DLATE ? 0 : 9); ++j) {
            if (!K_DLATE) {
              throttle(K_DMAR);
              if (j == (K_DMAR < 8 ? K_DMAR - 1 : 8)) { if (K_DMAR >= 8) VMWAIT(8); lds_add(L, C_DMA1); }
            }
            const int i = j * 4 + sw; dma16(rd, lane * 16, f * 32 * DM * 2 + i * 1024, ldsb + L_WD + i * 1024); if (K_FFNEARLY) __builtin_amdgcn_s_sleep(K_FPACE);
          }
        }
      }
      VMWAIT(0);
      if (isW || K_FFNEARLY == 2 || K_DLATE) lds_add(L, C_DMA1);
      if (isW || !K_DLATE) lds_add(L, C_DMA2);
      STAMP(12);
    }
  }

  if (isW) {
    const int cc = lane & 15;
    if (wave < 8) {
      const int pid = (cc & 7) * 32 + (wsig * 8 + wave) * 2 + (cc >> 3);
      lds_wait(L, C_HS2, 1);
      lds_wait(L, C_DMA1, 4);
      v4u wf[2][4][2];
#pragma unroll
      for (int hh = 0; hh < 2; ++hh)
#pragma unroll
        for (int ks = 0; ks < 4; ++ks)
#pragma unroll
          for (int nt = 0; nt < 2; ++nt) wf[hh][ks][nt] = trfrag(L, L_W + (wave * 256 + hh * 128 + 32 * ks + 8 * g + q4) * 64 + (nt ^ (g & 1)) * 32 + p4 * 8, 4 * 64);
      v4u ml, fr[2][4];
      for (unsigned spins = 0;;) {
        MEMBAR();
        ml = ldsc1(rws, WS_ML + pid * 16);
        const int nv = __builtin_popcountll(__builtin_amdgcn_ballot_w64(max4(ml) != SENT));
        if (nv >= 4 * K_WNEAR) break;
        if (give_up(spins, L)) break;
        if (nv < 4 * 12) __builtin_amdgcn_s_sleep(K_WSL2); else __builtin_amdgcn_s_sleep(K_WSL);
      }
      STAMP(13);
      for (unsigned spins = 0;;) {
        MEMBAR();
        ml = ldsc1(rws, WS_ML + pid * 16);
#pragma unroll
        for (int hh = 0; hh < 2; ++hh)
#pragma unroll
          for (int ks = 0; ks < 4; ++ks) fr[hh][ks] = ldsc1(rws, WS_OP + pid * 512 + (hh * 128 + 32 * ks + 8 * g) * 2);
        unsigned mx = max4(ml);
#pragma unroll
        for (int hh = 0; hh < 2; ++hh)
#pragma unroll
          for (int ks = 0; ks < 4; ++ks) mx = umax(mx, max4(fr[hh][ks]));
        if (__all(mx != SENT)) break;
        if (give_up(spins, L)) break;
      }
      float wgt[2];
#pragma unroll
      for (int hh = 0; hh < 2; ++hh) {
        const float M = asf(ml[2 * hh]), Lv = asf(ml[2 * hh + 1]);
        const float M2 = dpp<ROR8>(M), L2 = dpp<ROR8>(Lv), Mx = fmaxf(M, M2);
        const float e1 = __builtin_amdgcn_exp2f((M - Mx) * LOG2E) * Lv, e2 = __builtin_amdgcn_exp2f((M2 - Mx) * LOG2E) * L2;
        wgt[hh] = e1 * __builtin_amdgcn_rcpf(e1 + e2);
      }
      STAMP(14);
#pragma unroll
      for (int nt = 0; nt < 2; ++nt) {
        f4 d0 = {0.f, 0.f, 0.f, 0.f}, d1 = {0.f, 0.f, 0.f, 0.f};
#pragma unroll
        for (int ks = 0; ks < 4; ++ks) { d0 = mfma(wf[0][ks][nt], fr[0][ks], d0); d1 = mfma(wf[1][ks][nt], fr[1][ks], d1); }
        f4 t = wgt[0] * d0 + wgt[1] * d1;
        t.x += dpp<ROR8>(t.x); t.y += dpp<ROR8>(t.y); t.z += dpp<ROR8>(t.z); t.w += dpp<ROR8>(t.w);
        if (cc < 8) *(LAS f4*)(L + L_RED + (((wave * 2 + nt) * 4 + g) * 8 + cc) * 16) = t;
      }
      LGWAIT();
      if (lds_add(L, C_SYNC1 + 16) == 7u) {
        MEMBAR();
        f4 s = {0.f, 0.f, 0.f, 0.f};
#pragma unroll
        for (int w = 0; w < 8; ++w) s += *(LAS f4*)(L + L_RED + (w * 64 + lane) * 16);
        const int nt = lane >> 5, rr = (lane >> 3) & 3, b = lane & 7;
        stsc1(san4(__builtin_bit_cast(v4u, s)), rws, WS_YP + ((wsig * 8 + b) * DM + wng * 32 + nt * 16 + 4 * rr) * 4);
        STAMP(15);
      }
    }
  } else if (isF) {
    const bool isR = f < 72;
    const int rb_ = f / 9, rc0 = (f % 9) * 64;
    unsigned gbw = 0;
    const int gt = wave & 3, kt = wave >> 2;
    v4u wf[6], wd[4];
    const int dw = wave - 3;
    bool have_wd = false;
    auto read_wd = [&]() {
      lds_wait(L, C_DMA2, 4);
#pragma unroll
      for (int j = 0; j < 4; ++j) wd[j] = trfrag(L, L_WD + (8 * g + q4) * (DM * 2) + (dw >= 0 ? 4 * dw + j : 0) * 32 + p4 * 8, 4 * DM * 2);
    };
    if (wave < 8) {
      const int b = wave;
      const bool act = lane < 48;
      const rsrc_t rx = mkr(p.x, NB * DM * 2);
      v2u xv[3], ga[3], sb[3], cb[3];
      lds_wait(L, C_HS2, 1);
#pragma unroll
      for (int t = 0; t < 3; ++t) xv[t] = __builtin_amdgcn_raw_buffer_load_b64(rx, act ? (b * DM + 4 * (lane + 48 * t)) * 2 : 0x7ffffff0, 0, 0);
      for (unsigned spins = 0;;) {
        MEMBAR();
        unsigned mx = 0;
#pragma unroll
        for (int t = 0; t < 3; ++t) {
          const int o = act ? WS_MOD + (b * NMOD + 4 * (lane + 48 * t)) * 2 : 0x7ffffff0;
          ga[t] = ldsc1_2(rws, act ? o + 2 * DM * 2 : o); sb[t] = ldsc1_2(rws, act ? o + 3 * DM * 2 : o); cb[t] = ldsc1_2(rws, act ? o + 4 * DM * 2 : o);
          mx = umax(mx, umax(umax(umax(ga[t].x, ga[t].y), umax(sb[t].x, sb[t].y)), umax(cb[t].x, cb[t].y)));
        }
        if (wave == 0) { gbw = __builtin_amdgcn_raw_buffer_load_b32(rws, isR ? WS_MOD + (rb_ * NMOD + 5 * DM + rc0 + (lane & ~1)) * 2 : 0x7ffffff0, 0, AUX_SC1); mx = umax(mx, gbw); }
        if (__all(mx != SENT)) break;
        if (give_up(spins, L)) break;
      }
      float xf[12], gf[12], cf[12], sf[12];
#pragma unroll
      for (int t = 0; t < 3; ++t)
#pragma unroll
        for (int e = 0; e < 2; ++e) {
          xf[4 * t + 2 * e] = bflo(xv[t][e]); xf[4 * t + 2 * e + 1] = bfhi(xv[t][e]);
          gf[4 * t + 2 * e] = bflo(ga[t][e]); gf[4 * t + 2 * e + 1] = bfhi(ga[t][e]);
          cf[4 * t + 2 * e] = 1.f + bflo(cb[t][e]); cf[4 * t + 2 * e + 1] = 1.f + bfhi(cb[t][e]);
          sf[4 * t + 2 * e] = bflo(sb[t][e]); sf[4 * t + 2 * e + 1] = bfhi(sb[t][e]);
        }
#pragma unroll
      for (int e = 0; e < 12; ++e) asm volatile("" : "+v"(xf[e]), "+v"(gf[e]), "+v"(cf[e]), "+v"(sf[e]));
      lds_wait(L, C_DMA1, 4);
#pragma unroll
      for (int k6 = 0; k6 < 6; ++k6) wf[k6] = trfrag(L, ((gt >> 1) ? L_WU : L_W) + (32 * (kt * 6 + k6) + 8 * g + q4) * 64 + ((gt & 1) ^ (g & 1)) * 32 + p4 * 8, 4 * 64);
      if (wave >= 3 && __builtin_amdgcn_readfirstlane(ldsw(L, C_DMA2)) >= 4u) { read_wd(); have_wd = true; }
      if (wave == 0) {
        for (unsigned spins = 0;;) {
          MEMBAR();
          const unsigned v = __builtin_amdgcn_raw_buffer_load_b32(rws, lane == 0 ? WS_YP + (((f & 1) * 8 + (f & 7)) * DM + (f % 18) * 32) * 4 : 0x7ffffff0, 0, AUX_SC1);
          if (__builtin_amdgcn_readfirstlane(v) != SENT) break;
          if (give_up(spins, L)) break;
          __builtin_amdgcn_s_sleep(K_YSL);
        }
        if (lane == 0) stw(L, C_Y, 1u);
      } else lds_wait(L, C_Y, 1);
      STAMP(16);
      v4u y0[3], y1[3];
      for (unsigned spins = 0;;) {
        MEMBAR();
        unsigned mx = 0;
#pragma unroll
        for (int t = 0; t < 3; ++t) {
          const int o = act ? WS_YP + (b * DM + 4 * (lane + 48 * t)) * 4 : 0x7ffffff0;
          y0[t] = ldsc1(rws, o); y1[t] = ldsc1(rws, act ? o + NB * DM * 4 : o);
        }
#pragma unroll
        for (int t = 0; t < 3; ++t) mx = umax(mx, umax(max4(y0[t]), max4(y1[t])));
        if (__all(mx != SENT)) break;
        if (give_up(spins, L)) break;
      }
      STAMP(17);
      float x1[12];
#pragma unroll
      for (int e = 0; e < 12; ++e) x1[e] = __builtin_fmaf(gf[e], asf(y0[e >> 2][e & 3]) + asf(y1[e >> 2][e & 3]), xf[e]);
      const float sh = asf(__builtin_amdgcn_readfirstlane(asu(x1[0])));
      float s1 = 0.f, s2 = 0.f;
#pragma unroll
      for (int e = 0; e < 12; ++e) { const float d = x1[e] - sh; s1 += d; s2 = __builtin_fmaf(d, d, s2); }
      s1 = act ? s1 : 0.f; s2 = act ? s2 : 0.f;
      s1 += dpp<ROR1>(s1); s2 += dpp<ROR1>(s2); s1 += dpp<ROR2>(s1); s2 += dpp<ROR2>(s2);
      s1 += dpp<ROR4>(s1); s2 += dpp<ROR4>(s2); s1 += dpp<ROR8>(s1); s2 += dpp<ROR8>(s2);
      s1 = x16sum(s1); s2 = x16sum(s2); s1 = x32sum(s1); s2 = x32sum(s2);
      const float dm = s1 * (1.f / DM), var = fmaxf(__builtin_fmaf(-dm, dm, s2 * (1.f / DM)), 0.f);
      const float rstd = __builtin_amdgcn_rsqf(var + 1e-5f), mo = -(sh + dm) * rstd;
#pragma unroll
      for (int t = 0; t < 3; ++t) {
        float hv[4];
#pragma unroll
        for (int e = 0; e < 4; ++e) hv[e] = __builtin_fmaf(__builtin_fmaf(x1[4 * t + e], rstd, mo), cf[4 * t + e], sf[4 * t + e]);
        if (act) {
          *(LAS v2u*)(L + L_H2 + b * XSTR + 8 * (lane + 48 * t)) = v2u{pk(hv[0], hv[1]), pk(hv[2], hv[3])};
          const int col = 4 * (lane + 48 * t) - rc0;
          if (isR && b == rb_ && col >= 0 && col < 64) *(LAS f4*)(L + L_X1S + col * 4) = f4{x1[4 * t], x1[4 * t + 1], x1[4 * t + 2], x1[4 * t + 3]};
        }
      }
    } else {
      lds_wait(L, C_DMA1, 4);
#pragma unroll
      for (int k6 = 0; k6 < 6; ++k6) wf[k6] = trfrag(L, ((gt >> 1) ? L_WU : L_W) + (32 * (kt * 6 + k6) + 8 * g + q4) * 64 + ((gt & 1) ^ (g & 1)) * 32 + p4 * 8, 4 * 64);
      lds_wait(L, C_Y, 1);
      if (K_DLATE) {
        const rsrc_t rd = mkr(p.wd, DFF * DM * 2);
#pragma unroll 1
        for (int j = 0; j < 9; ++j) { const int i = j * 4 + (wave - 8); dma16(rd, lane * 16, f * 32 * DM * 2 + i * 1024, ldsb + L_WD + i * 1024); }
      }
      if (__builtin_amdgcn_readfirstlane(ldsw(L, C_DMA2)) >= 4u) { read_wd(); have_wd = true; }
    }
    RAWBAR();
    STAMP(18);
    {
      f4 D0 = {0.f, 0.f, 0.f, 0.f}, D1 = {0.f, 0.f, 0.f, 0.f};
#pragma unroll
      for (int k6 = 0; k6 < 6; k6 += 2) {
        D0 = mfma(wf[k6], lds128(L, L_H2 + (lane & 7) * XSTR + (32 * (kt * 6 + k6) + 8 * g) * 2), D0);
        D1 = mfma(wf[k6 + 1], lds128(L, L_H2 + (lane & 7) * XSTR + (32 * (kt * 6 + k6 + 1) + 8 * g) * 2), D1);
      }
      if ((lane & 15) < 8) *(LAS f4*)(L + L_GU + (((kt * 4 + gt) * 4 + g) * 8 + (lane & 7)) * 16) = D0 + D1;
      if (K_DLATE && wave >= 8) { VMWAIT(0); lds_add(L, C_DMA2); }
    }
    RAWBAR();
    STAMP(7);
    if (!have_wd && wave >= 4) read_wd();
    if (tid < 256) {
      const int b = tid & 7, fc = tid >> 3;
      const int o = (((fc >> 4) * 4 + ((fc >> 2) & 3)) * 8 + b) * 16 + (fc & 3) * 4;
      float gs = 0.f, us = 0.f;
#pragma unroll
      for (int k3 = 0; k3 < 3; ++k3) { gs += *(LAS float*)(L + L_GU + k3 * 2048 + o); us += *(LAS float*)(L + L_GU + k3 * 2048 + 1024 + o); }
      const float a = rb(gelu_t(rb(gs))) * rb(us);
      *(LAS u16*)(L + L_ACT + b * ASTR + fc * 2) = (u16)pk(a, a);
      if (!have_wd && wave == 3) read_wd();
    }
    RAWBAR();
    STAMP(31);
    if (wave >= 3) {
      const v4u af = lds128(L, L_ACT + (lane & 7) * ASTR + g * 16);
      const int b = lane & 7, hi = (lane >> 3) & 1;
      const f4 z4 = {0.f, 0.f, 0.f, 0.f};
      const f4 T0 = mfma(wd[0], af, z4), T1 = mfma(wd[1], af, z4), T2 = mfma(wd[2], af, z4), T3 = mfma(wd[3], af, z4);
      const f4 Da = hi ? T2 : T0, Db = hi ? T3 : T1;
      stsc1(san4(v4u{pk(Da.x, Da.y), pk(Da.z, Da.w), pk(Db.x, Db.y), pk(Db.z, Db.w)}), rws, WS_DP + ((b * 9 + dw) * 144 + f) * 128 + (hi * 4 + g) * 16);
    }
    STAMP(19);
    if (isR) {
      v4u a, b2;
      const int o = WS_DP + f * 144 * 128 + tid * 16;
      for (unsigned spins = 0;;) {
        MEMBAR();
        a = ldsc1(rws, o); b2 = ldsc1(rws, tid < 384 ? o + 768 * 16 : 0x7ffffff0);
        if (__all(umax(max4(a), max4(b2)) != SENT)) break;
        if (give_up(spins, L)) break;
      }
      STAMP(20);
      float s[8];
#pragma unroll
      for (int e = 0; e < 4; ++e) { s[2 * e] = bflo(a[e]) + bflo(b2[e]); s[2 * e + 1] = bfhi(a[e]) + bfhi(b2[e]); }
#pragma unroll
      for (int e = 0; e < 8; ++e) { float v = s[e]; v += dpp<ROR8>(v); v = x32sum(x16sum(v)); s[e] = v; }
      if (lane < 8) {
        *(LAS f4*)(L + L_RED2 + (wave * 8 + lane) * 32) = f4{s[0], s[1], s[2], s[3]};
        *(LAS f4*)(L + L_RED2 + (wave * 8 + lane) * 32 + 16) = f4{s[4], s[5], s[6], s[7]};
      }
      RAWBAR();
    }
    if (isR && wave == 0) {
      const int ro = (((lane >> 5) * 4 + ((lane >> 2) & 3)) * 8 + ((lane >> 4) & 1) * 4 + (lane & 3)) * 4;
      float dn = 0.f;
#pragma unroll
      for (int w = 0; w < 12; ++w) dn += *(LAS float*)(L + L_RED2 + w * 256 + ro);
      const float gv = (lane & 1) ? bfhi(gbw) : bflo(gbw);
      const float o = *(LAS float*)(L + L_X1S + lane * 4) + rb(gv * rb(dn));
      const unsigned two = pk(o, dpp<QX1>(o));
      if (!(lane & 1)) *(GAS unsigned*)(p.out + rb_ * DM + rc0 + lane) = two;
      if (wg == 0 && lane == 0) {
        const u64 E = ((u64)ldsw(L, C_E1) << 32) | ldsw(L, C_E0);
        __hip_atomic_store((GAS u64*)(p.ws + WS_EPOCH), E + 1, __ATOMIC_RELAXED, __HIP_MEMORY_SCOPE_AGENT);
      }
      STAMP(21);
    }
  }
}

}

extern "C" void kernel_launch(void* const* d_in, const int* in_sizes, int n_in, void* d_out, int out_size, void* d_ws, size_t ws_size, hipStream_t stream) {
  ak::Params p;
  p.x = (const ak::u16*)d_in[0]; p.K = (const ak::u16*)d_in[1]; p.V = (const ak::u16*)d_in[2];
  p.wq = (const ak::u16*)d_in[3]; p.wo = (const ak::u16*)d_in[4];
  p.wg = (const ak::u16*)d_in[6]; p.wu = (const ak::u16*)d_in[7]; p.wd = (const ak::u16*)d_in[8];
  p.c = (const ak::u16*)d_in[9]; p.wm = (const ak::u16*)d_in[10];
  p.out = (ak::u16*)d_out; p.ws = (unsigned char*)d_ws;
  (void)hipFuncSetAttribute((const void*)ak::fused_kernel, hipFuncAttributeMaxDynamicSharedMemorySize, ak::L_END);
  ak::fused_kernel<<<ak::NWG, ak::NTHR, ak::L_END, stream>>>(p);
}
```

```cpp
#include <hip/hip_runtime.h>
#include <stdint.h>

namespace ak {

typedef unsigned short u16;
typedef unsigned long long u64;
typedef unsigned v4u __attribute__((ext_vector_type(4)));
typedef unsigned v2u __attribute__((ext_vector_type(2)));
typedef float f4 __attribute__((ext_vector_type(4)));
typedef float f2 __attribute__((ext_vector_type(2)));
typedef __bf16 bf8 __attribute__((ext_vector_type(8)));
typedef __bf16 bf2 __attribute__((ext_vector_type(2)));
typedef short s4v __attribute__((ext_vector_type(4)));
#define LAS __attribute__((address_space(3)))
#define GAS __attribute__((address_space(1)))
typedef __amdgpu_buffer_rsrc_t rsrc_t;

constexpr int K_RPAT = 23333333;
constexpr int K_RTAIL = 5;
constexpr int K_TTAIL = 40;
constexpr int K_RA_W = 2;
constexpr int K_RA_MC = 1;
constexpr int K_WFB = 1;
constexpr int K_WFB_B = 5;
constexpr int K_WFB_A = 2;
constexpr int K_WFB_B2 = 9;
constexpr int K_WFB_A2 = -2;
constexpr int K_PF = 3;
constexpr int K_NP = 2;
constexpr int K_NL = 2;
constexpr int K_TRIG_WO = 14;
constexpr int K_WOPACE = 20;
constexpr int K_DMAR = 5;

constexpr int NB = 8, DM = 576, DFF = 4608, NMOD = 3456, NQ = 4096;
constexpr int NWG = 256, NTHR = 768;
constexpr int NP = K_NP, P = NP * 8;
__host__ __device__ constexpr unsigned rdigit(unsigned v, int k) { for (int i = 7; i > k; --i) v /= 10; return v % 10; }
constexpr float LOG2E = 1.4426950408889634f;
constexpr float SCALE = 0.08838834764831845f;
constexpr unsigned SENT = 0xFFFFFFFFu;
constexpr int AUX_NT = 2, AUX_SC1 = 16;
constexpr unsigned SPIN_CAP = 1u << 15, LDS_CAP = 1u << 18;

constexpr int WS_EPOCH = 0;
constexpr int WS_CLR = 4096, WS_CLR2 = 6144;
constexpr int WS_MOD = 8192;
constexpr int WS_QP = 65536;
constexpr int WS_OP = WS_QP + 3 * NB * NQ * 4;
constexpr int WS_ML = WS_OP + NWG * 512;
constexpr int WS_YP = WS_ML + NWG * 16;
constexpr int WS_DP = WS_YP + 2 * NB * DM * 4;
constexpr int WS_END = WS_DP + 72 * 144 * 128;

constexpr int L_W = 0;
constexpr int L_SIMG = 73728;
constexpr int L_HIMG = 24576;
constexpr int HSTR = 400;
constexpr int L_WU = 36864, L_WD = 73728;
constexpr int L_GU = 110592;
constexpr int L_ACT = L_GU + 6144;
constexpr int ASTR = 80;
constexpr int L_RED2 = L_ACT + 640;
constexpr int L_X1S = L_RED2 + 3072;
constexpr int L_H2 = L_X1S + 256;
constexpr int XSTR = 1168;
static_assert(L_H2 + 8 * XSTR <= 131072, "ffn lds");
constexpr int L_RED = 131072;
constexpr int SCW = 2304;
constexpr int L_SC = 141312;
constexpr int L_KV = 28672, L_KVM = 83968;
constexpr int L_Q = 160128;
constexpr int L_CTL = L_Q + 512;
constexpr int L_END = L_CTL + 128;
static_assert(L_KV + K_NL * 65536 <= L_Q && L_KVM + 65536 <= L_Q && L_SC + 8 * SCW <= L_Q && L_END <= 163840, "lds map");
enum { C_SYNC1 = 0, C_HS = 1, C_TRIG = 2, C_MRG = 3, C_DEAD = 4, C_DMA0 = 5, C_DMA1 = 6, C_HS2 = 7, C_KVL = 10, C_Y = 11, C_DMA2 = 12, C_E0 = 8, C_E1 = 9, C_RED = 16, C_MDONE = 17, C_END = 18, C_PRG = 19 };

struct Params {
  const u16 *x, *K, *V, *wq, *wo, *wg, *wu, *wd, *c, *wm;
  u16* out;
  unsigned char* ws;
};

__device__ __forceinline__ float asf(unsigned u) { return __builtin_bit_cast(float, u); }
__device__ __forceinline__ unsigned asu(float f) { return __builtin_bit_cast(unsigned, f); }
__device__ __forceinline__ float bflo(unsigned u) { return asf(u << 16); }
__device__ __forceinline__ float bfhi(unsigned u) { return asf(u & 0xffff0000u); }
__device__ __forceinline__ unsigned pk(float a, float b) {
  f2 v = {a, b};
  return __builtin_bit_cast(unsigned, __builtin_convertvector(v, bf2));
}
__device__ __forceinline__ float rb(float a) { return bflo(pk(a, a)); }
__device__ __forceinline__ unsigned san(unsigned u) { return u < 0xFFFFFFFEu ? u : 0xFFFFFFFEu; }
__device__ __forceinline__ v4u san4(v4u v) { return v4u{san(v.x), san(v.y), san(v.z), san(v.w)}; }
__device__ __forceinline__ unsigned umax(unsigned a, unsigned b) { return a > b ? a : b; }
__device__ __forceinline__ unsigned max4(v4u v) { return umax(umax(v.x, v.y), umax(v.z, v.w)); }
template <int C> __device__ __forceinline__ float dpp(float x) {
  return asf((unsigned)__builtin_amdgcn_mov_dpp((int)asu(x), C, 0xf, 0xf, true));
}
template <int C> __device__ __forceinline__ unsigned dppu(unsigned x) { return (unsigned)__builtin_amdgcn_mov_dpp((int)x, C, 0xf, 0xf, true); }
constexpr int ROR1 = 0x121, ROR2 = 0x122, ROR4 = 0x124, ROR8 = 0x128, QX1 = 0xB1, QX2 = 0x4E;
__device__ __forceinline__ float x32max(float x) { auto t = __builtin_amdgcn_permlane32_swap(asu(x), asu(x), false, false); return fmaxf(asf(t[0]), asf(t[1])); }
__device__ __forceinline__ float x32sum(float x) { auto t = __builtin_amdgcn_permlane32_swap(asu(x), asu(x), false, false); return asf(t[0]) + asf(t[1]); }
__device__ __forceinline__ float x16sum(float x) { auto t = __builtin_amdgcn_permlane16_swap(asu(x), asu(x), false, false); return asf(t[0]) + asf(t[1]); }
__device__ __forceinline__ float row16sum(float x) { x += dpp<ROR1>(x); x += dpp<ROR2>(x); x += dpp<ROR4>(x); x += dpp<ROR8>(x); return x; }
__device__ __forceinline__ f4 mfma(v4u a, v4u b, f4 c) {
  return __builtin_amdgcn_mfma_f32_16x16x32_bf16(__builtin_bit_cast(bf8, a), __builtin_bit_cast(bf8, b), c, 0, 0, 0);
}
__device__ __forceinline__ rsrc_t mkr(const void* p, unsigned bytes) { return __builtin_amdgcn_make_buffer_rsrc((void*)p, 0, bytes, 0x00020000); }
__device__ __forceinline__ v4u ldsc1(rsrc_t r, int off) { return __builtin_amdgcn_raw_buffer_load_b128(r, off, 0, AUX_SC1); }
__device__ __forceinline__ v2u ldsc1_2(rsrc_t r, int off) { return __builtin_amdgcn_raw_buffer_load_b64(r, off, 0, AUX_SC1); }
__device__ __forceinline__ void stsc1(v4u v, rsrc_t r, int off) { __builtin_amdgcn_raw_buffer_store_b128(v, r, off, 0, AUX_SC1); }
__device__ __forceinline__ void stsc1_2(v2u v, rsrc_t r, int off) { __builtin_amdgcn_raw_buffer_store_b64(v, r, off, 0, AUX_SC1); }
#define MEMBAR() asm volatile("" ::: "memory")
#define VMWAIT(n) asm volatile("s_waitcnt vmcnt(" #n ")" ::: "memory")
#define LGWAIT() asm volatile("s_waitcnt lgkmcnt(0)" ::: "memory")
#define RAWBAR() asm volatile("s_waitcnt lgkmcnt(0)\n\ts_barrier" ::: "memory")
__device__ __forceinline__ void dma16(rsrc_t r, int voff, unsigned soff, unsigned ldsa) {
  unsigned keep;
  asm volatile("s_mov_b32 %0, m0\n\ts_mov_b32 m0, %1\n\ts_nop 0\n\tbuffer_load_dwordx4 %2, %3, %4 offen nt lds\n\ts_mov_b32 m0, %0" : "=&s"(keep) : "s"(ldsa), "v"(voff), "s"(r), "s"(soff) : "memory");
}
__device__ __forceinline__ v4u ld_hidden(rsrc_t r, int voff) {
  v4u v;
  asm volatile("buffer_load_dwordx4 %0, %1, %2, 0 offen" : "=&v"(v) : "v"(voff), "s"(r) : "memory");
  return v;
}
__device__ __forceinline__ v4u lds128(LAS unsigned char* L, int off) { return *(LAS v4u*)(L + off); }
__device__ __forceinline__ v4u trfrag(LAS unsigned char* L, int a0, int d) {
  s4v lo = __builtin_amdgcn_ds_read_tr16_b64_v4i16((LAS s4v*)(L + a0));
  s4v hi = __builtin_amdgcn_ds_read_tr16_b64_v4i16((LAS s4v*)(L + a0 + d));
  v2u l = __builtin_bit_cast(v2u, lo), h = __builtin_bit_cast(v2u, hi);
  return v4u{l.x, l.y, h.x, h.y};
}
__device__ __forceinline__ unsigned ldsw(LAS unsigned char* L, int w) { return *(volatile LAS unsigned*)(L + L_CTL + 4 * w); }
__device__ __forceinline__ void stw(LAS unsigned char* L, int w, unsigned v) { *(volatile LAS unsigned*)(L + L_CTL + 4 * w) = v; }
__device__ __forceinline__ unsigned lds_add(LAS unsigned char* L, int w) {
  unsigned o = 0;
  if ((threadIdx.x & 63) == 0) o = __hip_atomic_fetch_add((LAS unsigned*)(L + L_CTL + 4 * w), 1u, __ATOMIC_RELAXED, __HIP_MEMORY_SCOPE_WORKGROUP);
  return __builtin_amdgcn_readfirstlane(o);
}
template <int SLP = 1> __device__ __forceinline__ void lds_wait(LAS unsigned char* L, int w, unsigned n) {
  for (unsigned spins = 0; __builtin_amdgcn_readfirstlane(ldsw(L, w)) < n; ++spins) {
    if (__builtin_amdgcn_readfirstlane(ldsw(L, C_DEAD))) break;
    if (spins > LDS_CAP) { stw(L, C_DEAD, 1u); break; }
    __builtin_amdgcn_s_sleep(SLP);
  }
  MEMBAR();
}
__device__ __forceinline__ bool give_up(unsigned& spins, LAS unsigned char* L) {
  if (++spins > SPIN_CAP) { stw(L, C_DEAD, 1u); return true; }
  return (spins & 15u) == 0u && __builtin_amdgcn_readfirstlane(ldsw(L, C_DEAD)) != 0u;
}
__device__ __forceinline__ u64 mix64(u64 z) {
  z = (z ^ (z >> 30)) * 0xBF58476D1CE4E5B9ull;
  z = (z ^ (z >> 27)) * 0x94D049BB133111EBull;
  return z ^ (z >> 31);
}
__device__ __forceinline__ void throttle(unsigned r) {
  asm volatile("s_cmp_gt_u32 %0, 6\n\ts_cbranch_scc1 9f\n\ts_waitcnt vmcnt(5)\n\t"
               "s_cmp_gt_u32 %0, 5\n\ts_cbranch_scc1 9f\n\ts_waitcnt vmcnt(4)\n\t"
               "s_cmp_gt_u32 %0, 4\n\ts_cbranch_scc1 9f\n\ts_waitcnt vmcnt(3)\n\t"
               "s_cmp_gt_u32 %0, 3\n\ts_cbranch_scc1 9f\n\ts_waitcnt vmcnt(2)\n\t"
               "s_cmp_gt_u32 %0, 2\n\ts_cbranch_scc1 9f\n\ts_waitcnt vmcnt(1)\n\t"
               "s_cmp_gt_u32 %0, 1\n\ts_cbranch_scc1 9f\n\ts_waitcnt vmcnt(0)\n9:" ::"s"(r) : "scc", "memory");
}

__device__ __forceinline__ float sigm(float z) { return __builtin_amdgcn_rcpf(1.f + __builtin_amdgcn_exp2f(-LOG2E * z)); }
__device__ __forceinline__ float silu_acc(float a) {
  const float hi = 1.44269502162933349609375f, lo = 1.92596299112661746e-8f;
  const float t0 = -a * hi;
  const float r = __builtin_fmaf(-a, lo, __builtin_fmaf(-a, hi, -t0));
  const float e0 = __builtin_amdgcn_exp2f(fminf(t0, 126.f));
  const float d = 1.f + __builtin_fmaf(e0, r * 0.693147180559945f, e0);
  const float rc = __builtin_amdgcn_rcpf(d), q = a * rc;
  return __builtin_fmaf(__builtin_fmaf(-q, d, a), rc, q);
}
__device__ __forceinline__ float gelu_t(float x) { const float z = 0.7978845608028654f * (x + 0.044715f * x * x * x); return x * sigm(2.f * z); }

__device__ __forceinline__ float quadsum(f4 d) {
  float t = dpp<0x00>(d.x);
  t += dpp<0x55>(d.y); t += dpp<0xAA>(d.z); t += dpp<0xFF>(d.w);
  return t;
}
__device__ __forceinline__ unsigned score_pair(f4 D0, f4 D1) {
  const unsigned r1 = pk(quadsum(D0), quadsum(D1));
  return pk(bflo(r1) * SCALE, bfhi(r1) * SCALE);
}
__device__ __forceinline__ v4u pfrag(unsigned v, const unsigned (&sel)[4]) {
  return v4u{__builtin_amdgcn_perm(v, v, sel[0]), __builtin_amdgcn_perm(v, v, sel[1]), __builtin_amdgcn_perm(v, v, sel[2]), __builtin_amdgcn_perm(v, v, sel[3])};
}

__global__ void __launch_bounds__(NTHR, 3) fused_kernel(Params p) {
  extern __shared__ __attribute__((aligned(16))) unsigned char smem[];
  LAS unsigned char* L = (LAS unsigned char*)smem;
  const int tid = threadIdx.x, lane = tid & 63, wave = __builtin_amdgcn_readfirstlane(tid >> 6);
  const int wg = blockIdx.x;
  const unsigned ldsb = (unsigned)(size_t)L;
  if (tid < 32) *(LAS unsigned*)(L + L_CTL + 4 * tid) = 0u;
  const rsrc_t rws = mkr(p.ws, WS_END);
  const int xl = wg & 7, xi = wg >> 3;
  const bool isM = wg < 54, isMc = wg < 18, isQ = wg >= 54 && wg < 246;
  const bool isF = wg < 144;
  const int f = xl * 18 + xi;
  const int wpair = xl + 8 * ((xi - 18) >> 1);
  const bool isW = wg >= 144 && wg < 192 && wpair < 18;
  const int wsig = wpair / 9, wng = 2 * (wpair % 9) + ((xi - 18) & 1);
  const int ahead = wg >> 1, aslot = wg;
  const int ab = ahead >> 4, akv = ahead & 15;
  const int qi = wg - 54, qcg = qi / 3, qkq = qi % 3;
  const int g = lane >> 4, q4 = (lane & 15) >> 2, p4 = lane & 3;

  if (wave < 8) {
    v2u E7 = {0u, 0u};
    if (wave == 7) {
      asm volatile("buffer_load_dwordx2 %0, %1, %2, 0 offen sc1" : "=&v"(E7) : "v"(WS_EPOCH), "s"(rws) : "memory");
      const v4u ss = {SENT, SENT, SENT, SENT};
      if (isM) stsc1(ss, rws, WS_MOD + ((lane >> 3) * NMOD + wg * 64) * 2 + (lane & 7) * 16);
      if (isQ) {
#pragma unroll
        for (int j = 0; j < 2; ++j) stsc1(ss, rws, WS_QP + (((qkq * 8 + 4 * j + (lane >> 4)) * NQ) + qcg * 64) * 4 + (lane & 15) * 16);
      }
      if (lane <= 32) stsc1(ss, rws, lane < 32 ? WS_OP + aslot * 512 + lane * 16 : WS_ML + aslot * 16);
      if (isW) stsc1(ss, rws, WS_YP + ((wsig * 8 + (lane >> 3)) * DM + wng * 32) * 4 + (lane & 7) * 16);
    }
    v4u cvm = {0u, 0u, 0u, 0u};
    if (isMc) cvm = ld_hidden(mkr(p.c, NB * DM * 2), tid * 16);
    if (isMc || isQ) {
      const int stride = isM ? NMOD * 2 : NQ * 2, n = isM ? 9 : 3;
      const rsrc_t rw = isM ? mkr(p.wm, DM * NMOD * 2) : mkr(p.wq, DM * NQ * 2);
      const int cb = isM ? wg * 128 : qcg * 128, r0 = isM ? 0 : qkq * 192;
      const int c16 = lane & 7;
#pragma unroll 1
      for (int j = 0; j < n; ++j) {
        const int i = wave * n + j;
        const int fz = ((lane >> 4) & 1) | ((i & 1) << 1);
        dma16(rw, (lane >> 3) * stride + ((((c16 >> 1) ^ fz) << 1) | (c16 & 1)) * 16 + cb, (r0 + i * 8) * stride, ldsb + L_W + i * 1024);
      }
    }
    auto say_cleared = [&]() {
      if (wave == 7) {
        const unsigned e0 = __builtin_amdgcn_readfirstlane(E7.x), e1 = __builtin_amdgcn_readfirstlane(E7.y);
        if (lane == 0) { __hip_atomic_store((GAS u64*)(p.ws + WS_CLR) + wg, mix64((((u64)e1 << 32) | e0) + 1), __ATOMIC_RELAXED, __HIP_MEMORY_SCOPE_AGENT); stw(L, C_E0, e0); stw(L, C_E1, e1); }
        LGWAIT();
      }
    };
    if (isMc) {
      RAWBAR();
      asm volatile("s_waitcnt vmcnt(9)" : "+v"(cvm), "+v"(E7)::"memory");
      __builtin_amdgcn_sched_barrier(0);
      say_cleared();
      v4u o;
#pragma unroll
      for (int e = 0; e < 4; ++e) o[e] = pk(silu_acc(bflo(cvm[e])), silu_acc(bfhi(cvm[e])));
      *(LAS v4u*)(L + L_SIMG + (tid / 72) * XSTR + (tid % 72) * 16) = o;
      asm volatile("s_waitcnt vmcnt(0) lgkmcnt(0)" ::: "memory");
      lds_add(L, C_DMA0);
    }
    const unsigned char* kb = (const unsigned char*)p.K + ((size_t)wg << 20);
    const unsigned char* vb = (const unsigned char*)p.V + ((size_t)wg << 20);
    const int koff = ((lane >> 2) & 3) * 256 + (g * 4 + (lane & 3)) * 16, voff = lane * 16;
    const int wb = wave * 4096;
    constexpr int ntw = 32;
    const int radd0 = isW ? K_RA_W : (isMc ? K_RA_MC : 0);
    int radd = radd0, step = 0;
    auto pace = [&]() {
      if (K_WFB) {
        const int pg = (int)lds_add(L, C_PRG) - 8 * step;
        radd = radd0 + (pg > K_WFB_B2 ? 2 : (pg > K_WFB_B ? 1 : (pg < K_WFB_A2 ? -2 : (pg < K_WFB_A ? -1 : 0))));
        ++step;
      }
    };
#define LOADK(j, u) __builtin_amdgcn_raw_buffer_load_b128(mkr(kb, (j) < ntw ? (1u << 20) : 0u), koff, (j) * 32768 + wb + (u) * 1024, AUX_NT)
#define LOADV(j, u) __builtin_amdgcn_raw_buffer_load_b128(mkr(vb, (j) < ntw ? (1u << 20) : 0u), voff, (j) * 32768 + wb + (u) * 1024, AUX_NT)
#define SB() __builtin_amdgcn_sched_barrier(0)
#define PIN(x) do { asm volatile("" : "+v"(x)); SB(); } while (0)
    constexpr int NPK = 2 * (NP + 1);
    v4u ring[8], pre[P];
#pragma unroll
    for (int n = 0; n < 8 + P; ++n) {
      if (n == K_PF && !isMc) { RAWBAR(); asm volatile("s_waitcnt vmcnt(%1)" : "+v"(E7) : "n"(K_PF) : "memory"); __builtin_amdgcn_sched_barrier(0); say_cleared(); lds_add(L, C_DMA0); }
      if (n >= K_PF) throttle(K_PF);
      if (n < 8) ring[n] = LOADK(n >> 2, n & 3); else pre[n - 8] = LOADK(n >> 2, n & 3);
      SB();
    }
    const int nl = isMc ? 1 : K_NL;
    const int nlk = 2 * nl;
    const int kvl = (isMc ? L_KVM : L_KV) + wave * (nl * 8192);
    {
      const rsrc_t rk = mkr(kb, 1 << 20);
#pragma unroll 1
      for (int n = 0; n < 4 * nlk; ++n) {
        throttle(K_PF);
        dma16(rk, koff, (NPK + (n >> 2)) * 32768 + wb + (n & 3) * 1024, ldsb + kvl + n * 1024);
      }
    }
    unsigned sel[4];
    {
      const int i = lane & 15, beta = i >> 2, pos = i & 1;
      const unsigned b0 = 2 * ((i >> 1) & 1), b1 = b0 + 1;
#pragma unroll
      for (int w = 0; w < 4; ++w) sel[w] = beta != w ? 0x0c0c0c0cu : (pos ? ((b1 << 24) | (b0 << 16) | 0x0c0cu) : (0x0c0c0000u | (b1 << 8) | b0));
    }
    RAWBAR();
    v4u qa0, qa1;
    {
      const int i = lane & 15;
      const v4u qf = lds128(L, L_Q + ((i >> 2) & 1) * 256 + (4 * g + (i & 3)) * 16);
      const v4u z = {0u, 0u, 0u, 0u};
      qa0 = (i >> 3) ? z : qf;
      qa1 = (i >> 3) ? qf : z;
    }
    const f4 zero = {0.f, 0.f, 0.f, 0.f};
    const int tn = NPK + nlk;
    const int scw = L_SC + wave * SCW;
    const int sca = scw + ((lane >> 2) & 3) * 16 + (2 * (lane & 1) + (lane >> 5)) * 4 + ((lane >> 4) & 1) * 2;
    const int scs = 16 * (lane & 1);
    float mx = -INFINITY;
    unsigned sv[NPK + 2 * K_NL];
    {
      f4 D0 = mfma(qa0, ring[0], zero); PIN(D0); ring[0] = LOADK(tn, 0); SB();
      D0 = mfma(qa1, ring[1], D0);      PIN(D0); ring[1] = LOADK(tn, 1); SB();
      f4 D1 = mfma(qa0, ring[2], zero); PIN(D1); throttle(3); ring[2] = LOADK(tn, 2); SB();
      D1 = mfma(qa1, ring[3], D1);      PIN(D1); throttle(3); ring[3] = LOADK(tn, 3); SB();
      sv[0] = score_pair(D0, D1);
      D0 = mfma(qa0, ring[4], zero); D0 = mfma(qa1, ring[5], D0);
      D1 = mfma(qa0, ring[6], zero); D1 = mfma(qa1, ring[7], D1);
      sv[1] = score_pair(D0, D1);
      SB();
#pragma unroll
      for (int t = 2; t < NPK; ++t) {
        D0 = mfma(qa0, pre[4 * t - 8], zero); D0 = mfma(qa1, pre[4 * t - 7], D0);
        D1 = mfma(qa0, pre[4 * t - 6], zero); D1 = mfma(qa1, pre[4 * t - 5], D1);
        sv[t] = score_pair(D0, D1);
      }
      SB();
      VMWAIT(4);
#pragma unroll
      for (int t = 0; t < 2 * K_NL; ++t) {
        sv[NPK + t] = 0xff80ff80u;
        if (t < nlk) {
          const int a = kvl + t * 4096 + lane * 16;
          D0 = mfma(qa0, lds128(L, a), zero); D0 = mfma(qa1, lds128(L, a + 1024), D0);
          D1 = mfma(qa0, lds128(L, a + 2048), zero); D1 = mfma(qa1, lds128(L, a + 3072), D1);
          sv[NPK + t] = score_pair(D0, D1);
        }
      }
      LGWAIT();
      lds_add(L, C_KVL);
      throttle(3); ring[4] = LOADK(tn + 1, 0); SB();
      throttle(3); ring[5] = LOADK(tn + 1, 1); SB();
      throttle(3); ring[6] = LOADK(tn + 1, 2); SB();
      throttle(3); ring[7] = LOADK(tn + 1, 3); SB();
      lds_wait(L, C_KVL, 8);
#pragma unroll
      for (int t = 0; t < NPK + 2 * K_NL; ++t) {
        if (t < tn) {
          mx = fmaxf(mx, fmaxf(bflo(sv[t]), bfhi(sv[t])));
          *(LAS u16*)(L + sca + t * 64) = (u16)(sv[t] >> scs);
        }
      }
    }
#pragma unroll 1
    for (int j = tn; j < ntw; j += 2) {
      if (j >= K_TRIG_WO && j < K_TRIG_WO + 2 && lane == 0 && wave == 0) stw(L, C_TRIG, 1u);
      pace();
      const bool more = j + 2 < ntw;
      const unsigned char* nb = more ? kb : vb;
      const int nj = more ? j + 2 : 0, no = more ? koff : voff;
#define LOADN(jj, u) __builtin_amdgcn_raw_buffer_load_b128(mkr(nb, 1u << 20), no, (jj) * 32768 + wb + (u) * 1024, AUX_NT)
      f4 D0 = mfma(qa0, ring[0], zero); PIN(D0); throttle(rdigit(K_RPAT, 0) + (radd > 0 ? 1u : 0u)); ring[0] = LOADN(nj, 0); SB();
      D0 = mfma(qa1, ring[1], D0);      PIN(D0); throttle(rdigit(K_RPAT, 1)); ring[1] = LOADN(nj, 1); SB();
      f4 D1 = mfma(qa0, ring[2], zero); PIN(D1); throttle(rdigit(K_RPAT, 2) + (radd > 2 ? 1u : 0u)); ring[2] = LOADN(nj, 2); SB();
      D1 = mfma(qa1, ring[3], D1);      PIN(D1); throttle(rdigit(K_RPAT, 3) - (radd < -1 ? 1u : 0u)); ring[3] = LOADN(nj, 3); SB();
      const unsigned s0 = score_pair(D0, D1);
      mx = fmaxf(mx, fmaxf(bflo(s0), bfhi(s0)));
      *(LAS u16*)(L + sca + j * 64) = (u16)(s0 >> scs);
      D0 = mfma(qa0, ring[4], zero); PIN(D0); throttle(rdigit(K_RPAT, 4) + (radd > 1 ? 1u : 0u)); ring[4] = LOADN(nj + 1, 0); SB();
      D0 = mfma(qa1, ring[5], D0);   PIN(D0); throttle(rdigit(K_RPAT, 5)); ring[5] = LOADN(nj + 1, 1); SB();
      D1 = mfma(qa0, ring[6], zero); PIN(D1); throttle(rdigit(K_RPAT, 6) + (radd > 3 ? 1u : 0u)); ring[6] = LOADN(nj + 1, 2); SB();
      D1 = mfma(qa1, ring[7], D1);   PIN(D1); throttle(rdigit(K_RPAT, 7) - (radd < 0 ? 1u : 0u)); ring[7] = LOADN(nj + 1, 3); SB();
#undef LOADN
      const unsigned s1 = score_pair(D0, D1);
      mx = fmaxf(mx, fmaxf(bflo(s1), bfhi(s1)));
      *(LAS u16*)(L + sca + (j + 1) * 64) = (u16)(s1 >> scs);
    }
    mx = fmaxf(mx, dpp<ROR4>(mx)); mx = fmaxf(mx, dpp<ROR8>(mx)); mx = x32max(mx);
    float m0h, m1h, l0 = 0.f, l1 = 0.f;
    {
      auto t = __builtin_amdgcn_permlane16_swap(asu(mx), asu(mx), false, false);
      m0h = asf(t[0]); m1h = asf(t[1]);
      LGWAIT();
#pragma unroll 3
      for (int i = lane; i < ntw * 16; i += 64) {
        const unsigned s = *(LAS unsigned*)(L + scw + i * 4);
        const float p0 = __builtin_amdgcn_exp2f((bflo(s) - m0h) * LOG2E), p1 = __builtin_amdgcn_exp2f((bfhi(s) - m1h) * LOG2E);
        l0 += p0; l1 += p1;
        *(LAS unsigned*)(L + scw + i * 4) = pk(p0, p1);
      }
      l0 = x32sum(x16sum(row16sum(l0))); l1 = x32sum(x16sum(row16sum(l1)));
      LGWAIT();
    }
    f4 acc = zero;
#pragma unroll 1
    for (int j = 0; j < ntw; j += 2) {
      const bool tail = j + K_TTAIL / 4 >= ntw;
      pace();
      const v4u pa = lds128(L, scw + j * 64 + g * 16), pb = lds128(L, scw + j * 64 + 64 + g * 16);
      acc = mfma(pfrag(pa.x, sel), ring[0], acc); PIN(acc); throttle(tail ? K_RTAIL : rdigit(K_RPAT, 0) + (radd > 0 ? 1u : 0u)); ring[0] = LOADV(j + 2, 0); SB();
      acc = mfma(pfrag(pa.y, sel), ring[1], acc); PIN(acc); throttle(tail ? K_RTAIL : rdigit(K_RPAT, 1)); ring[1] = LOADV(j + 2, 1); SB();
      acc = mfma(pfrag(pa.z, sel), ring[2], acc); PIN(acc); throttle(tail ? K_RTAIL : rdigit(K_RPAT, 2) + (radd > 2 ? 1u : 0u)); ring[2] = LOADV(j + 2, 2); SB();
      acc = mfma(pfrag(pa.w, sel), ring[3], acc); PIN(acc); throttle(tail ? K_RTAIL : rdigit(K_RPAT, 3) - (radd < -1 ? 1u : 0u)); ring[3] = LOADV(j + 2, 3); SB();
      acc = mfma(pfrag(pb.x, sel), ring[4], acc); PIN(acc); throttle(tail ? K_RTAIL : rdigit(K_RPAT, 4) + (radd > 1 ? 1u : 0u)); ring[4] = LOADV(j + 3, 0); SB();
      acc = mfma(pfrag(pb.y, sel), ring[5], acc); PIN(acc); throttle(tail ? K_RTAIL : rdigit(K_RPAT, 5)); ring[5] = LOADV(j + 3, 1); SB();
      acc = mfma(pfrag(pb.z, sel), ring[6], acc); PIN(acc); throttle(tail ? K_RTAIL : rdigit(K_RPAT, 6) + (radd > 3 ? 1u : 0u)); ring[6] = LOADV(j + 3, 2); SB();
      acc = mfma(pfrag(pb.w, sel), ring[7], acc); PIN(acc); throttle(tail ? K_RTAIL : rdigit(K_RPAT, 7) - (radd < 0 ? 1u : 0u)); ring[7] = LOADV(j + 3, 3); SB();
    }
#undef LOADK
#undef LOADV
    if (lds_add(L, C_END) == 7u && lane == 0) stw(L, C_TRIG, 2u);
    LAS float* mg = (LAS float*)(L + scw);
    const int cc = lane & 15;
    *(LAS f2*)(mg + 8 + 8 * cc + 2 * g) = f2{acc.x, acc.y};
    *(LAS f2*)(mg + 8 + 128 + 8 * cc + 2 * g) = f2{acc.z, acc.w};
    if (lane == 0) *(LAS f4*)mg = f4{m0h, l0, m1h, l1};
    LGWAIT();
    if (lds_add(L, C_MRG) == 7u) {
      MEMBAR();
      const int h = lane >> 5, d0 = 4 * (lane & 31);
      LAS float* m0 = (LAS float*)(L + L_SC);
      f2 ml[8]; f4 a[8];
#pragma unroll
      for (int w = 0; w < 8; ++w) { ml[w] = *(LAS f2*)(m0 + w * (SCW / 4) + 2 * h); a[w] = *(LAS f4*)(m0 + w * (SCW / 4) + 8 + 128 * h + d0); }
      float M = ml[0].x;
#pragma unroll
      for (int w = 1; w < 8; ++w) M = fmaxf(M, ml[w].x);
      float Ls = 0.f; f4 O = {0.f, 0.f, 0.f, 0.f};
#pragma unroll
      for (int w = 0; w < 8; ++w) { const float e = __builtin_amdgcn_exp2f((ml[w].x - M) * LOG2E); Ls += e * ml[w].y; O += e * a[w]; }
      const float inv = __builtin_amdgcn_rcpf(Ls);
      const unsigned o0 = pk(O.x * inv, O.y * inv), o1 = pk(O.z * inv, O.w * inv);
      const unsigned n0 = dppu<QX1>(o0), n1 = dppu<QX1>(o1);
      if (!(lane & 1)) stsc1(san4(v4u{o0, o1, n0, n1}), rws, WS_OP + aslot * 512 + (h * 128 + d0) * 2);
      const unsigned M1 = __builtin_amdgcn_readlane(asu(M), 32), L1 = __builtin_amdgcn_readlane(asu(Ls), 32);
      if (lane == 0) stsc1(san4(v4u{asu(M), asu(Ls), M1, L1}), rws, WS_ML + aslot * 16);
    }
  } else {
    RAWBAR();
    const int sw = wave - 8, stid = tid - 512;
    const u64 Es = __hip_atomic_load((GAS u64*)(p.ws + WS_EPOCH), __ATOMIC_RELAXED, __HIP_MEMORY_SCOPE_AGENT);
    const int fz = (q4 >> 1) | ((g & 1) << 1);
    auto mod_mfma = [&]() {
      f4 D[3] = {{0.f, 0.f, 0.f, 0.f}, {0.f, 0.f, 0.f, 0.f}, {0.f, 0.f, 0.f, 0.f}};
#pragma unroll
      for (int ks = 0; ks < 18; ++ks) {
        const v4u wf = trfrag(L, L_W + (32 * ks + 8 * g + q4) * 128 + (sw ^ fz) * 32 + p4 * 8, 4 * 128);
        const v4u sf = lds128(L, L_SIMG + (lane & 7) * XSTR + (32 * ks + 8 * g) * 2);
        D[ks % 3] = mfma(wf, sf, D[ks % 3]);
      }
      const f4 Ds = (D[0] + D[1]) + D[2];
      if ((lane & 15) < 8) stsc1_2(v2u{san(pk(Ds.x, Ds.y)), san(pk(Ds.z, Ds.w))}, rws, WS_MOD + ((lane & 7) * NMOD + wg * 64 + sw * 16 + 4 * g) * 2);
    };
    const u64 tagv = mix64((((u64)__builtin_amdgcn_readfirstlane((unsigned)(Es >> 32)) << 32) | (unsigned)__builtin_amdgcn_readfirstlane((unsigned)Es)) + 1);
    const unsigned tg0 = (unsigned)tagv, tg1 = (unsigned)(tagv >> 32);
    if (isMc) {
      if (sw == 0) {
        const v4u cv = __builtin_amdgcn_raw_buffer_load_b128(mkr(p.c, NB * DM * 2), tid * 16, 0, 0);
        v4u o;
#pragma unroll
        for (int e = 0; e < 4; ++e) o[e] = pk(silu_acc(bflo(cv[e])), silu_acc(bfhi(cv[e])));
        *(LAS v4u*)(L + L_SIMG + (tid / 72) * XSTR + (tid % 72) * 16) = o;
        LGWAIT();
        lds_add(L, C_SYNC1);
      }
      lds_wait(L, C_SYNC1, 1); lds_wait(L, C_DMA0, 8);
      mod_mfma();
    } else if (isQ) {
      const int qb = 2 * sw + (lane >> 5), qj = lane & 31;
      const bool qact = qj < 24;
      const rsrc_t rx = mkr(p.x, NB * DM * 2);
      v4u xv[3];
#pragma unroll
      for (int t = 0; t < 3; ++t) xv[t] = __builtin_amdgcn_raw_buffer_load_b128(rx, qact ? (qb * DM + 192 * t + 8 * qj) * 2 : 0x7ffffff0, 0, 0);
      float xf[3][8];
      float sum = 0.f;
#pragma unroll
      for (int t = 0; t < 3; ++t)
#pragma unroll
        for (int e = 0; e < 4; ++e) { xf[t][2 * e] = bflo(xv[t][e]); xf[t][2 * e + 1] = bfhi(xv[t][e]); sum += xf[t][2 * e] + xf[t][2 * e + 1]; }
      sum = x16sum(row16sum(sum));
      const float mean = sum * (1.f / DM);
      float vs = 0.f;
#pragma unroll
      for (int t = 0; t < 3; ++t)
#pragma unroll
        for (int e = 0; e < 8; ++e) { const float d = xf[t][e] - mean; vs += qact ? d * d : 0.f; }
      vs = x16sum(row16sum(vs));
      const float mb = rb(mean), den = rb(__builtin_sqrtf(rb(rb(vs * (1.f / DM)) + 1e-5f)));
      float ln[8];
#pragma unroll
      for (int e = 0; e < 8; ++e) { const float xe = qkq == 0 ? xf[0][e] : (qkq == 1 ? xf[1][e] : xf[2][e]); ln[e] = rb(rb(xe - mb) / den); }
      lds_wait(L, C_DMA0, 8);
      v4u wf[6];
#pragma unroll
      for (int ks = 0; ks < 6; ++ks) wf[ks] = trfrag(L, L_W + (32 * ks + 8 * g + q4) * 128 + (sw ^ fz) * 32 + p4 * 8, 4 * 128);
      v4u sa, ca;
      const int mo = qact ? WS_MOD + (qb * NMOD + 192 * qkq + 8 * qj) * 2 : 0x7ffffff0;
      if (sw == 0) {
        const int pw = (lane < 3 ? 0 : 6) + 3 * qkq + lane;
        const int fo = lane < 6 ? WS_CLR + pw * 8 : 0x7ffffff0, io = lane < 6 ? WS_MOD + (7 * NMOD + pw * 64 + 62) * 2 : 0x7ffffff0;
        bool cleared = false;
        for (unsigned spins = 0;;) {
          MEMBAR();
          if (!cleared) { const v2u fl = ldsc1_2(rws, fo); cleared = __all(lane >= 6 || (fl.x == tg0 && fl.y == tg1)); }
          else if (__all(__builtin_amdgcn_raw_buffer_load_b32(rws, io, 0, AUX_SC1) != SENT)) break;
          if (give_up(spins, L)) break;
          __builtin_amdgcn_s_sleep(1);
        }
        if (lane == 0) stw(L, C_HS, 1u);
      } else lds_wait(L, C_HS, 1);
      for (unsigned spins = 0;;) {
        MEMBAR();
        sa = ldsc1(rws, mo); ca = ldsc1(rws, qact ? mo + DM * 2 : mo);
        if (__all(umax(max4(sa), max4(ca)) != SENT)) break;
        if (give_up(spins, L)) break;
        __builtin_amdgcn_s_sleep(1);
      }
      v4u h;
#pragma unroll
      for (int e = 0; e < 4; ++e) {
        const unsigned c1 = pk(1.f + bflo(ca[e]), 1.f + bfhi(ca[e]));
        const unsigned m1 = pk(ln[2 * e] * bflo(c1), ln[2 * e + 1] * bfhi(c1));
        h[e] = pk(bflo(m1) + bflo(sa[e]), bfhi(m1) + bfhi(sa[e]));
      }
      if (qact) *(LAS v4u*)(L + L_HIMG + qb * HSTR + qj * 16) = h;
      LGWAIT();
      lds_add(L, C_SYNC1); lds_wait(L, C_SYNC1, 4);
      f4 D0 = {0.f, 0.f, 0.f, 0.f}, D1 = {0.f, 0.f, 0.f, 0.f};
#pragma unroll
      for (int ks = 0; ks < 6; ks += 2) {
        D0 = mfma(wf[ks], lds128(L, L_HIMG + (lane & 7) * HSTR + (32 * ks + 8 * g) * 2), D0);
        D1 = mfma(wf[ks + 1], lds128(L, L_HIMG + (lane & 7) * HSTR + (32 * ks + 32 + 8 * g) * 2), D1);
      }
      if ((lane & 15) < 8) stsc1(san4(__builtin_bit_cast(v4u, D0 + D1)), rws, WS_QP + ((qkq * 8 + (lane & 7)) * NQ + qcg * 64 + sw * 16 + 4 * g) * 4);
    }
    if (sw == 0) {
      v4u a0, a1, a2;
      const int qo = WS_QP + (ab * NQ + akv * 256 + 4 * lane) * 4;
      const int fo = lane < 12 ? WS_CLR + (54 + 12 * akv + lane) * 8 : 0x7ffffff0;
      const int io = lane < 12 ? WS_QP + (((lane % 3) * 8 + ab) * NQ + (4 * akv + lane / 3) * 64 + 63) * 4 : 0x7ffffff0;
      bool cleared = false;
      for (unsigned spins = 0;;) {
        MEMBAR();
        if (!cleared) { const v2u fl = ldsc1_2(rws, fo); cleared = __all(lane >= 12 || (fl.x == tg0 && fl.y == tg1)); }
        else if (__all(__builtin_amdgcn_raw_buffer_load_b32(rws, io, 0, AUX_SC1) != SENT)) break;
        if (give_up(spins, L)) break;
        __builtin_amdgcn_s_sleep(1);
      }
      for (unsigned spins = 0;;) {
        MEMBAR();
        a0 = ldsc1(rws, qo); a1 = ldsc1(rws, qo + NB * NQ * 4); a2 = ldsc1(rws, qo + 2 * NB * NQ * 4);
        if (__all(umax(umax(max4(a0), max4(a1)), max4(a2)) != SENT)) break;
        if (give_up(spins, L)) break;
        __builtin_amdgcn_s_sleep(1);
      }
      const f4 s = (__builtin_bit_cast(f4, a0) + __builtin_bit_cast(f4, a1)) + __builtin_bit_cast(f4, a2);
      *(LAS v2u*)(L + L_Q + lane * 8) = v2u{pk(s.x, s.y), pk(s.z, s.w)};
    }
    RAWBAR();
    if (isF && sw == 3) {
      const v4u ss = {SENT, SENT, SENT, SENT};
#pragma unroll 3
      for (int j = 0; j < 9; ++j) stsc1(ss, rws, WS_DP + ((j * 8 + (lane >> 3)) * 144 + f) * 128 + (lane & 7) * 16);
      VMWAIT(0);
      if (lane == 0) __hip_atomic_store((GAS u64*)(p.ws + WS_CLR2) + f, tagv, __ATOMIC_RELAXED, __HIP_MEMORY_SCOPE_AGENT);
    }
    if (isM && !isMc) {
      lds_wait(L, C_DMA0, 8); lds_wait(L, C_KVL, 8);
      const rsrc_t rw = mkr(p.wm, DM * NMOD * 2);
      const int c16 = lane & 7;
#pragma unroll 1
      for (int j = 0; j < 18; ++j) {
        const int i = sw * 18 + j;
        const int fs = ((lane >> 4) & 1) | ((i & 1) << 1);
        dma16(rw, (lane >> 3) * (NMOD * 2) + ((((c16 >> 1) ^ fs) << 1) | (c16 & 1)) * 16 + wg * 128, i * 8 * (NMOD * 2), ldsb + L_W + i * 1024);
        __builtin_amdgcn_s_sleep(K_WOPACE);
      }
      const rsrc_t rc = mkr(p.c, NB * DM * 2);
#pragma unroll 1
      for (int j = 0; j < 3; ++j) {
        const int ch = stid + 256 * j;
        const v4u cv = __builtin_amdgcn_raw_buffer_load_b128(rc, ch * 16, 0, 0);
        v4u o;
#pragma unroll
        for (int e = 0; e < 4; ++e) o[e] = pk(silu_acc(bflo(cv[e])), silu_acc(bfhi(cv[e])));
        if (ch < 576) *(LAS v4u*)(L + L_SIMG + (ch / 72) * XSTR + (ch % 72) * 16) = o;
      }
      VMWAIT(0); LGWAIT();
      lds_add(L, C_SYNC1); lds_wait(L, C_SYNC1, 4);
      mod_mfma();
    }
    if ((isF || isW) && sw == 3) {
      for (unsigned spins = 0;;) {
        MEMBAR();
        const v4u a = ldsc1(rws, WS_CLR + lane * 32), b = ldsc1(rws, WS_CLR + lane * 32 + 16);
        bool ok = a.x == tg0 && a.y == tg1 && a.z == tg0 && a.w == tg1 && b.x == tg0 && b.y == tg1 && b.z == tg0 && b.w == tg1;
        if (isF) {
          const v4u c2 = ldsc1(rws, lane < 36 ? WS_CLR2 + lane * 32 : 0x7ffffff0), d2 = ldsc1(rws, lane < 36 ? WS_CLR2 + lane * 32 + 16 : 0x7ffffff0);
          ok = ok && (lane >= 36 || (c2.x == tg0 && c2.y == tg1 && c2.z == tg0 && c2.w == tg1 && d2.x == tg0 && d2.y == tg1 && d2.z == tg0 && d2.w == tg1));
        }
        if (__all(ok)) break;
        if (give_up(spins, L)) break;
        __builtin_amdgcn_s_sleep(127);
      }
      if (lane == 0) stw(L, C_HS2, 1u);
    }
    if (isW || isF) {
      if (isM && !isMc) { lds_add(L, C_MDONE); lds_wait(L, C_MDONE, 4); }
      lds_wait(L, C_KVL, 8);
      const int sc = ((lane & 3) ^ ((lane >> 4) & 2)) * 16;
      if (isW) {
        const rsrc_t rw = mkr(p.wo, NQ * DM * 2);
        const int vo = (lane >> 2) * (DM * 2) + sc + wng * 64;
        lds_wait<16>(L, C_TRIG, 1);
#pragma unroll 1
        for (int j = 0; j < 32; ++j) { const int i = j * 4 + sw; dma16(rw, vo, (wsig * 2048 + i * 16) * DM * 2, ldsb + L_W + i * 1024); __builtin_amdgcn_s_sleep(K_WOPACE); }
        VMWAIT(0);
        lds_add(L, C_DMA1);
      } else {
        const rsrc_t rg = mkr(p.wg, DM * DFF * 2), ru = mkr(p.wu, DM * DFF * 2), rd = mkr(p.wd, DFF * DM * 2);
        const int vo = (lane >> 2) * (DFF * 2) + sc + f * 64;
        lds_wait<4>(L, C_TRIG, 2);
#pragma unroll 1
        for (int j = 0; j < 9; ++j) {
          const int i = j * 4 + sw;
          throttle(K_DMAR); dma16(rg, vo, i * 16 * DFF * 2, ldsb + L_W + i * 1024);
          throttle(K_DMAR); dma16(ru, vo, i * 16 * DFF * 2, ldsb + L_WU + i * 1024);
        }
#pragma unroll 1
        for (int j = 0; j < 9; ++j) {
          throttle(K_DMAR);
          if (j == K_DMAR - 1) lds_add(L, C_DMA1);
          const int i = j * 4 + sw; dma16(rd, lane * 16, f * 32 * DM * 2 + i * 1024, ldsb + L_WD + i * 1024);
        }
        static_assert(K_DMAR >= 1 && K_DMAR <= 6, "the throttle counts up to six");
        VMWAIT(0);
        lds_add(L, C_DMA2);
      }
    }
  }

  if (isW) {
    const int cc = lane & 15;
    if (wave < 8) {
      const int pid = (cc & 7) * 32 + (wsig * 8 + wave) * 2 + (cc >> 3);
      lds_wait(L, C_HS2, 1);
      lds_wait(L, C_DMA1, 4);
      v4u wf[2][4][2];
#pragma unroll
      for (int hh = 0; hh < 2; ++hh)
#pragma unroll
        for (int ks = 0; ks < 4; ++ks)
#pragma unroll
          for (int nt = 0; nt < 2; ++nt) wf[hh][ks][nt] = trfrag(L, L_W + (wave * 256 + hh * 128 + 32 * ks + 8 * g + q4) * 64 + (nt ^ (g & 1)) * 32 + p4 * 8, 4 * 64);
      v4u ml, fr[2][4];
      for (unsigned spins = 0;;) {
        MEMBAR();
        ml = ldsc1(rws, WS_ML + pid * 16);
        const int nv = __builtin_popcountll(__builtin_amdgcn_ballot_w64(max4(ml) != SENT));
        if (nv >= 4 * 15) break;
        if (give_up(spins, L)) break;
        if (nv < 4 * 12) __builtin_amdgcn_s_sleep(24); else __builtin_amdgcn_s_sleep(1);
      }
      for (unsigned spins = 0;;) {
        MEMBAR();
        ml = ldsc1(rws, WS_ML + pid * 16);
#pragma unroll
        for (int hh = 0; hh < 2; ++hh)
#pragma unroll
          for (int ks = 0; ks < 4; ++ks) fr[hh][ks] = ldsc1(rws, WS_OP + pid * 512 + (hh * 128 + 32 * ks + 8 * g) * 2);
        unsigned mx = max4(ml);
#pragma unroll
        for (int hh = 0; hh < 2; ++hh)
#pragma unroll
          for (int ks = 0; ks < 4; ++ks) mx = umax(mx, max4(fr[hh][ks]));
        if (__all(mx != SENT)) break;
        if (give_up(spins, L)) break;
      }
      float wgt[2];
#pragma unroll
      for (int hh = 0; hh < 2; ++hh) {
        const float M = asf(ml[2 * hh]), Lv = asf(ml[2 * hh + 1]);
        const float M2 = dpp<ROR8>(M), L2 = dpp<ROR8>(Lv), Mx = fmaxf(M, M2);
        const float e1 = __builtin_amdgcn_exp2f((M - Mx) * LOG2E) * Lv, e2 = __builtin_amdgcn_exp2f((M2 - Mx) * LOG2E) * L2;
        wgt[hh] = e1 * __builtin_amdgcn_rcpf(e1 + e2);
      }
#pragma unroll
      for (int nt = 0; nt < 2; ++nt) {
        f4 d0 = {0.f, 0.f, 0.f, 0.f}, d1 = {0.f, 0.f, 0.f, 0.f};
#pragma unroll
        for (int ks = 0; ks < 4; ++ks) { d0 = mfma(wf[0][ks][nt], fr[0][ks], d0); d1 = mfma(wf[1][ks][nt], fr[1][ks], d1); }
        f4 t = wgt[0] * d0 + wgt[1] * d1;
        t.x += dpp<ROR8>(t.x); t.y += dpp<ROR8>(t.y); t.z += dpp<ROR8>(t.z); t.w += dpp<ROR8>(t.w);
        if (cc < 8) *(LAS f4*)(L + L_RED + (((wave * 2 + nt) * 4 + g) * 8 + cc) * 16) = t;
      }
      LGWAIT();
      if (lds_add(L, C_RED) == 7u) {
        MEMBAR();
        f4 s = {0.f, 0.f, 0.f, 0.f};
#pragma unroll
        for (int w = 0; w < 8; ++w) s += *(LAS f4*)(L + L_RED + (w * 64 + lane) * 16);
        const int nt = lane >> 5, rr = (lane >> 3) & 3, b = lane & 7;
        stsc1(san4(__builtin_bit_cast(v4u, s)), rws, WS_YP + ((wsig * 8 + b) * DM + wng * 32 + nt * 16 + 4 * rr) * 4);
      }
    }
  } else if (isF) {
    const bool isR = f < 72;
    const int rb_ = f / 9, rc0 = (f % 9) * 64;
    unsigned gbw = 0;
    const int gt = wave & 3, kt = wave >> 2;
    v4u wf[6], wd[4];
    const int dw = wave - 3;
    bool have_wd = false;
    auto read_wd = [&]() {
      lds_wait(L, C_DMA2, 4);
#pragma unroll
      for (int j = 0; j < 4; ++j) wd[j] = trfrag(L, L_WD + (8 * g + q4) * (DM * 2) + (dw >= 0 ? 4 * dw + j : 0) * 32 + p4 * 8, 4 * DM * 2);
    };
    if (wave < 8) {
      const int b = wave;
      const bool act = lane < 48;
      const rsrc_t rx = mkr(p.x, NB * DM * 2);
      v2u xv[3], ga[3], sb[3], cb[3];
      lds_wait(L, C_HS2, 1);
#pragma unroll
      for (int t = 0; t < 3; ++t) xv[t] = __builtin_amdgcn_raw_buffer_load_b64(rx, act ? (b * DM + 4 * (lane + 48 * t)) * 2 : 0x7ffffff0, 0, 0);
      for (unsigned spins = 0;;) {
        MEMBAR();
        unsigned mx = 0;
#pragma unroll
        for (int t = 0; t < 3; ++t) {
          const int o = act ? WS_MOD + (b * NMOD + 4 * (lane + 48 * t)) * 2 : 0x7ffffff0;
          ga[t] = ldsc1_2(rws, act ? o + 2 * DM * 2 : o); sb[t] = ldsc1_2(rws, act ? o + 3 * DM * 2 : o); cb[t] = ldsc1_2(rws, act ? o + 4 * DM * 2 : o);
          mx = umax(mx, umax(umax(umax(ga[t].x, ga[t].y), umax(sb[t].x, sb[t].y)), umax(cb[t].x, cb[t].y)));
        }
        if (wave == 0) { gbw = __builtin_amdgcn_raw_buffer_load_b32(rws, isR ? WS_MOD + (rb_ * NMOD + 5 * DM + rc0 + (lane & ~1)) * 2 : 0x7ffffff0, 0, AUX_SC1); mx = umax(mx, gbw); }
        if (__all(mx != SENT)) break;
        if (give_up(spins, L)) break;
      }
      float xf[12], gf[12], cf[12], sf[12];
#pragma unroll
      for (int t = 0; t < 3; ++t)
#pragma unroll
        for (int e = 0; e < 2; ++e) {
          xf[4 * t + 2 * e] = bflo(xv[t][e]); xf[4 * t + 2 * e + 1] = bfhi(xv[t][e]);
          gf[4 * t + 2 * e] = bflo(ga[t][e]); gf[4 * t + 2 * e + 1] = bfhi(ga[t][e]);
          cf[4 * t + 2 * e] = 1.f + bflo(cb[t][e]); cf[4 * t + 2 * e + 1] = 1.f + bfhi(cb[t][e]);
          sf[4 * t + 2 * e] = bflo(sb[t][e]); sf[4 * t + 2 * e + 1] = bfhi(sb[t][e]);
        }
#pragma unroll
      for (int e = 0; e < 12; ++e) asm volatile("" : "+v"(xf[e]), "+v"(gf[e]), "+v"(cf[e]), "+v"(sf[e]));
      lds_wait(L, C_DMA1, 4);
#pragma unroll
      for (int k6 = 0; k6 < 6; ++k6) wf[k6] = trfrag(L, ((gt >> 1) ? L_WU : L_W) + (32 * (kt * 6 + k6) + 8 * g + q4) * 64 + ((gt & 1) ^ (g & 1)) * 32 + p4 * 8, 4 * 64);
      if (wave >= 3 && __builtin_amdgcn_readfirstlane(ldsw(L, C_DMA2)) >= 4u) { read_wd(); have_wd = true; }
      if (wave == 0) {
        for (unsigned spins = 0;;) {
          MEMBAR();
          const unsigned v = __builtin_amdgcn_raw_buffer_load_b32(rws, lane == 0 ? WS_YP + (((f & 1) * 8 + (f & 7)) * DM + (f % 18) * 32) * 4 : 0x7ffffff0, 0, AUX_SC1);
          if (__builtin_amdgcn_readfirstlane(v) != SENT) break;
          if (give_up(spins, L)) break;
          __builtin_amdgcn_s_sleep(1);
        }
        if (lane == 0) stw(L, C_Y, 1u);
      } else lds_wait(L, C_Y, 1);
      v4u y0[3], y1[3];
      for (unsigned spins = 0;;) {
        MEMBAR();
        unsigned mx = 0;
#pragma unroll
        for (int t = 0; t < 3; ++t) {
          const int o = act ? WS_YP + (b * DM + 4 * (lane + 48 * t)) * 4 : 0x7ffffff0;
          y0[t] = ldsc1(rws, o); y1[t] = ldsc1(rws, act ? o + NB * DM * 4 : o);
        }
#pragma unroll
        for (int t = 0; t < 3; ++t) mx = umax(mx, umax(max4(y0[t]), max4(y1[t])));
        if (__all(mx != SENT)) break;
        if (give_up(spins, L)) break;
      }
      float x1[12];
#pragma unroll
      for (int e = 0; e < 12; ++e) x1[e] = __builtin_fmaf(gf[e], asf(y0[e >> 2][e & 3]) + asf(y1[e >> 2][e & 3]), xf[e]);
      const float sh = asf(__builtin_amdgcn_readfirstlane(asu(x1[0])));
      float s1 = 0.f, s2 = 0.f;
#pragma unroll
      for (int e = 0; e < 12; ++e) { const float d = x1[e] - sh; s1 += d; s2 = __builtin_fmaf(d, d, s2); }
      s1 = act ? s1 : 0.f; s2 = act ? s2 : 0.f;
      s1 += dpp<ROR1>(s1); s2 += dpp<ROR1>(s2); s1 += dpp<ROR2>(s1); s2 += dpp<ROR2>(s2);
      s1 += dpp<ROR4>(s1); s2 += dpp<ROR4>(s2); s1 += dpp<ROR8>(s1); s2 += dpp<ROR8>(s2);
      s1 = x16sum(s1); s2 = x16sum(s2); s1 = x32sum(s1); s2 = x32sum(s2);
      const float dm = s1 * (1.f / DM), var = fmaxf(__builtin_fmaf(-dm, dm, s2 * (1.f / DM)), 0.f);
      const float rstd = __builtin_amdgcn_rsqf(var + 1e-5f), mo = -(sh + dm) * rstd;
#pragma unroll
      for (int t = 0; t < 3; ++t) {
        float hv[4];
#pragma unroll
        for (int e = 0; e < 4; ++e) hv[e] = __builtin_fmaf(__builtin_fmaf(x1[4 * t + e], rstd, mo), cf[4 * t + e], sf[4 * t + e]);
        if (act) {
          *(LAS v2u*)(L + L_H2 + b * XSTR + 8 * (lane + 48 * t)) = v2u{pk(hv[0], hv[1]), pk(hv[2], hv[3])};
          const int col = 4 * (lane + 48 * t) - rc0;
          if (isR && b == rb_ && col >= 0 && col < 64) *(LAS f4*)(L + L_X1S + col * 4) = f4{x1[4 * t], x1[4 * t + 1], x1[4 * t + 2], x1[4 * t + 3]};
        }
      }
    } else {
      lds_wait(L, C_DMA1, 4);
#pragma unroll
      for (int k6 = 0; k6 < 6; ++k6) wf[k6] = trfrag(L, ((gt >> 1) ? L_WU : L_W) + (32 * (kt * 6 + k6) + 8 * g + q4) * 64 + ((gt & 1) ^ (g & 1)) * 32 + p4 * 8, 4 * 64);
      lds_wait(L, C_Y, 1);
      if (__builtin_amdgcn_readfirstlane(ldsw(L, C_DMA2)) >= 4u) { read_wd(); have_wd = true; }
    }
    RAWBAR();
    {
      f4 D0 = {0.f, 0.f, 0.f, 0.f}, D1 = {0.f, 0.f, 0.f, 0.f};
#pragma unroll
      for (int k6 = 0; k6 < 6; k6 += 2) {
        D0 = mfma(wf[k6], lds128(L, L_H2 + (lane & 7) * XSTR + (32 * (kt * 6 + k6) + 8 * g) * 2), D0);
        D1 = mfma(wf[k6 + 1], lds128(L, L_H2 + (lane & 7) * XSTR + (32 * (kt * 6 + k6 + 1) + 8 * g) * 2), D1);
      }
      if ((lane & 15) < 8) *(LAS f4*)(L + L_GU + (((kt * 4 + gt) * 4 + g) * 8 + (lane & 7)) * 16) = D0 + D1;
    }
    RAWBAR();
    if (!have_wd && wave >= 4) read_wd();
    if (tid < 256) {
      const int b = tid & 7, fc = tid >> 3;
      const int o = (((fc >> 4) * 4 + ((fc >> 2) & 3)) * 8 + b) * 16 + (fc & 3) * 4;
      float gs = 0.f, us = 0.f;
#pragma unroll
      for (int k3 = 0; k3 < 3; ++k3) { gs += *(LAS float*)(L + L_GU + k3 * 2048 + o); us += *(LAS float*)(L + L_GU + k3 * 2048 + 1024 + o); }
      const float a = rb(gelu_t(rb(gs))) * rb(us);
      *(LAS u16*)(L + L_ACT + b * ASTR + fc * 2) = (u16)pk(a, a);
      if (!have_wd && wave == 3) read_wd();
    }
    RAWBAR();
    if (wave >= 3) {
      const v4u af = lds128(L, L_ACT + (lane & 7) * ASTR + g * 16);
      const int b = lane & 7, hi = (lane >> 3) & 1;
      const f4 z4 = {0.f, 0.f, 0.f, 0.f};
      const f4 T0 = mfma(wd[0], af, z4), T1 = mfma(wd[1], af, z4), T2 = mfma(wd[2], af, z4), T3 = mfma(wd[3], af, z4);
      const f4 Da = hi ? T2 : T0, Db = hi ? T3 : T1;
      stsc1(san4(v4u{pk(Da.x, Da.y), pk(Da.z, Da.w), pk(Db.x, Db.y), pk(Db.z, Db.w)}), rws, WS_DP + ((b * 9 + dw) * 144 + f) * 128 + (hi * 4 + g) * 16);
    }
    if (isR) {
      v4u a, b2;
      const int o = WS_DP + f * 144 * 128 + tid * 16;
      for (unsigned spins = 0;;) {
        MEMBAR();
        a = ldsc1(rws, o); b2 = ldsc1(rws, tid < 384 ? o + 768 * 16 : 0x7ffffff0);
        if (__all(umax(max4(a), max4(b2)) != SENT)) break;
        if (give_up(spins, L)) break;
      }
      float s[8];
#pragma unroll
      for (int e = 0; e < 4; ++e) { s[2 * e] = bflo(a[e]) + bflo(b2[e]); s[2 * e + 1] = bfhi(a[e]) + bfhi(b2[e]); }
#pragma unroll
      for (int e = 0; e < 8; ++e) { float v = s[e]; v += dpp<ROR8>(v); v = x32sum(x16sum(v)); s[e] = v; }
      if (lane < 8) {
        *(LAS f4*)(L + L_RED2 + (wave * 8 + lane) * 32) = f4{s[0], s[1], s[2], s[3]};
        *(LAS f4*)(L + L_RED2 + (wave * 8 + lane) * 32 + 16) = f4{s[4], s[5], s[6], s[7]};
      }
      RAWBAR();
    }
    if (isR && wave == 0) {
      const int ro = (((lane >> 5) * 4 + ((lane >> 2) & 3)) * 8 + ((lane >> 4) & 1) * 4 + (lane & 3)) * 4;
      float dn = 0.f;
#pragma unroll
      for (int w = 0; w < 12; ++w) dn += *(LAS float*)(L + L_RED2 + w * 256 + ro);
      const float gv = (lane & 1) ? bfhi(gbw) : bflo(gbw);
      const float o = *(LAS float*)(L + L_X1S + lane * 4) + rb(gv * rb(dn));
      const unsigned two = pk(o, dpp<QX1>(o));
      if (!(lane & 1)) *(GAS unsigned*)(p.out + rb_ * DM + rc0 + lane) = two;
      if (wg == 0 && lane == 0) {
        const u64 E = ((u64)ldsw(L, C_E1) << 32) | ldsw(L, C_E0);
        __hip_atomic_store((GAS u64*)(p.ws + WS_EPOCH), E + 1, __ATOMIC_RELAXED, __HIP_MEMORY_SCOPE_AGENT);
      }
    }
  }
}

}

extern "C" void kernel_launch(void* const* d_in, const int* in_sizes, int n_in, void* d_out, int out_size, void* d_ws, size_t ws_size, hipStream_t stream) {
  ak::Params p;
  p.x = (const ak::u16*)d_in[0]; p.K = (const ak::u16*)d_in[1]; p.V = (const ak::u16*)d_in[2];
  p.wq = (const ak::u16*)d_in[3]; p.wo = (const ak::u16*)d_in[4];
  p.wg = (const ak::u16*)d_in[6]; p.wu = (const ak::u16*)d_in[7]; p.wd = (const ak::u16*)d_in[8];
  p.c = (const ak::u16*)d_in[9]; p.wm = (const ak::u16*)d_in[10];
  p.out = (ak::u16*)d_out; p.ws = (unsigned char*)d_ws;
  (void)hipFuncSetAttribute((const void*)ak::fused_kernel, hipFuncAttributeMaxDynamicSharedMemorySize, ak::L_END);
  ak::fused_kernel<<<ak::NWG, ak::NTHR, ak::L_END, stream>>>(p);
}
```

```cpp
#include <hip/hip_runtime.h>
#include <stdint.h>

namespace ak {

typedef unsigned short u16;
typedef unsigned long long u64;
typedef unsigned v4u __attribute__((ext_vector_type(4)));
typedef unsigned v2u __attribute__((ext_vector_type(2)));
typedef float f4 __attribute__((ext_vector_type(4)));
typedef float f2 __attribute__((ext_vector_type(2)));
typedef __bf16 bf8 __attribute__((ext_vector_type(8)));
typedef __bf16 bf2 __attribute__((ext_vector_type(2)));
typedef short s4v __attribute__((ext_vector_type(4)));
#define LAS __attribute__((address_space(3)))
#define GAS __attribute__((address_space(1)))
typedef __amdgpu_buffer_rsrc_t rsrc_t;

constexpr int K_RPAT = 23333333;
constexpr int K_RTAIL = 5;
constexpr int K_TTAIL = 40;
constexpr int K_RA_W = 2;
constexpr int K_RA_MC = 1;
constexpr int K_WFB = 1;
constexpr int K_WFB_B = 5;
constexpr int K_WFB_A = 2;
constexpr int K_WFB_B2 = 9;
constexpr int K_WFB_A2 = -2;
constexpr int K_PF = 3;
constexpr int K_NP = 2;
constexpr int K_NL = 2;
constexpr int K_TRIG_WO = 14;
constexpr int K_WOPACE = 20;
constexpr int K_DMAR = 5;

constexpr int NB = 8, DM = 576, DFF = 4608, NMOD = 3456, NQ = 4096;
constexpr int NWG = 256, NTHR = 768;
constexpr int NP = K_NP, P = NP * 8;
__host__ __device__ constexpr unsigned rdigit(unsigned v, int k) { for (int i = 7; i > k; --i) v /= 10; return v % 10; }
constexpr float LOG2E = 1.4426950408889634f;
constexpr float SCALE = 0.08838834764831845f;
constexpr unsigned SENT = 0xFFFFFFFFu;
constexpr int AUX_NT = 2, AUX_SC1 = 16;
constexpr unsigned SPIN_CAP = 1u << 15, LDS_CAP = 1u << 18;

constexpr int WS_EPOCH = 0;
constexpr int WS_CLR = 4096, WS_CLR2 = 6144;
constexpr int WS_MOD = 8192;
constexpr int WS_QP = 65536;
constexpr int WS_OP = WS_QP + 3 * NB * NQ * 4;
constexpr int WS_ML = WS_OP + NWG * 512;
constexpr int WS_YP = WS_ML + NWG * 16;
constexpr int WS_DP = WS_YP + 2 * NB * DM * 4;
constexpr int WS_END = WS_DP + 72 * 144 * 128;

constexpr int L_W = 0;
constexpr int L_SIMG = 73728;
constexpr int L_HIMG = 24576;
constexpr int HSTR = 400;
constexpr int L_WU = 36864, L_WD = 73728;
constexpr int L_GU = 110592;
constexpr int L_ACT = L_GU + 6144;
constexpr int ASTR = 80;
constexpr int L_RED2 = L_ACT + 640;
constexpr int L_X1S = L_RED2 + 3072;
constexpr int L_H2 = L_X1S + 256;
constexpr int XSTR = 1168;
static_assert(L_H2 + 8 * XSTR <= 131072, "ffn lds");
constexpr int L_RED = 131072;
constexpr int SCW = 2304;
constexpr int L_SC = 141312;
constexpr int L_KV = 28672, L_KVM = 83968;
constexpr int L_Q = 160128;
constexpr int L_CTL = L_Q + 512;
constexpr int L_END = L_CTL + 128;
static_assert(L_KV + K_NL * 65536 <= L_Q && L_KVM + 65536 <= L_Q && L_SC + 8 * SCW <= L_Q && L_END <= 163840, "lds map");
enum { C_SYNC1 = 0, C_HS = 1, C_TRIG = 2, C_MRG = 3, C_DEAD = 4, C_DMA0 = 5, C_DMA1 = 6, C_HS2 = 7, C_KVL = 10, C_Y = 11, C_DMA2 = 12, C_E0 = 8, C_E1 = 9, C_RED = 16, C_MDONE = 17, C_END = 18, C_PRG = 19 };

struct Params {
  const u16 *x, *K, *V, *wq, *wo, *wg, *wu, *wd, *c, *wm;
  u16* out;
  unsigned char* ws;
};

__device__ __forceinline__ float asf(unsigned u) { return __builtin_bit_cast(float, u); }
__device__ __forceinline__ unsigned asu(float f) { return __builtin_bit_cast(unsigned, f); }
__device__ __forceinline__ float bflo(unsigned u) { return asf(u << 16); }
__device__ __forceinline__ float bfhi(unsigned u) { return asf(u & 0xffff0000u); }
__device__ __forceinline__ unsigned pk(float a, float b) {
  f2 v = {a, b};
  return __builtin_bit_cast(unsigned, __builtin_convertvector(v, bf2));
}
__device__ __forceinline__ float rb(float a) { return bflo(pk(a, a)); }
__device__ __forceinline__ unsigned san(unsigned u) { return u < 0xFFFFFFFEu ? u : 0xFFFFFFFEu; }
__device__ __forceinline__ v4u san4(v4u v) { return v4u{san(v.x), san(v.y), san(v.z), san(v.w)}; }
__device__ __forceinline__ unsigned umax(unsigned a, unsigned b) { return a > b ? a : b; }
__device__ __forceinline__ unsigned max4(v4u v) { return umax(umax(v.x, v.y), umax(v.z, v.w)); }
template <int C> __device__ __forceinline__ float dpp(float x) {
  return asf((unsigned)__builtin_amdgcn_mov_dpp((int)asu(x), C, 0xf, 0xf, true));
}
template <int C> __device__ __forceinline__ unsigned dppu(unsigned x) { return (unsigned)__builtin_amdgcn_mov_dpp((int)x, C, 0xf, 0xf, true); }
constexpr int ROR1 = 0x121, ROR2 = 0x122, ROR4 = 0x124, ROR8 = 0x128, QX1 = 0xB1, QX2 = 0x4E;
__device__ __forceinline__ float x32max(float x) { auto t = __builtin_amdgcn_permlane32_swap(asu(x), asu(x), false, false); return fmaxf(asf(t[0]), asf(t[1])); }
__device__ __forceinline__ float x32sum(float x) { auto t = __builtin_amdgcn_permlane32_swap(asu(x), asu(x), false, false); return asf(t[0]) + asf(t[1]); }
__device__ __forceinline__ float x16sum(float x) { auto t = __builtin_amdgcn_permlane16_swap(asu(x), asu(x), false, false); return asf(t[0]) + asf(t[1]); }
__device__ __forceinline__ float row16sum(float x) { x += dpp<ROR1>(x); x += dpp<ROR2>(x); x += dpp<ROR4>(x); x += dpp<ROR8>(x); return x; }
__device__ __forceinline__ f4 mfma(v4u a, v4u b, f4 c) {
  return __builtin_amdgcn_mfma_f32_16x16x32_bf16(__builtin_bit_cast(bf8, a), __builtin_bit_cast(bf8, b), c, 0, 0, 0);
}
__device__ __forceinline__ rsrc_t mkr(const void* p, unsigned bytes) { return __builtin_amdgcn_make_buffer_rsrc((void*)p, 0, bytes, 0x00020000); }
__device__ __forceinline__ v4u ldsc1(rsrc_t r, int off) { return __builtin_amdgcn_raw_buffer_load_b128(r, off, 0, AUX_SC1); }
__device__ __forceinline__ v2u ldsc1_2(rsrc_t r, int off) { return __builtin_amdgcn_raw_buffer_load_b64(r, off, 0, AUX_SC1); }
__device__ __forceinline__ void stsc1(v4u v, rsrc_t r, int off) { __builtin_amdgcn_raw_buffer_store_b128(v, r, off, 0, AUX_SC1); }
__device__ __forceinline__ void stsc1_2(v2u v, rsrc_t r, int off) { __builtin_amdgcn_raw_buffer_store_b64(v, r, off, 0, AUX_SC1); }
#define MEMBAR() asm volatile("" ::: "memory")
#define VMWAIT(n) asm volatile("s_waitcnt vmcnt(" #n ")" ::: "memory")
#define LGWAIT() asm volatile("s_waitcnt lgkmcnt(0)" ::: "memory")
#define RAWBAR() asm volatile("s_waitcnt lgkmcnt(0)\n\ts_barrier" ::: "memory")
__device__ __forceinline__ void dma16(rsrc_t r, int voff, unsigned soff, unsigned ldsa) {
  unsigned keep;
  asm volatile("s_mov_b32 %0, m0\n\ts_mov_b32 m0, %1\n\ts_nop 2\n\tbuffer_load_dwordx4 %2, %3, %4 offen nt lds\n\ts_mov_b32 m0, %0" : "=&s"(keep) : "s"(ldsa), "v"(voff), "s"(r), "s"(soff) : "memory");
}
__device__ __forceinline__ v4u ld_hidden(rsrc_t r, int voff) {
  v4u v;
  asm volatile("buffer_load_dwordx4 %0, %1, %2, 0 offen" : "=&v"(v) : "v"(voff), "s"(r) : "memory");
  return v;
}
__device__ __forceinline__ v4u lds128(LAS unsigned char* L, int off) { return *(LAS v4u*)(L + off); }
__device__ __forceinline__ v4u trfrag(LAS unsigned char* L, int a0, int d) {
  s4v lo = __builtin_amdgcn_ds_read_tr16_b64_v4i16((LAS s4v*)(L + a0));
  s4v hi = __builtin_amdgcn_ds_read_tr16_b64_v4i16((LAS s4v*)(L + a0 + d));
  v2u l = __builtin_bit_cast(v2u, lo), h = __builtin_bit_cast(v2u, hi);
  return v4u{l.x, l.y, h.x, h.y};
}
__device__ __forceinline__ unsigned ldsw(LAS unsigned char* L, int w) { return *(volatile LAS unsigned*)(L + L_CTL + 4 * w); }
__device__ __forceinline__ void stw(LAS unsigned char* L, int w, unsigned v) { *(volatile LAS unsigned*)(L + L_CTL + 4 * w) = v; }
__device__ __forceinline__ unsigned lds_add(LAS unsigned char* L, int w) {
  unsigned o = 0;
  if ((threadIdx.x & 63) == 0) o = __hip_atomic_fetch_add((LAS unsigned*)(L + L_CTL + 4 * w), 1u, __ATOMIC_RELAXED, __HIP_MEMORY_SCOPE_WORKGROUP);
  return __builtin_amdgcn_readfirstlane(o);
}
template <int SLP = 1> __device__ __forceinline__ void lds_wait(LAS unsigned char* L, int w, unsigned n) {
  for (unsigned spins = 0; __builtin_amdgcn_readfirstlane(ldsw(L, w)) < n; ++spins) {
    if (__builtin_amdgcn_readfirstlane(ldsw(L, C_DEAD))) break;
    if (spins > LDS_CAP) { stw(L, C_DEAD, 1u); break; }
    __builtin_amdgcn_s_sleep(SLP);
  }
  MEMBAR();
}
__device__ __forceinline__ bool give_up(unsigned& spins, LAS unsigned char* L) {
  if (++spins > SPIN_CAP) { stw(L, C_DEAD, 1u); return true; }
  return (spins & 15u) == 0u && __builtin_amdgcn_readfirstlane(ldsw(L, C_DEAD)) != 0u;
}
__device__ __forceinline__ u64 mix64(u64 z) {
  z += 0x9E3779B97F4A7C15ull;
  z = (z ^ (z >> 30)) * 0xBF58476D1CE4E5B9ull;
  z = (z ^ (z >> 27)) * 0x94D049BB133111EBull;
  return z ^ (z >> 31);
}
__device__ __forceinline__ void throttle(unsigned r) {
  asm volatile("s_cmp_gt_u32 %0, 6\n\ts_cbranch_scc1 9f\n\ts_waitcnt vmcnt(5)\n\t"
               "s_cmp_gt_u32 %0, 5\n\ts_cbranch_scc1 9f\n\ts_waitcnt vmcnt(4)\n\t"
               "s_cmp_gt_u32 %0, 4\n\ts_cbranch_scc1 9f\n\ts_waitcnt vmcnt(3)\n\t"
               "s_cmp_gt_u32 %0, 3\n\ts_cbranch_scc1 9f\n\ts_waitcnt vmcnt(2)\n\t"
               "s_cmp_gt_u32 %0, 2\n\ts_cbranch_scc1 9f\n\ts_waitcnt vmcnt(1)\n\t"
               "s_cmp_gt_u32 %0, 1\n\ts_cbranch_scc1 9f\n\ts_waitcnt vmcnt(0)\n9:" ::"s"(r) : "scc", "memory");
}

__device__ __forceinline__ float sigm(float z) { return __builtin_amdgcn_rcpf(1.f + __builtin_amdgcn_exp2f(-LOG2E * z)); }
__device__ __forceinline__ float silu_acc(float a) {
  const float hi = 1.44269502162933349609375f, lo = 1.92596299112661746e-8f;
  const float t0 = -a * hi;
  const float r = __builtin_fmaf(-a, lo, __builtin_fmaf(-a, hi, -t0));
  const float e0 = __builtin_amdgcn_exp2f(fminf(t0, 126.f));
  const float d = 1.f + __builtin_fmaf(e0, r * 0.693147180559945f, e0);
  const float rc = __builtin_amdgcn_rcpf(d), q = a * rc;
  return __builtin_fmaf(__builtin_fmaf(-q, d, a), rc, q);
}
__device__ __forceinline__ float gelu_t(float x) { const float z = 0.7978845608028654f * (x + 0.044715f * x * x * x); return x * sigm(2.f * z); }

__device__ __forceinline__ float quadsum(f4 d) {
  float t = dpp<0x00>(d.x);
  t += dpp<0x55>(d.y); t += dpp<0xAA>(d.z); t += dpp<0xFF>(d.w);
  return t;
}
__device__ __forceinline__ unsigned score_pair(f4 D0, f4 D1) {
  const unsigned r1 = pk(quadsum(D0), quadsum(D1));
  return pk(bflo(r1) * SCALE, bfhi(r1) * SCALE);
}
__device__ __forceinline__ v4u pfrag(unsigned v, const unsigned (&sel)[4]) {
  return v4u{__builtin_amdgcn_perm(v, v, sel[0]), __builtin_amdgcn_perm(v, v, sel[1]), __builtin_amdgcn_perm(v, v, sel[2]), __builtin_amdgcn_perm(v, v, sel[3])};
}

__global__ void __launch_bounds__(NTHR, 3) fused_kernel(Params p) {
  extern __shared__ __attribute__((aligned(16))) unsigned char smem[];
  LAS unsigned char* L = (LAS unsigned char*)smem;
  const int tid = threadIdx.x, lane = tid & 63, wave = __builtin_amdgcn_readfirstlane(tid >> 6);
  const int wg = blockIdx.x;
  const unsigned ldsb = (unsigned)(size_t)L;
  if (tid < 32) *(LAS unsigned*)(L + L_CTL + 4 * tid) = 0u;
  const rsrc_t rws = mkr(p.ws, WS_END);
  const int xl = wg & 7, xi = wg >> 3;
  const bool isM = wg < 54, isMc = wg < 18, isQ = wg >= 54 && wg < 246;
  const bool isF = wg < 144;
  const int f = xl * 18 + xi;
  const int wpair = xl + 8 * ((xi - 18) >> 1);
  const bool isW = wg >= 144 && wg < 192 && wpair < 18;
  const int wsig = wpair / 9, wng = 2 * (wpair % 9) + ((xi - 18) & 1);
  const int ahead = wg >> 1, aslot = wg;
  const int ab = ahead >> 4, akv = ahead & 15;
  const int qi = wg - 54, qcg = qi / 3, qkq = qi % 3;
  const int g = lane >> 4, q4 = (lane & 15) >> 2, p4 = lane & 3;

  if (wave < 8) {
    v2u E7 = {0u, 0u};
    if (wave == 7) {
      asm volatile("buffer_load_dwordx2 %0, %1, %2, 0 offen sc1" : "=&v"(E7) : "v"(WS_EPOCH), "s"(rws) : "memory");
      const v4u ss = {SENT, SENT, SENT, SENT};
      if (isM) stsc1(ss, rws, WS_MOD + ((lane >> 3) * NMOD + wg * 64) * 2 + (lane & 7) * 16);
      if (isQ) {
#pragma unroll
        for (int j = 0; j < 2; ++j) stsc1(ss, rws, WS_QP + (((qkq * 8 + 4 * j + (lane >> 4)) * NQ) + qcg * 64) * 4 + (lane & 15) * 16);
      }
      if (lane <= 32) stsc1(ss, rws, lane < 32 ? WS_OP + aslot * 512 + lane * 16 : WS_ML + aslot * 16);
      if (isW) stsc1(ss, rws, WS_YP + ((wsig * 8 + (lane >> 3)) * DM + wng * 32) * 4 + (lane & 7) * 16);
    }
    v4u cvm = {0u, 0u, 0u, 0u};
    if (isMc) cvm = ld_hidden(mkr(p.c, NB * DM * 2), tid * 16);
    if (isMc || isQ) {
      const int stride = isM ? NMOD * 2 : NQ * 2, n = isM ? 9 : 3;
      const rsrc_t rw = isM ? mkr(p.wm, DM * NMOD * 2) : mkr(p.wq, DM * NQ * 2);
      const int cb = isM ? wg * 128 : qcg * 128, r0 = isM ? 0 : qkq * 192;
      const int c16 = lane & 7;
#pragma unroll 1
      for (int j = 0; j < n; ++j) {
        const int i = wave * n + j;
        const int fz = ((lane >> 4) & 1) | ((i & 1) << 1);
        dma16(rw, (lane >> 3) * stride + ((((c16 >> 1) ^ fz) << 1) | (c16 & 1)) * 16 + cb, (r0 + i * 8) * stride, ldsb + L_W + i * 1024);
      }
    }
    auto say_cleared = [&]() {
      if (wave == 7) {
        const unsigned e0 = __builtin_amdgcn_readfirstlane(E7.x), e1 = __builtin_amdgcn_readfirstlane(E7.y);
        if (lane == 0) { __hip_atomic_store((GAS u64*)(p.ws + WS_CLR) + wg, mix64((((u64)e1 << 32) | e0) + 1), __ATOMIC_RELAXED, __HIP_MEMORY_SCOPE_AGENT); stw(L, C_E0, e0); stw(L, C_E1, e1); }
        LGWAIT();
      }
    };
    if (isMc) {
      RAWBAR();
      asm volatile("s_waitcnt vmcnt(9)" : "+v"(cvm), "+v"(E7)::"memory");
      __builtin_amdgcn_sched_barrier(0);
      say_cleared();
      v4u o;
#pragma unroll
      for (int e = 0; e < 4; ++e) o[e] = pk(silu_acc(bflo(cvm[e])), silu_acc(bfhi(cvm[e])));
      *(LAS v4u*)(L + L_SIMG + (tid / 72) * XSTR + (tid % 72) * 16) = o;
      asm volatile("s_waitcnt vmcnt(0) lgkmcnt(0)" ::: "memory");
      lds_add(L, C_DMA0);
    }
    const unsigned char* kb = (const unsigned char*)p.K + ((size_t)wg << 20);
    const unsigned char* vb = (const unsigned char*)p.V + ((size_t)wg << 20);
    const int koff = ((lane >> 2) & 3) * 256 + (g * 4 + (lane & 3)) * 16, voff = lane * 16;
    const int wb = wave * 4096;
    constexpr int ntw = 32;
    const int radd0 = isW ? K_RA_W : (isMc ? K_RA_MC : 0);
    int radd = radd0, step = 0;
    auto pace = [&]() {
      if (K_WFB) {
        const int pg = (int)lds_add(L, C_PRG) - 8 * step;
        radd = radd0 + (pg > K_WFB_B2 ? 2 : (pg > K_WFB_B ? 1 : (pg < K_WFB_A2 ? -2 : (pg < K_WFB_A ? -1 : 0))));
        ++step;
      }
    };
#define LOADK(j, u) __builtin_amdgcn_raw_buffer_load_b128(mkr(kb, (j) < ntw ? (1u << 20) : 0u), koff, (j) * 32768 + wb + (u) * 1024, AUX_NT)
#define LOADV(j, u) __builtin_amdgcn_raw_buffer_load_b128(mkr(vb, (j) < ntw ? (1u << 20) : 0u), voff, (j) * 32768 + wb + (u) * 1024, AUX_NT)
#define SB() __builtin_amdgcn_sched_barrier(0)
#define PIN(x) do { asm volatile("" : "+v"(x)); SB(); } while (0)
    constexpr int NPK = 2 * (NP + 1);
    v4u ring[8], pre[P];
#pragma unroll
    for (int n = 0; n < 8 + P; ++n) {
      if (n == K_PF && !isMc) { RAWBAR(); asm volatile("s_waitcnt vmcnt(%1)" : "+v"(E7) : "n"(K_PF) : "memory"); __builtin_amdgcn_sched_barrier(0); say_cleared(); lds_add(L, C_DMA0); }
      if (n >= K_PF) throttle(K_PF);
      if (n < 8) ring[n] = LOADK(n >> 2, n & 3); else pre[n - 8] = LOADK(n >> 2, n & 3);
      SB();
    }
    const int nl = isMc ? 1 : K_NL;
    const int nlk = 2 * nl;
    const int kvl = (isMc ? L_KVM : L_KV) + wave * (nl * 8192);
    {
      const rsrc_t rk = mkr(kb, 1 << 20);
#pragma unroll 1
      for (int n = 0; n < 4 * nlk; ++n) {
        throttle(K_PF);
        dma16(rk, koff, (NPK + (n >> 2)) * 32768 + wb + (n & 3) * 1024, ldsb + kvl + n * 1024);
      }
    }
    unsigned sel[4];
    {
      const int i = lane & 15, beta = i >> 2, pos = i & 1;
      const unsigned b0 = 2 * ((i >> 1) & 1), b1 = b0 + 1;
#pragma unroll
      for (int w = 0; w < 4; ++w) sel[w] = beta != w ? 0x0c0c0c0cu : (pos ? ((b1 << 24) | (b0 << 16) | 0x0c0cu) : (0x0c0c0000u | (b1 << 8) | b0));
    }
    RAWBAR();
    v4u qa0, qa1;
    {
      const int i = lane & 15;
      const v4u qf = lds128(L, L_Q + ((i >> 2) & 1) * 256 + (4 * g + (i & 3)) * 16);
      const v4u z = {0u, 0u, 0u, 0u};
      qa0 = (i >> 3) ? z : qf;
      qa1 = (i >> 3) ? qf : z;
    }
    const f4 zero = {0.f, 0.f, 0.f, 0.f};
    const int tn = NPK + nlk;
    const int scw = L_SC + wave * SCW;
    const int sca = scw + ((lane >> 2) & 3) * 16 + (2 * (lane & 1) + (lane >> 5)) * 4 + ((lane >> 4) & 1) * 2;
    const int scs = 16 * (lane & 1);
    float mx = -INFINITY;
    unsigned sv[NPK + 2 * K_NL];
    {
      f4 D0 = mfma(qa0, ring[0], zero); PIN(D0); ring[0] = LOADK(tn, 0); SB();
      D0 = mfma(qa1, ring[1], D0);      PIN(D0); ring[1] = LOADK(tn, 1); SB();
      f4 D1 = mfma(qa0, ring[2], zero); PIN(D1); throttle(3); ring[2] = LOADK(tn, 2); SB();
      D1 = mfma(qa1, ring[3], D1);      PIN(D1); throttle(3); ring[3] = LOADK(tn, 3); SB();
      sv[0] = score_pair(D0, D1);
      D0 = mfma(qa0, ring[4], zero); D0 = mfma(qa1, ring[5], D0);
      D1 = mfma(qa0, ring[6], zero); D1 = mfma(qa1, ring[7], D1);
      sv[1] = score_pair(D0, D1);
      PIN(sv[1]);
      static_assert(NPK == 6 && K_NL == 2, "the interleave below is written for four register tiles and four parked tiles");
#define WAITING(t) do { \
        if ((t) < NPK) { \
          D0 = mfma(qa0, pre[(4 * (t) - 8) & (P - 1)], zero); D0 = mfma(qa1, pre[(4 * (t) - 7) & (P - 1)], D0); \
          D1 = mfma(qa0, pre[(4 * (t) - 6) & (P - 1)], zero); D1 = mfma(qa1, pre[(4 * (t) - 5) & (P - 1)], D1); \
          sv[t] = score_pair(D0, D1); \
        } else { \
          sv[t] = 0xff80ff80u;        \
          if ((t) - NPK < nlk) { \
            const int a = kvl + ((t) - NPK) * 4096 + lane * 16; \
            D0 = mfma(qa0, lds128(L, a), zero); D0 = mfma(qa1, lds128(L, a + 1024), D0); \
            D1 = mfma(qa0, lds128(L, a + 2048), zero); D1 = mfma(qa1, lds128(L, a + 3072), D1); \
            sv[t] = score_pair(D0, D1); \
          } \
        } \
        PIN(sv[t]); } while (0)
      throttle(3); ring[4] = LOADK(tn + 1, 0); SB(); WAITING(2); WAITING(3);
      throttle(3); ring[5] = LOADK(tn + 1, 1); SB(); WAITING(4); WAITING(5);
      throttle(3); ring[6] = LOADK(tn + 1, 2); SB();
      VMWAIT(7);
      WAITING(6); WAITING(7);
      throttle(3); ring[7] = LOADK(tn + 1, 3); SB(); WAITING(8); WAITING(9);
#undef WAITING
      LGWAIT();
      lds_add(L, C_KVL);
      lds_wait(L, C_KVL, 8);
#pragma unroll
      for (int t = 0; t < NPK + 2 * K_NL; ++t) {
        if (t < tn) {
          mx = fmaxf(mx, fmaxf(bflo(sv[t]), bfhi(sv[t])));
          *(LAS u16*)(L + sca + t * 64) = (u16)(sv[t] >> scs);
        }
      }
    }
#pragma unroll 1
    for (int j = tn; j < ntw; j += 2) {
      if (j >= K_TRIG_WO && j < K_TRIG_WO + 2 && lane == 0 && wave == 0) stw(L, C_TRIG, 1u);
      pace();
      const bool more = j + 2 < ntw;
      const unsigned char* nb = more ? kb : vb;
      const int nj = more ? j + 2 : 0, no = more ? koff : voff;
#define LOADN(jj, u) __builtin_amdgcn_raw_buffer_load_b128(mkr(nb, 1u << 20), no, (jj) * 32768 + wb + (u) * 1024, AUX_NT)
      f4 D0 = mfma(qa0, ring[0], zero); PIN(D0); throttle(rdigit(K_RPAT, 0) + (radd > 0 ? 1u : 0u)); ring[0] = LOADN(nj, 0); SB();
      D0 = mfma(qa1, ring[1], D0);      PIN(D0); throttle(rdigit(K_RPAT, 1)); ring[1] = LOADN(nj, 1); SB();
      f4 D1 = mfma(qa0, ring[2], zero); PIN(D1); throttle(rdigit(K_RPAT, 2) + (radd > 2 ? 1u : 0u)); ring[2] = LOADN(nj, 2); SB();
      D1 = mfma(qa1, ring[3], D1);      PIN(D1); throttle(rdigit(K_RPAT, 3) - (radd < -1 ? 1u : 0u)); ring[3] = LOADN(nj, 3); SB();
      const unsigned s0 = score_pair(D0, D1);
      mx = fmaxf(mx, fmaxf(bflo(s0), bfhi(s0)));
      *(LAS u16*)(L + sca + j * 64) = (u16)(s0 >> scs);
      D0 = mfma(qa0, ring[4], zero); PIN(D0); throttle(rdigit(K_RPAT, 4) + (radd > 1 ? 1u : 0u)); ring[4] = LOADN(nj + 1, 0); SB();
      D0 = mfma(qa1, ring[5], D0);   PIN(D0); throttle(rdigit(K_RPAT, 5)); ring[5] = LOADN(nj + 1, 1); SB();
      D1 = mfma(qa0, ring[6], zero); PIN(D1); throttle(rdigit(K_RPAT, 6) + (radd > 3 ? 1u : 0u)); ring[6] = LOADN(nj + 1, 2); SB();
      D1 = mfma(qa1, ring[7], D1);   PIN(D1); throttle(rdigit(K_RPAT, 7) - (radd < 0 ? 1u : 0u)); ring[7] = LOADN(nj + 1, 3); SB();
#undef LOADN
      const unsigned s1 = score_pair(D0, D1);
      mx = fmaxf(mx, fmaxf(bflo(s1), bfhi(s1)));
      *(LAS u16*)(L + sca + (j + 1) * 64) = (u16)(s1 >> scs);
    }
    mx = fmaxf(mx, dpp<ROR4>(mx)); mx = fmaxf(mx, dpp<ROR8>(mx)); mx = x32max(mx);
    float m0h, m1h, l0 = 0.f, l1 = 0.f;
    {
      auto t = __builtin_amdgcn_permlane16_swap(asu(mx), asu(mx), false, false);
      m0h = asf(t[0]); m1h = asf(t[1]);
      LGWAIT();
#pragma unroll 3
      for (int i = lane; i < ntw * 16; i += 64) {
        const unsigned s = *(LAS unsigned*)(L + scw + i * 4);
        const float p0 = __builtin_amdgcn_exp2f((bflo(s) - m0h) * LOG2E), p1 = __builtin_amdgcn_exp2f((bfhi(s) - m1h) * LOG2E);
        l0 += p0; l1 += p1;
        *(LAS unsigned*)(L + scw + i * 4) = pk(p0, p1);
      }
      l0 = x32sum(x16sum(row16sum(l0))); l1 = x32sum(x16sum(row16sum(l1)));
      LGWAIT();
    }
    f4 acc = zero;
#pragma unroll 1
    for (int j = 0; j < ntw; j += 2) {
      const bool tail = j + K_TTAIL / 4 >= ntw;
      pace();
      const v4u pa = lds128(L, scw + j * 64 + g * 16), pb = lds128(L, scw + j * 64 + 64 + g * 16);
      acc = mfma(pfrag(pa.x, sel), ring[0], acc); PIN(acc); throttle(tail ? K_RTAIL : rdigit(K_RPAT, 0) + (radd > 0 ? 1u : 0u)); ring[0] = LOADV(j + 2, 0); SB();
      acc = mfma(pfrag(pa.y, sel), ring[1], acc); PIN(acc); throttle(tail ? K_RTAIL : rdigit(K_RPAT, 1)); ring[1] = LOADV(j + 2, 1); SB();
      acc = mfma(pfrag(pa.z, sel), ring[2], acc); PIN(acc); throttle(tail ? K_RTAIL : rdigit(K_RPAT, 2) + (radd > 2 ? 1u : 0u)); ring[2] = LOADV(j + 2, 2); SB();
      acc = mfma(pfrag(pa.w, sel), ring[3], acc); PIN(acc); throttle(tail ? K_RTAIL : rdigit(K_RPAT, 3) - (radd < -1 ? 1u : 0u)); ring[3] = LOADV(j + 2, 3); SB();
      acc = mfma(pfrag(pb.x, sel), ring[4], acc); PIN(acc); throttle(tail ? K_RTAIL : rdigit(K_RPAT, 4) + (radd > 1 ? 1u : 0u)); ring[4] = LOADV(j + 3, 0); SB();
      acc = mfma(pfrag(pb.y, sel), ring[5], acc); PIN(acc); throttle(tail ? K_RTAIL : rdigit(K_RPAT, 5)); ring[5] = LOADV(j + 3, 1); SB();
      acc = mfma(pfrag(pb.z, sel), ring[6], acc); PIN(acc); throttle(tail ? K_RTAIL : rdigit(K_RPAT, 6) + (radd > 3 ? 1u : 0u)); ring[6] = LOADV(j + 3, 2); SB();
      acc = mfma(pfrag(pb.w, sel), ring[7], acc); PIN(acc); throttle(tail ? K_RTAIL : rdigit(K_RPAT, 7) - (radd < 0 ? 1u : 0u)); ring[7] = LOADV(j + 3, 3); SB();
    }
#undef LOADK
#undef LOADV
    if (lds_add(L, C_END) == 7u && lane == 0) stw(L, C_TRIG, 2u);
    LAS float* mg = (LAS float*)(L + scw);
    const int cc = lane & 15;
    *(LAS f2*)(mg + 8 + 8 * cc + 2 * g) = f2{acc.x, acc.y};
    *(LAS f2*)(mg + 8 + 128 + 8 * cc + 2 * g) = f2{acc.z, acc.w};
    if (lane == 0) *(LAS f4*)mg = f4{m0h, l0, m1h, l1};
    LGWAIT();
    if (lds_add(L, C_MRG) == 7u) {
      MEMBAR();
      const int h = lane >> 5, d0 = 4 * (lane & 31);
      LAS float* m0 = (LAS float*)(L + L_SC);
      f2 ml[8]; f4 a[8];
#pragma unroll
      for (int w = 0; w < 8; ++w) { ml[w] = *(LAS f2*)(m0 + w * (SCW / 4) + 2 * h); a[w] = *(LAS f4*)(m0 + w * (SCW / 4) + 8 + 128 * h + d0); }
      float M = ml[0].x;
#pragma unroll
      for (int w = 1; w < 8; ++w) M = fmaxf(M, ml[w].x);
      float Ls = 0.f; f4 O = {0.f, 0.f, 0.f, 0.f};
#pragma unroll
      for (int w = 0; w < 8; ++w) { const float e = __builtin_amdgcn_exp2f((ml[w].x - M) * LOG2E); Ls += e * ml[w].y; O += e * a[w]; }
      const float inv = __builtin_amdgcn_rcpf(Ls);
      const unsigned o0 = pk(O.x * inv, O.y * inv), o1 = pk(O.z * inv, O.w * inv);
      const unsigned n0 = dppu<QX1>(o0), n1 = dppu<QX1>(o1);
      if (!(lane & 1)) stsc1(san4(v4u{o0, o1, n0, n1}), rws, WS_OP + aslot * 512 + (h * 128 + d0) * 2);
      const unsigned M1 = __builtin_amdgcn_readlane(asu(M), 32), L1 = __builtin_amdgcn_readlane(asu(Ls), 32);
      if (lane == 0) stsc1(san4(v4u{asu(M), asu(Ls), M1, L1}), rws, WS_ML + aslot * 16);
    }
  } else {
    RAWBAR();
    const int sw = wave - 8, stid = tid - 512;
    const u64 Es = __hip_atomic_load((GAS u64*)(p.ws + WS_EPOCH), __ATOMIC_RELAXED, __HIP_MEMORY_SCOPE_AGENT);
    const int fz = (q4 >> 1) | ((g & 1) << 1);
    auto mod_mfma = [&]() {
      f4 D[3] = {{0.f, 0.f, 0.f, 0.f}, {0.f, 0.f, 0.f, 0.f}, {0.f, 0.f, 0.f, 0.f}};
#pragma unroll
      for (int ks = 0; ks < 18; ++ks) {
        const v4u wf = trfrag(L, L_W + (32 * ks + 8 * g + q4) * 128 + (sw ^ fz) * 32 + p4 * 8, 4 * 128);
        const v4u sf = lds128(L, L_SIMG + (lane & 7) * XSTR + (32 * ks + 8 * g) * 2);
        D[ks % 3] = mfma(wf, sf, D[ks % 3]);
      }
      const f4 Ds = (D[0] + D[1]) + D[2];
      if ((lane & 15) < 8) stsc1_2(v2u{san(pk(Ds.x, Ds.y)), san(pk(Ds.z, Ds.w))}, rws, WS_MOD + ((lane & 7) * NMOD + wg * 64 + sw * 16 + 4 * g) * 2);
    };
    const u64 tagv = mix64((((u64)__builtin_amdgcn_readfirstlane((unsigned)(Es >> 32)) << 32) | (unsigned)__builtin_amdgcn_readfirstlane((unsigned)Es)) + 1);
    const unsigned tg0 = (unsigned)tagv, tg1 = (unsigned)(tagv >> 32);
    if (isMc) {
      if (sw == 0) {
        const v4u cv = __builtin_amdgcn_raw_buffer_load_b128(mkr(p.c, NB * DM * 2), tid * 16, 0, 0);
        v4u o;
#pragma unroll
        for (int e = 0; e < 4; ++e) o[e] = pk(silu_acc(bflo(cv[e])), silu_acc(bfhi(cv[e])));
        *(LAS v4u*)(L + L_SIMG + (tid / 72) * XSTR + (tid % 72) * 16) = o;
        LGWAIT();
        lds_add(L, C_SYNC1);
      }
      lds_wait(L, C_SYNC1, 1); lds_wait(L, C_DMA0, 8);
      mod_mfma();
    } else if (isQ) {
      const int qb = 2 * sw + (lane >> 5), qj = lane & 31;
      const bool qact = qj < 24;
      const rsrc_t rx = mkr(p.x, NB * DM * 2);
      v4u xv[3];
#pragma unroll
      for (int t = 0; t < 3; ++t) xv[t] = __builtin_amdgcn_raw_buffer_load_b128(rx, qact ? (qb * DM + 192 * t + 8 * qj) * 2 : 0x7ffffff0, 0, 0);
      float xf[3][8];
      float sum = 0.f;
#pragma unroll
      for (int t = 0; t < 3; ++t)
#pragma unroll
        for (int e = 0; e < 4; ++e) { xf[t][2 * e] = bflo(xv[t][e]); xf[t][2 * e + 1] = bfhi(xv[t][e]); sum += xf[t][2 * e] + xf[t][2 * e + 1]; }
      sum = x16sum(row16sum(sum));
      const float mean = sum * (1.f / DM);
      float vs = 0.f;
#pragma unroll
      for (int t = 0; t < 3; ++t)
#pragma unroll
        for (int e = 0; e < 8; ++e) { const float d = xf[t][e] - mean; vs += qact ? d * d : 0.f; }
      vs = x16sum(row16sum(vs));
      const float mb = rb(mean), den = rb(__builtin_sqrtf(rb(rb(vs * (1.f / DM)) + 1e-5f)));
      float ln[8];
#pragma unroll
      for (int e = 0; e < 8; ++e) { const float xe = qkq == 0 ? xf[0][e] : (qkq == 1 ? xf[1][e] : xf[2][e]); ln[e] = rb(rb(xe - mb) / den); }
      lds_wait(L, C_DMA0, 8);
      v4u wf[6];
#pragma unroll
      for (int ks = 0; ks < 6; ++ks) wf[ks] = trfrag(L, L_W + (32 * ks + 8 * g + q4) * 128 + (sw ^ fz) * 32 + p4 * 8, 4 * 128);
      v4u sa, ca;
      const int mo = qact ? WS_MOD + (qb * NMOD + 192 * qkq + 8 * qj) * 2 : 0x7ffffff0;
      if (sw == 0) {
        const int pw = (lane < 3 ? 0 : 6) + 3 * qkq + lane;
        const int fo = lane < 6 ? WS_CLR + pw * 8 : 0x7ffffff0, io = lane < 6 ? WS_MOD + (7 * NMOD + pw * 64 + 62) * 2 : 0x7ffffff0;
        bool cleared = false;
        for (unsigned spins = 0;;) {
          MEMBAR();
          if (!cleared) { const v2u fl = ldsc1_2(rws, fo); cleared = __all(lane >= 6 || (fl.x == tg0 && fl.y == tg1)); }
          else if (__all(__builtin_amdgcn_raw_buffer_load_b32(rws, io, 0, AUX_SC1) != SENT)) break;
          if (give_up(spins, L)) break;
          __builtin_amdgcn_s_sleep(1);
        }
        if (lane == 0) stw(L, C_HS, 1u);
      } else lds_wait(L, C_HS, 1);
      for (unsigned spins = 0;;) {
        MEMBAR();
        sa = ldsc1(rws, mo); ca = ldsc1(rws, qact ? mo + DM * 2 : mo);
        if (__all(umax(max4(sa), max4(ca)) != SENT)) break;
        if (give_up(spins, L)) break;
        __builtin_amdgcn_s_sleep(1);
      }
      v4u h;
#pragma unroll
      for (int e = 0; e < 4; ++e) {
        const unsigned c1 = pk(1.f + bflo(ca[e]), 1.f + bfhi(ca[e]));
        const unsigned m1 = pk(ln[2 * e] * bflo(c1), ln[2 * e + 1] * bfhi(c1));
        h[e] = pk(bflo(m1) + bflo(sa[e]), bfhi(m1) + bfhi(sa[e]));
      }
      if (qact) *(LAS v4u*)(L + L_HIMG + qb * HSTR + qj * 16) = h;
      LGWAIT();
      lds_add(L, C_SYNC1); lds_wait(L, C_SYNC1, 4);
      f4 D0 = {0.f, 0.f, 0.f, 0.f}, D1 = {0.f, 0.f, 0.f, 0.f};
#pragma unroll
      for (int ks = 0; ks < 6; ks += 2) {
        D0 = mfma(wf[ks], lds128(L, L_HIMG + (lane & 7) * HSTR + (32 * ks + 8 * g) * 2), D0);
        D1 = mfma(wf[ks + 1], lds128(L, L_HIMG + (lane & 7) * HSTR + (32 * ks + 32 + 8 * g) * 2), D1);
      }
      if ((lane & 15) < 8) stsc1(san4(__builtin_bit_cast(v4u, D0 + D1)), rws, WS_QP + ((qkq * 8 + (lane & 7)) * NQ + qcg * 64 + sw * 16 + 4 * g) * 4);
    }
    if (sw == 0) {
      v4u a0, a1, a2;
      const int qo = WS_QP + (ab * NQ + akv * 256 + 4 * lane) * 4;
      const int fo = lane < 12 ? WS_CLR + (54 + 12 * akv + lane) * 8 : 0x7ffffff0;
      const int io = lane < 12 ? WS_QP + (((lane % 3) * 8 + ab) * NQ + (4 * akv + lane / 3) * 64 + 63) * 4 : 0x7ffffff0;
      bool cleared = false;
      for (unsigned spins = 0;;) {
        MEMBAR();
        if (!cleared) { const v2u fl = ldsc1_2(rws, fo); cleared = __all(lane >= 12 || (fl.x == tg0 && fl.y == tg1)); }
        else if (__all(__builtin_amdgcn_raw_buffer_load_b32(rws, io, 0, AUX_SC1) != SENT)) break;
        if (give_up(spins, L)) break;
        __builtin_amdgcn_s_sleep(1);
      }
      for (unsigned spins = 0;;) {
        MEMBAR();
        a0 = ldsc1(rws, qo); a1 = ldsc1(rws, qo + NB * NQ * 4); a2 = ldsc1(rws, qo + 2 * NB * NQ * 4);
        if (__all(umax(umax(max4(a0), max4(a1)), max4(a2)) != SENT)) break;
        if (give_up(spins, L)) break;
        __builtin_amdgcn_s_sleep(1);
      }
      const f4 s = (__builtin_bit_cast(f4, a0) + __builtin_bit_cast(f4, a1)) + __builtin_bit_cast(f4, a2);
      *(LAS v2u*)(L + L_Q + lane * 8) = v2u{pk(s.x, s.y), pk(s.z, s.w)};
    }
    RAWBAR();
    if (isF && sw == 3) {
      const v4u ss = {SENT, SENT, SENT, SENT};
#pragma unroll 3
      for (int j = 0; j < 9; ++j) stsc1(ss, rws, WS_DP + ((j * 8 + (lane >> 3)) * 144 + f) * 128 + (lane & 7) * 16);
      VMWAIT(0);
      if (lane == 0) __hip_atomic_store((GAS u64*)(p.ws + WS_CLR2) + f, tagv, __ATOMIC_RELAXED, __HIP_MEMORY_SCOPE_AGENT);
    }
    if (isM && !isMc) {
      lds_wait(L, C_DMA0, 8); lds_wait(L, C_KVL, 8);
      const rsrc_t rw = mkr(p.wm, DM * NMOD * 2);
      const int c16 = lane & 7;
#pragma unroll 1
      for (int j = 0; j < 18; ++j) {
        const int i = sw * 18 + j;
        const int fs = ((lane >> 4) & 1) | ((i & 1) << 1);
        dma16(rw, (lane >> 3) * (NMOD * 2) + ((((c16 >> 1) ^ fs) << 1) | (c16 & 1)) * 16 + wg * 128, i * 8 * (NMOD * 2), ldsb + L_W + i * 1024);
        __builtin_amdgcn_s_sleep(K_WOPACE);
      }
      const rsrc_t rc = mkr(p.c, NB * DM * 2);
#pragma unroll 1
      for (int j = 0; j < 3; ++j) {
        const int ch = stid + 256 * j;
        const v4u cv = __builtin_amdgcn_raw_buffer_load_b128(rc, ch * 16, 0, 0);
        v4u o;
#pragma unroll
        for (int e = 0; e < 4; ++e) o[e] = pk(silu_acc(bflo(cv[e])), silu_acc(bfhi(cv[e])));
        if (ch < 576) *(LAS v4u*)(L + L_SIMG + (ch / 72) * XSTR + (ch % 72) * 16) = o;
      }
      VMWAIT(0); LGWAIT();
      lds_add(L, C_SYNC1); lds_wait(L, C_SYNC1, 4);
      mod_mfma();
    }
    if ((isF || isW) && sw == 3) {
      for (unsigned spins = 0;;) {
        MEMBAR();
        const v4u a = ldsc1(rws, WS_CLR + lane * 32), b = ldsc1(rws, WS_CLR + lane * 32 + 16);
        bool ok = a.x == tg0 && a.y == tg1 && a.z == tg0 && a.w == tg1 && b.x == tg0 && b.y == tg1 && b.z == tg0 && b.w == tg1;
        if (isF) {
          const v4u c2 = ldsc1(rws, lane < 36 ? WS_CLR2 + lane * 32 : 0x7ffffff0), d2 = ldsc1(rws, lane < 36 ? WS_CLR2 + lane * 32 + 16 : 0x7ffffff0);
          ok = ok && (lane >= 36 || (c2.x == tg0 && c2.y == tg1 && c2.z == tg0 && c2.w == tg1 && d2.x == tg0 && d2.y == tg1 && d2.z == tg0 && d2.w == tg1));
        }
        if (__all(ok)) break;
        if (give_up(spins, L)) break;
        __builtin_amdgcn_s_sleep(127);
      }
      if (lane == 0) stw(L, C_HS2, 1u);
    }
    if (isW || isF) {
      if (isM && !isMc) { lds_add(L, C_MDONE); lds_wait(L, C_MDONE, 4); }
      lds_wait(L, C_KVL, 8);
      const int sc = ((lane & 3) ^ ((lane >> 4) & 2)) * 16;
      if (isW) {
        const rsrc_t rw = mkr(p.wo, NQ * DM * 2);
        const int vo = (lane >> 2) * (DM * 2) + sc + wng * 64;
        lds_wait<16>(L, C_TRIG, 1);
#pragma unroll 1
        for (int j = 0; j < 32; ++j) { const int i = j * 4 + sw; dma16(rw, vo, (wsig * 2048 + i * 16) * DM * 2, ldsb + L_W + i * 1024); __builtin_amdgcn_s_sleep(K_WOPACE); }
        VMWAIT(0);
        lds_add(L, C_DMA1);
      } else {
        const rsrc_t rg = mkr(p.wg, DM * DFF * 2), ru = mkr(p.wu, DM * DFF * 2), rd = mkr(p.wd, DFF * DM * 2);
        const int vo = (lane >> 2) * (DFF * 2) + sc + f * 64;
        lds_wait<4>(L, C_TRIG, 2);
#pragma unroll 1
        for (int j = 0; j < 9; ++j) {
          const int i = j * 4 + sw;
          throttle(K_DMAR); dma16(rg, vo, i * 16 * DFF * 2, ldsb + L_W + i * 1024);
          throttle(K_DMAR); dma16(ru, vo, i * 16 * DFF * 2, ldsb + L_WU + i * 1024);
        }
#pragma unroll 1
        for (int j = 0; j < 9; ++j) {
          throttle(K_DMAR);
          if (j == K_DMAR - 1) lds_add(L, C_DMA1);
          const int i = j * 4 + sw; dma16(rd, lane * 16, f * 32 * DM * 2 + i * 1024, ldsb + L_WD + i * 1024);
        }
        static_assert(K_DMAR >= 1 && K_DMAR <= 6, "the throttle counts up to six");
        VMWAIT(0);
        lds_add(L, C_DMA2);
      }
    }
  }

  if (isW) {
    const int cc = lane & 15;
    if (wave < 8) {
      const int pid = (cc & 7) * 32 + (wsig * 8 + wave) * 2 + (cc >> 3);
      lds_wait(L, C_HS2, 1);
      lds_wait(L, C_DMA1, 4);
      v4u wf[2][4][2];
#pragma unroll
      for (int hh = 0; hh < 2; ++hh)
#pragma unroll
        for (int ks = 0; ks < 4; ++ks)
#pragma unroll
          for (int nt = 0; nt < 2; ++nt) wf[hh][ks][nt] = trfrag(L, L_W + (wave * 256 + hh * 128 + 32 * ks + 8 * g + q4) * 64 + (nt ^ (g & 1)) * 32 + p4 * 8, 4 * 64);
      v4u ml, fr[2][4];
      for (unsigned spins = 0;;) {
        MEMBAR();
        ml = ldsc1(rws, WS_ML + pid * 16);
        const int nv = __builtin_popcountll(__builtin_amdgcn_ballot_w64(max4(ml) != SENT));
        if (nv >= 4 * 15) break;
        if (give_up(spins, L)) break;
        if (nv < 4 * 12) __builtin_amdgcn_s_sleep(24); else __builtin_amdgcn_s_sleep(1);
      }
      for (unsigned spins = 0;;) {
        MEMBAR();
        ml = ldsc1(rws, WS_ML + pid * 16);
#pragma unroll
        for (int hh = 0; hh < 2; ++hh)
#pragma unroll
          for (int ks = 0; ks < 4; ++ks) fr[hh][ks] = ldsc1(rws, WS_OP + pid * 512 + (hh * 128 + 32 * ks + 8 * g) * 2);
        unsigned mx = max4(ml);
#pragma unroll
        for (int hh = 0; hh < 2; ++hh)
#pragma unroll
          for (int ks = 0; ks < 4; ++ks) mx = umax(mx, max4(fr[hh][ks]));
        if (__all(mx != SENT)) break;
        if (give_up(spins, L)) break;
      }
      float wgt[2];
#pragma unroll
      for (int hh = 0; hh < 2; ++hh) {
        const float M = asf(ml[2 * hh]), Lv = asf(ml[2 * hh + 1]);
        const float M2 = dpp<ROR8>(M), L2 = dpp<ROR8>(Lv), Mx = fmaxf(M, M2);
        const float e1 = __builtin_amdgcn_exp2f((M - Mx) * LOG2E) * Lv, e2 = __builtin_amdgcn_exp2f((M2 - Mx) * LOG2E) * L2;
        wgt[hh] = e1 * __builtin_amdgcn_rcpf(e1 + e2);
      }
#pragma unroll
      for (int nt = 0; nt < 2; ++nt) {
        f4 d0 = {0.f, 0.f, 0.f, 0.f}, d1 = {0.f, 0.f, 0.f, 0.f};
#pragma unroll
        for (int ks = 0; ks < 4; ++ks) { d0 = mfma(wf[0][ks][nt], fr[0][ks], d0); d1 = mfma(wf[1][ks][nt], fr[1][ks], d1); }
        f4 t = wgt[0] * d0 + wgt[1] * d1;
        t.x += dpp<ROR8>(t.x); t.y += dpp<ROR8>(t.y); t.z += dpp<ROR8>(t.z); t.w += dpp<ROR8>(t.w);
        if (cc < 8) *(LAS f4*)(L + L_RED + (((wave * 2 + nt) * 4 + g) * 8 + cc) * 16) = t;
      }
      LGWAIT();
      if (lds_add(L, C_RED) == 7u) {
        MEMBAR();
        f4 s = {0.f, 0.f, 0.f, 0.f};
#pragma unroll
        for (int w = 0; w < 8; ++w) s += *(LAS f4*)(L + L_RED + (w * 64 + lane) * 16);
        const int nt = lane >> 5, rr = (lane >> 3) & 3, b = lane & 7;
        stsc1(san4(__builtin_bit_cast(v4u, s)), rws, WS_YP + ((wsig * 8 + b) * DM + wng * 32 + nt * 16 + 4 * rr) * 4);
      }
    }
  } else if (isF) {
    const bool isR = f < 72;
    const int rb_ = f / 9, rc0 = (f % 9) * 64;
    unsigned gbw = 0;
    const int gt = wave & 3, kt = wave >> 2;
    v4u wf[6], wd[4];
    const int dw = wave - 3;
    bool have_wd = false;
    auto read_wd = [&]() {
      lds_wait(L, C_DMA2, 4);
#pragma unroll
      for (int j = 0; j < 4; ++j) wd[j] = trfrag(L, L_WD + (8 * g + q4) * (DM * 2) + (dw >= 0 ? 4 * dw + j : 0) * 32 + p4 * 8, 4 * DM * 2);
    };
    if (wave < 8) {
      const int b = wave;
      const bool act = lane < 48;
      const rsrc_t rx = mkr(p.x, NB * DM * 2);
      v2u xv[3], ga[3], sb[3], cb[3];
      lds_wait(L, C_HS2, 1);
#pragma unroll
      for (int t = 0; t < 3; ++t) xv[t] = __builtin_amdgcn_raw_buffer_load_b64(rx, act ? (b * DM + 4 * (lane + 48 * t)) * 2 : 0x7ffffff0, 0, 0);
      for (unsigned spins = 0;;) {
        MEMBAR();
        unsigned mx = 0;
#pragma unroll
        for (int t = 0; t < 3; ++t) {
          const int o = act ? WS_MOD + (b * NMOD + 4 * (lane + 48 * t)) * 2 : 0x7ffffff0;
          ga[t] = ldsc1_2(rws, act ? o + 2 * DM * 2 : o); sb[t] = ldsc1_2(rws, act ? o + 3 * DM * 2 : o); cb[t] = ldsc1_2(rws, act ? o + 4 * DM * 2 : o);
          mx = umax(mx, umax(umax(umax(ga[t].x, ga[t].y), umax(sb[t].x, sb[t].y)), umax(cb[t].x, cb[t].y)));
        }
        if (wave == 0) { gbw = __builtin_amdgcn_raw_buffer_load_b32(rws, isR ? WS_MOD + (rb_ * NMOD + 5 * DM + rc0 + (lane & ~1)) * 2 : 0x7ffffff0, 0, AUX_SC1); mx = umax(mx, gbw); }
        if (__all(mx != SENT)) break;
        if (give_up(spins, L)) break;
      }
      float xf[12], gf[12], cf[12], sf[12];
#pragma unroll
      for (int t = 0; t < 3; ++t)
#pragma unroll
        for (int e = 0; e < 2; ++e) {
          xf[4 * t + 2 * e] = bflo(xv[t][e]); xf[4 * t + 2 * e + 1] = bfhi(xv[t][e]);
          gf[4 * t + 2 * e] = bflo(ga[t][e]); gf[4 * t + 2 * e + 1] = bfhi(ga[t][e]);
          cf[4 * t + 2 * e] = 1.f + bflo(cb[t][e]); cf[4 * t + 2 * e + 1] = 1.f + bfhi(cb[t][e]);
          sf[4 * t + 2 * e] = bflo(sb[t][e]); sf[4 * t + 2 * e + 1] = bfhi(sb[t][e]);
        }
#pragma unroll
      for (int e = 0; e < 12; ++e) asm volatile("" : "+v"(xf[e]), "+v"(gf[e]), "+v"(cf[e]), "+v"(sf[e]));
      lds_wait(L, C_DMA1, 4);
#pragma unroll
      for (int k6 = 0; k6 < 6; ++k6) wf[k6] = trfrag(L, ((gt >> 1) ? L_WU : L_W) + (32 * (kt * 6 + k6) + 8 * g + q4) * 64 + ((gt & 1) ^ (g & 1)) * 32 + p4 * 8, 4 * 64);
      if (wave >= 3 && __builtin_amdgcn_readfirstlane(ldsw(L, C_DMA2)) >= 4u) { read_wd(); have_wd = true; }
      if (wave == 0) {
        for (unsigned spins = 0;;) {
          MEMBAR();
          const unsigned v = __builtin_amdgcn_raw_buffer_load_b32(rws, lane == 0 ? WS_YP + (((f & 1) * 8 + (f & 7)) * DM + (f % 18) * 32) * 4 : 0x7ffffff0, 0, AUX_SC1);
          if (__builtin_amdgcn_readfirstlane(v) != SENT) break;
          if (give_up(spins, L)) break;
          __builtin_amdgcn_s_sleep(1);
        }
        if (lane == 0) stw(L, C_Y, 1u);
      } else lds_wait(L, C_Y, 1);
      v4u y0[3], y1[3];
      for (unsigned spins = 0;;) {
        MEMBAR();
        unsigned mx = 0;
#pragma unroll
        for (int t = 0; t < 3; ++t) {
          const int o = act ? WS_YP + (b * DM + 4 * (lane + 48 * t)) * 4 : 0x7ffffff0;
          y0[t] = ldsc1(rws, o); y1[t] = ldsc1(rws, act ? o + NB * DM * 4 : o);
        }
#pragma unroll
        for (int t = 0; t < 3; ++t) mx = umax(mx, umax(max4(y0[t]), max4(y1[t])));
        if (__all(mx != SENT)) break;
        if (give_up(spins, L)) break;
      }
      float x1[12];
#pragma unroll
      for (int e = 0; e < 12; ++e) x1[e] = __builtin_fmaf(gf[e], asf(y0[e >> 2][e & 3]) + asf(y1[e >> 2][e & 3]), xf[e]);
      const float sh = asf(__builtin_amdgcn_readfirstlane(asu(x1[0])));
      float s1 = 0.f, s2 = 0.f;
#pragma unroll
      for (int e = 0; e < 12; ++e) { const float d = x1[e] - sh; s1 += d; s2 = __builtin_fmaf(d, d, s2); }
      s1 = act ? s1 : 0.f; s2 = act ? s2 : 0.f;
      s1 += dpp<ROR1>(s1); s2 += dpp<ROR1>(s2); s1 += dpp<ROR2>(s1); s2 += dpp<ROR2>(s2);
      s1 += dpp<ROR4>(s1); s2 += dpp<ROR4>(s2); s1 += dpp<ROR8>(s1); s2 += dpp<ROR8>(s2);
      s1 = x16sum(s1); s2 = x16sum(s2); s1 = x32sum(s1); s2 = x32sum(s2);
      const float dm = s1 * (1.f / DM), var = fmaxf(__builtin_fmaf(-dm, dm, s2 * (1.f / DM)), 0.f);
      const float rstd = __builtin_amdgcn_rsqf(var + 1e-5f), mo = -(sh + dm) * rstd;
#pragma unroll
      for (int t = 0; t < 3; ++t) {
        float hv[4];
#pragma unroll
        for (int e = 0; e < 4; ++e) hv[e] = __builtin_fmaf(__builtin_fmaf(x1[4 * t + e], rstd, mo), cf[4 * t + e], sf[4 * t + e]);
        if (act) {
          *(LAS v2u*)(L + L_H2 + b * XSTR + 8 * (lane + 48 * t)) = v2u{pk(hv[0], hv[1]), pk(hv[2], hv[3])};
          const int col = 4 * (lane + 48 * t) - rc0;
          if (isR && b == rb_ && col >= 0 && col < 64) *(LAS f4*)(L + L_X1S + col * 4) = f4{x1[4 * t], x1[4 * t + 1], x1[4 * t + 2], x1[4 * t + 3]};
        }
      }
    } else {
      lds_wait(L, C_DMA1, 4);
#pragma unroll
      for (int k6 = 0; k6 < 6; ++k6) wf[k6] = trfrag(L, ((gt >> 1) ? L_WU : L_W) + (32 * (kt * 6 + k6) + 8 * g + q4) * 64 + ((gt & 1) ^ (g & 1)) * 32 + p4 * 8, 4 * 64);
      lds_wait(L, C_Y, 1);
      if (__builtin_amdgcn_readfirstlane(ldsw(L, C_DMA2)) >= 4u) { read_wd(); have_wd = true; }
    }
    RAWBAR();
    {
      f4 D0 = {0.f, 0.f, 0.f, 0.f}, D1 = {0.f, 0.f, 0.f, 0.f};
#pragma unroll
      for (int k6 = 0; k6 < 6; k6 += 2) {
        D0 = mfma(wf[k6], lds128(L, L_H2 + (lane & 7) * XSTR + (32 * (kt * 6 + k6) + 8 * g) * 2), D0);
        D1 = mfma(wf[k6 + 1], lds128(L, L_H2 + (lane & 7) * XSTR + (32 * (kt * 6 + k6 + 1) + 8 * g) * 2), D1);
      }
      if ((lane & 15) < 8) *(LAS f4*)(L + L_GU + (((kt * 4 + gt) * 4 + g) * 8 + (lane & 7)) * 16) = D0 + D1;
    }
    RAWBAR();
    if (!have_wd && wave >= 4) read_wd();
    if (tid < 256) {
      const int b = tid & 7, fc = tid >> 3;
      const int o = (((fc >> 4) * 4 + ((fc >> 2) & 3)) * 8 + b) * 16 + (fc & 3) * 4;
      float gs = 0.f, us = 0.f;
#pragma unroll
      for (int k3 = 0; k3 < 3; ++k3) { gs += *(LAS float*)(L + L_GU + k3 * 2048 + o); us += *(LAS float*)(L + L_GU + k3 * 2048 + 1024 + o); }
      const float a = rb(gelu_t(rb(gs))) * rb(us);
      *(LAS u16*)(L + L_ACT + b * ASTR + fc * 2) = (u16)pk(a, a);
      if (!have_wd && wave == 3) read_wd();
    }
    RAWBAR();
    if (wave >= 3) {
      const v4u af = lds128(L, L_ACT + (lane & 7) * ASTR + g * 16);
      const int b = lane & 7, hi = (lane >> 3) & 1;
      const f4 z4 = {0.f, 0.f, 0.f, 0.f};
      const f4 T0 = mfma(wd[0], af, z4), T1 = mfma(wd[1], af, z4), T2 = mfma(wd[2], af, z4), T3 = mfma(wd[3], af, z4);
      const f4 Da = hi ? T2 : T0, Db = hi ? T3 : T1;
      stsc1(san4(v4u{pk(Da.x, Da.y), pk(Da.z, Da.w), pk(Db.x, Db.y), pk(Db.z, Db.w)}), rws, WS_DP + ((b * 9 + dw) * 144 + f) * 128 + (hi * 4 + g) * 16);
    }
    if (isR) {
      v4u a, b2;
      const int o = WS_DP + f * 144 * 128 + tid * 16;
      for (unsigned spins = 0;;) {
        MEMBAR();
        a = ldsc1(rws, o); b2 = ldsc1(rws, tid < 384 ? o + 768 * 16 : 0x7ffffff0);
        if (__all(umax(max4(a), max4(b2)) != SENT)) break;
        if (give_up(spins, L)) break;
      }
      float s[8];
#pragma unroll
      for (int e = 0; e < 4; ++e) { s[2 * e] = bflo(a[e]) + bflo(b2[e]); s[2 * e + 1] = bfhi(a[e]) + bfhi(b2[e]); }
#pragma unroll
      for (int e = 0; e < 8; ++e) { float v = s[e]; v += dpp<ROR8>(v); v = x32sum(x16sum(v)); s[e] = v; }
      if (lane < 8) {
        *(LAS f4*)(L + L_RED2 + (wave * 8 + lane) * 32) = f4{s[0], s[1], s[2], s[3]};
        *(LAS f4*)(L + L_RED2 + (wave * 8 + lane) * 32 + 16) = f4{s[4], s[5], s[6], s[7]};
      }
      RAWBAR();
    }
    if (isR && wave == 0) {
      const int ro = (((lane >> 5) * 4 + ((lane >> 2) & 3)) * 8 + ((lane >> 4) & 1) * 4 + (lane & 3)) * 4;
      float dn = 0.f;
#pragma unroll
      for (int w = 0; w < 12; ++w) dn += *(LAS float*)(L + L_RED2 + w * 256 + ro);
      const float gv = (lane & 1) ? bfhi(gbw) : bflo(gbw);
      const float o = *(LAS float*)(L + L_X1S + lane * 4) + rb(gv * rb(dn));
      const unsigned two = pk(o, dpp<QX1>(o));
      if (!(lane & 1)) *(GAS unsigned*)(p.out + rb_ * DM + rc0 + lane) = two;
      if (wg == 0 && lane == 0) {
        const u64 E = ((u64)ldsw(L, C_E1) << 32) | ldsw(L, C_E0);
        __hip_atomic_store((GAS u64*)(p.ws + WS_EPOCH), E + 1, __ATOMIC_RELAXED, __HIP_MEMORY_SCOPE_AGENT);
      }
    }
  }
}

}

extern "C" void kernel_launch(void* const* d_in, const int* in_sizes, int n_in, void* d_out, int out_size, void* d_ws, size_t ws_size, hipStream_t stream) {
  ak::Params p;
  p.x = (const ak::u16*)d_in[0]; p.K = (const ak::u16*)d_in[1]; p.V = (const ak::u16*)d_in[2];
  p.wq = (const ak::u16*)d_in[3]; p.wo = (const ak::u16*)d_in[4];
  p.wg = (const ak::u16*)d_in[6]; p.wu = (const ak::u16*)d_in[7]; p.wd = (const ak::u16*)d_in[8];
  p.c = (const ak::u16*)d_in[9]; p.wm = (const ak::u16*)d_in[10];
  p.out = (ak::u16*)d_out; p.ws = (unsigned char*)d_ws;
  (void)hipFuncSetAttribute((const void*)ak::fused_kernel, hipFuncAttributeMaxDynamicSharedMemorySize, ak::L_END);
  ak::fused_kernel<<<ak::NWG, ak::NTHR, ak::L_END, stream>>>(p);
}
```

```cpp
#include <hip/hip_runtime.h>
#include <stdint.h>

namespace ak {

typedef unsigned short u16;
typedef unsigned long long u64;
typedef unsigned v4u __attribute__((ext_vector_type(4)));
typedef unsigned v2u __attribute__((ext_vector_type(2)));
typedef float f4 __attribute__((ext_vector_type(4)));
typedef float f2 __attribute__((ext_vector_type(2)));
typedef __bf16 bf8 __attribute__((ext_vector_type(8)));
typedef __bf16 bf2 __attribute__((ext_vector_type(2)));
typedef short s4v __attribute__((ext_vector_type(4)));
#define LAS __attribute__((address_space(3)))
#define GAS __attribute__((address_space(1)))
typedef __amdgpu_buffer_rsrc_t rsrc_t;

constexpr int K_RPAT = 23333333;
constexpr int K_RTAIL = 5;
constexpr int K_TTAIL = 40;
constexpr int K_RA_W = 2;
constexpr int K_RA_MC = 1;
constexpr int K_WFB = 1;
constexpr int K_WFB_B = 5;
constexpr int K_WFB_A = 2;
constexpr int K_WFB_B2 = 9;
constexpr int K_WFB_A2 = -2;
constexpr int K_TLBW = 1;
constexpr int K_PF = 3;
constexpr int K_NP = 2;
constexpr int K_NL = 2;
constexpr int K_TRIG_WO = 14;
constexpr int K_WOPACE = 20;
constexpr int K_DMAR = 5;

constexpr int NB = 8, DM = 576, DFF = 4608, NMOD = 3456, NQ = 4096;
constexpr int NWG = 256, NTHR = 768;
constexpr int NP = K_NP, P = NP * 8;
__host__ __device__ constexpr unsigned rdigit(unsigned v, int k) { for (int i = 7; i > k; --i) v /= 10; return v % 10; }
constexpr float LOG2E = 1.4426950408889634f;
constexpr float SCALE = 0.08838834764831845f;
constexpr unsigned SENT = 0xFFFFFFFFu;
constexpr int AUX_NT = 2, AUX_SC1 = 16;
constexpr unsigned SPIN_CAP = 1u << 15, LDS_CAP = 1u << 18;

constexpr int WS_EPOCH = 0;
constexpr int WS_CLR = 4096, WS_CLR2 = 6144;
constexpr int WS_MOD = 8192;
constexpr int WS_QP = 65536;
constexpr int WS_OP = WS_QP + 3 * NB * NQ * 4;
constexpr int WS_ML = WS_OP + NWG * 512;
constexpr int WS_YP = WS_ML + NWG * 16;
constexpr int WS_DP = WS_YP + 2 * NB * DM * 4;
constexpr int WS_END = WS_DP + 72 * 144 * 128;

constexpr int L_W = 0;
constexpr int L_SIMG = 73728;
constexpr int L_HIMG = 24576;
constexpr int HSTR = 400;
constexpr int L_WU = 36864, L_WD = 73728;
constexpr int L_GU = 110592;
constexpr int L_ACT = L_GU + 6144;
constexpr int ASTR = 80;
constexpr int L_RED2 = L_ACT + 640;
constexpr int L_X1S = L_RED2 + 3072;
constexpr int L_H2 = L_X1S + 256;
constexpr int XSTR = 1168;
static_assert(L_H2 + 8 * XSTR <= 131072, "ffn lds");
constexpr int L_RED = 131072;
constexpr int SCW = 2304;
constexpr int L_SC = 141312;
constexpr int L_KV = 28672, L_KVM = 83968;
constexpr int L_Q = 160128;
constexpr int L_CTL = L_Q + 512;
constexpr int L_END = L_CTL + 128;
static_assert(L_KV + K_NL * 65536 <= L_Q && L_KVM + 65536 <= L_Q && L_SC + 8 * SCW <= L_Q && L_END <= 163840, "lds map");
enum { C_SYNC1 = 0, C_HS = 1, C_TRIG = 2, C_MRG = 3, C_DEAD = 4, C_DMA0 = 5, C_DMA1 = 6, C_HS2 = 7, C_KVL = 10, C_Y = 11, C_DMA2 = 12, C_E0 = 8, C_E1 = 9, C_RED = 16, C_MDONE = 17, C_END = 18, C_PRG = 19 };

struct Params {
  const u16 *x, *K, *V, *wq, *wo, *wg, *wu, *wd, *c, *wm;
  u16* out;
  unsigned char* ws;
};

__device__ __forceinline__ float asf(unsigned u) { return __builtin_bit_cast(float, u); }
__device__ __forceinline__ unsigned asu(float f) { return __builtin_bit_cast(unsigned, f); }
__device__ __forceinline__ float bflo(unsigned u) { return asf(u << 16); }
__device__ __forceinline__ float bfhi(unsigned u) { return asf(u & 0xffff0000u); }
__device__ __forceinline__ unsigned pk(float a, float b) {
  f2 v = {a, b};
  return __builtin_bit_cast(unsigned, __builtin_convertvector(v, bf2));
}
__device__ __forceinline__ float rb(float a) { return bflo(pk(a, a)); }
__device__ __forceinline__ unsigned san(unsigned u) { return u < 0xFFFFFFFEu ? u : 0xFFFFFFFEu; }
__device__ __forceinline__ v4u san4(v4u v) { return v4u{san(v.x), san(v.y), san(v.z), san(v.w)}; }
__device__ __forceinline__ unsigned umax(unsigned a, unsigned b) { return a > b ? a : b; }
__device__ __forceinline__ unsigned max4(v4u v) { return umax(umax(v.x, v.y), umax(v.z, v.w)); }
template <int C> __device__ __forceinline__ float dpp(float x) {
  return asf((unsigned)__builtin_amdgcn_mov_dpp((int)asu(x), C, 0xf, 0xf, true));
}
template <int C> __device__ __forceinline__ unsigned dppu(unsigned x) { return (unsigned)__builtin_amdgcn_mov_dpp((int)x, C, 0xf, 0xf, true); }
constexpr int ROR1 = 0x121, ROR2 = 0x122, ROR4 = 0x124, ROR8 = 0x128, QX1 = 0xB1, QX2 = 0x4E;
__device__ __forceinline__ float x32max(float x) { auto t = __builtin_amdgcn_permlane32_swap(asu(x), asu(x), false, false); return fmaxf(asf(t[0]), asf(t[1])); }
__device__ __forceinline__ float x32sum(float x) { auto t = __builtin_amdgcn_permlane32_swap(asu(x), asu(x), false, false); return asf(t[0]) + asf(t[1]); }
__device__ __forceinline__ float x16sum(float x) { auto t = __builtin_amdgcn_permlane16_swap(asu(x), asu(x), false, false); return asf(t[0]) + asf(t[1]); }
__device__ __forceinline__ float row16sum(float x) { x += dpp<ROR1>(x); x += dpp<ROR2>(x); x += dpp<ROR4>(x); x += dpp<ROR8>(x); return x; }
__device__ __forceinline__ f4 mfma(v4u a, v4u b, f4 c) {
  return __builtin_amdgcn_mfma_f32_16x16x32_bf16(__builtin_bit_cast(bf8, a), __builtin_bit_cast(bf8, b), c, 0, 0, 0);
}
__device__ __forceinline__ rsrc_t mkr(const void* p, unsigned bytes) { return __builtin_amdgcn_make_buffer_rsrc((void*)p, 0, bytes, 0x00020000); }
__device__ __forceinline__ v4u ldsc1(rsrc_t r, int off) { return __builtin_amdgcn_raw_buffer_load_b128(r, off, 0, AUX_SC1); }
__device__ __forceinline__ v2u ldsc1_2(rsrc_t r, int off) { return __builtin_amdgcn_raw_buffer_load_b64(r, off, 0, AUX_SC1); }
__device__ __forceinline__ void stsc1(v4u v, rsrc_t r, int off) { __builtin_amdgcn_raw_buffer_store_b128(v, r, off, 0, AUX_SC1); }
__device__ __forceinline__ void stsc1_2(v2u v, rsrc_t r, int off) { __builtin_amdgcn_raw_buffer_store_b64(v, r, off, 0, AUX_SC1); }
#define MEMBAR() asm volatile("" ::: "memory")
#define VMWAIT(n) asm volatile("s_waitcnt vmcnt(" #n ")" ::: "memory")
#define LGWAIT() asm volatile("s_waitcnt lgkmcnt(0)" ::: "memory")
#define RAWBAR() asm volatile("s_waitcnt lgkmcnt(0)\n\ts_barrier" ::: "memory")
__device__ __forceinline__ void dma16(rsrc_t r, int voff, unsigned soff, unsigned ldsa) {
  unsigned keep;
  asm volatile("s_mov_b32 %0, m0\n\ts_mov_b32 m0, %1\n\ts_nop 2\n\tbuffer_load_dwordx4 %2, %3, %4 offen nt lds\n\ts_mov_b32 m0, %0" : "=&s"(keep) : "s"(ldsa), "v"(voff), "s"(r), "s"(soff) : "memory");
}
__device__ __forceinline__ v4u ld_hidden(rsrc_t r, int voff) {
  v4u v;
  asm volatile("buffer_load_dwordx4 %0, %1, %2, 0 offen" : "=&v"(v) : "v"(voff), "s"(r) : "memory");
  return v;
}
__device__ __forceinline__ v4u lds128(LAS unsigned char* L, int off) { return *(LAS v4u*)(L + off); }
__device__ __forceinline__ v4u trfrag(LAS unsigned char* L, int a0, int d) {
  s4v lo = __builtin_amdgcn_ds_read_tr16_b64_v4i16((LAS s4v*)(L + a0));
  s4v hi = __builtin_amdgcn_ds_read_tr16_b64_v4i16((LAS s4v*)(L + a0 + d));
  v2u l = __builtin_bit_cast(v2u, lo), h = __builtin_bit_cast(v2u, hi);
  return v4u{l.x, l.y, h.x, h.y};
}
__device__ __forceinline__ unsigned ldsw(LAS unsigned char* L, int w) { return *(volatile LAS unsigned*)(L + L_CTL + 4 * w); }
__device__ __forceinline__ void stw(LAS unsigned char* L, int w, unsigned v) { *(volatile LAS unsigned*)(L + L_CTL + 4 * w) = v; }
__device__ __forceinline__ unsigned lds_add(LAS unsigned char* L, int w) {
  unsigned o = 0;
  if ((threadIdx.x & 63) == 0) o = __hip_atomic_fetch_add((LAS unsigned*)(L + L_CTL + 4 * w), 1u, __ATOMIC_RELAXED, __HIP_MEMORY_SCOPE_WORKGROUP);
  return __builtin_amdgcn_readfirstlane(o);
}
template <int SLP = 1> __device__ __forceinline__ void lds_wait(LAS unsigned char* L, int w, unsigned n) {
  for (unsigned spins = 0; __builtin_amdgcn_readfirstlane(ldsw(L, w)) < n; ++spins) {
    if (__builtin_amdgcn_readfirstlane(ldsw(L, C_DEAD))) break;
    if (spins > LDS_CAP) { stw(L, C_DEAD, 1u); break; }
    __builtin_amdgcn_s_sleep(SLP);
  }
  MEMBAR();
}
__device__ __forceinline__ bool give_up(unsigned& spins, LAS unsigned char* L) {
  if (++spins > SPIN_CAP) { stw(L, C_DEAD, 1u); return true; }
  return (spins & 15u) == 0u && __builtin_amdgcn_readfirstlane(ldsw(L, C_DEAD)) != 0u;
}
__device__ __forceinline__ u64 mix64(u64 z) {
  z += 0x9E3779B97F4A7C15ull;
  z = (z ^ (z >> 30)) * 0xBF58476D1CE4E5B9ull;
  z = (z ^ (z >> 27)) * 0x94D049BB133111EBull;
  return z ^ (z >> 31);
}
__device__ __forceinline__ void throttle(unsigned r) {
  asm volatile("s_cmp_gt_u32 %0, 6\n\ts_cbranch_scc1 9f\n\ts_waitcnt vmcnt(5)\n\t"
               "s_cmp_gt_u32 %0, 5\n\ts_cbranch_scc1 9f\n\ts_waitcnt vmcnt(4)\n\t"
               "s_cmp_gt_u32 %0, 4\n\ts_cbranch_scc1 9f\n\ts_waitcnt vmcnt(3)\n\t"
               "s_cmp_gt_u32 %0, 3\n\ts_cbranch_scc1 9f\n\ts_waitcnt vmcnt(2)\n\t"
               "s_cmp_gt_u32 %0, 2\n\ts_cbranch_scc1 9f\n\ts_waitcnt vmcnt(1)\n\t"
               "s_cmp_gt_u32 %0, 1\n\ts_cbranch_scc1 9f\n\ts_waitcnt vmcnt(0)\n9:" ::"s"(r) : "scc", "memory");
}

__device__ __forceinline__ float sigm(float z) { return __builtin_amdgcn_rcpf(1.f + __builtin_amdgcn_exp2f(-LOG2E * z)); }
__device__ __forceinline__ float silu_acc(float a) {
  const float hi = 1.44269502162933349609375f, lo = 1.92596299112661746e-8f;
  const float t0 = -a * hi;
  const float r = __builtin_fmaf(-a, lo, __builtin_fmaf(-a, hi, -t0));
  const float e0 = __builtin_amdgcn_exp2f(fminf(t0, 126.f));
  const float d = 1.f + __builtin_fmaf(e0, r * 0.693147180559945f, e0);
  const float rc = __builtin_amdgcn_rcpf(d), q = a * rc;
  return __builtin_fmaf(__builtin_fmaf(-q, d, a), rc, q);
}
__device__ __forceinline__ float gelu_t(float x) { const float z = 0.7978845608028654f * (x + 0.044715f * x * x * x); return x * sigm(2.f * z); }

__device__ __forceinline__ float quadsum(f4 d) {
  float t = dpp<0x00>(d.x);
  t += dpp<0x55>(d.y); t += dpp<0xAA>(d.z); t += dpp<0xFF>(d.w);
  return t;
}
__device__ __forceinline__ unsigned score_pair(f4 D0, f4 D1) {
  const unsigned r1 = pk(quadsum(D0), quadsum(D1));
  return pk(bflo(r1) * SCALE, bfhi(r1) * SCALE);
}
__device__ __forceinline__ v4u pfrag(unsigned v, const unsigned (&sel)[4]) {
  return v4u{__builtin_amdgcn_perm(v, v, sel[0]), __builtin_amdgcn_perm(v, v, sel[1]), __builtin_amdgcn_perm(v, v, sel[2]), __builtin_amdgcn_perm(v, v, sel[3])};
}

__global__ void __launch_bounds__(NTHR, 3) fused_kernel(Params p) {
  extern __shared__ __attribute__((aligned(16))) unsigned char smem[];
  LAS unsigned char* L = (LAS unsigned char*)smem;
  const int tid = threadIdx.x, lane = tid & 63, wave = __builtin_amdgcn_readfirstlane(tid >> 6);
  const int wg = blockIdx.x;
  const unsigned ldsb = (unsigned)(size_t)L;
  if (tid < 32) *(LAS unsigned*)(L + L_CTL + 4 * tid) = 0u;
  const rsrc_t rws = mkr(p.ws, WS_END);
  const int xl = wg & 7, xi = wg >> 3;
  const bool isM = wg < 54, isMc = wg < 18, isQ = wg >= 54 && wg < 246;
  const bool isF = wg < 144;
  const int f = xl * 18 + xi;
  const int wpair = xl + 8 * ((xi - 18) >> 1);
  const bool isW = wg >= 144 && wg < 192 && wpair < 18;
  const int wsig = wpair / 9, wng = 2 * (wpair % 9) + ((xi - 18) & 1);
  const int ahead = wg >> 1, aslot = wg;
  const int ab = ahead >> 4, akv = ahead & 15;
  const int qi = wg - 54, qcg = qi / 3, qkq = qi % 3;
  const int g = lane >> 4, q4 = (lane & 15) >> 2, p4 = lane & 3;

  if (wave < 8) {
    v2u E7 = {0u, 0u};
    if (wave == 7) {
      asm volatile("buffer_load_dwordx2 %0, %1, %2, 0 offen sc1" : "=&v"(E7) : "v"(WS_EPOCH), "s"(rws) : "memory");
      const v4u ss = {SENT, SENT, SENT, SENT};
      if (isM) stsc1(ss, rws, WS_MOD + ((lane >> 3) * NMOD + wg * 64) * 2 + (lane & 7) * 16);
      if (isQ) {
#pragma unroll
        for (int j = 0; j < 2; ++j) stsc1(ss, rws, WS_QP + (((qkq * 8 + 4 * j + (lane >> 4)) * NQ) + qcg * 64) * 4 + (lane & 15) * 16);
      }
      if (lane <= 32) stsc1(ss, rws, lane < 32 ? WS_OP + aslot * 512 + lane * 16 : WS_ML + aslot * 16);
      if (isW) stsc1(ss, rws, WS_YP + ((wsig * 8 + (lane >> 3)) * DM + wng * 32) * 4 + (lane & 7) * 16);
    }
    v4u cvm = {0u, 0u, 0u, 0u};
    if (isMc) cvm = ld_hidden(mkr(p.c, NB * DM * 2), tid * 16);
    if (isMc || isQ) {
      const int stride = isM ? NMOD * 2 : NQ * 2, n = isM ? 9 : 3;
      const rsrc_t rw = isM ? mkr(p.wm, DM * NMOD * 2) : mkr(p.wq, DM * NQ * 2);
      const int cb = isM ? wg * 128 : qcg * 128, r0 = isM ? 0 : qkq * 192;
      const int c16 = lane & 7;
#pragma unroll 1
      for (int j = 0; j < n; ++j) {
        const int i = wave * n + j;
        const int fz = ((lane >> 4) & 1) | ((i & 1) << 1);
        dma16(rw, (lane >> 3) * stride + ((((c16 >> 1) ^ fz) << 1) | (c16 & 1)) * 16 + cb, (r0 + i * 8) * stride, ldsb + L_W + i * 1024);
      }
    }
    auto say_cleared = [&]() {
      if (wave == 7) {
        const unsigned e0 = __builtin_amdgcn_readfirstlane(E7.x), e1 = __builtin_amdgcn_readfirstlane(E7.y);
        if (lane == 0) { __hip_atomic_store((GAS u64*)(p.ws + WS_CLR) + wg, mix64((((u64)e1 << 32) | e0) + 1), __ATOMIC_RELAXED, __HIP_MEMORY_SCOPE_AGENT); stw(L, C_E0, e0); stw(L, C_E1, e1); }
        LGWAIT();
      }
    };
    if (isMc) {
      RAWBAR();
      asm volatile("s_waitcnt vmcnt(9)" : "+v"(cvm), "+v"(E7)::"memory");
      __builtin_amdgcn_sched_barrier(0);
      say_cleared();
      v4u o;
#pragma unroll
      for (int e = 0; e < 4; ++e) o[e] = pk(silu_acc(bflo(cvm[e])), silu_acc(bfhi(cvm[e])));
      *(LAS v4u*)(L + L_SIMG + (tid / 72) * XSTR + (tid % 72) * 16) = o;
      asm volatile("s_waitcnt vmcnt(0) lgkmcnt(0)" ::: "memory");
      lds_add(L, C_DMA0);
    }
    const unsigned char* kb = (const unsigned char*)p.K + ((size_t)wg << 20);
    const unsigned char* vb = (const unsigned char*)p.V + ((size_t)wg << 20);
    const int koff = ((lane >> 2) & 3) * 256 + (g * 4 + (lane & 3)) * 16, voff = lane * 16;
    const int wb = wave * 4096;
    constexpr int ntw = 32;
    const int radd0 = isW ? K_RA_W : (isMc ? K_RA_MC : 0);
    int radd = radd0, step = 0;
    auto pace = [&]() {
      if (K_WFB) {
        const int pg = (int)lds_add(L, C_PRG) - 8 * step;
        radd = radd0 + (pg > K_WFB_B2 ? 2 : (pg > K_WFB_B ? 1 : (pg < K_WFB_A2 ? -2 : (pg < K_WFB_A ? -1 : 0))));
        ++step;
      }
    };
#define LOADK(j, u) __builtin_amdgcn_raw_buffer_load_b128(mkr(kb, (j) < ntw ? (1u << 20) : 0u), koff, (j) * 32768 + wb + (u) * 1024, AUX_NT)
#define LOADV(j, u) __builtin_amdgcn_raw_buffer_load_b128(mkr(vb, (j) < ntw ? (1u << 20) : 0u), voff, (j) * 32768 + wb + (u) * 1024, AUX_NT)
#define SB() __builtin_amdgcn_sched_barrier(0)
#define PIN(x) do { asm volatile("" : "+v"(x)); SB(); } while (0)
    constexpr int NPK = 2 * (NP + 1);
    v4u ring[8], pre[P];
#pragma unroll
    for (int n = 0; n < 8 + P; ++n) {
      if (n == K_PF && !isMc) { RAWBAR(); asm volatile("s_waitcnt vmcnt(%1)" : "+v"(E7) : "n"(K_PF) : "memory"); __builtin_amdgcn_sched_barrier(0); say_cleared(); lds_add(L, C_DMA0); }
      if (n >= K_PF) throttle(K_PF);
      if (n < 8) ring[n] = LOADK(n >> 2, n & 3); else pre[n - 8] = LOADK(n >> 2, n & 3);
      SB();
    }
    const int nl = isMc ? 1 : K_NL;
    const int nlk = 2 * nl;
    const int kvl = (isMc ? L_KVM : L_KV) + wave * (nl * 8192);
    {
      const rsrc_t rk = mkr(kb, 1 << 20);
#pragma unroll 1
      for (int n = 0; n < 4 * nlk; ++n) {
        throttle(K_PF);
        dma16(rk, koff, (NPK + (n >> 2)) * 32768 + wb + (n & 3) * 1024, ldsb + kvl + n * 1024);
      }
    }
    unsigned sel[4];
    {
      const int i = lane & 15, beta = i >> 2, pos = i & 1;
      const unsigned b0 = 2 * ((i >> 1) & 1), b1 = b0 + 1;
#pragma unroll
      for (int w = 0; w < 4; ++w) sel[w] = beta != w ? 0x0c0c0c0cu : (pos ? ((b1 << 24) | (b0 << 16) | 0x0c0cu) : (0x0c0c0000u | (b1 << 8) | b0));
    }
    RAWBAR();
    v4u qa0, qa1;
    {
      const int i = lane & 15;
      const v4u qf = lds128(L, L_Q + ((i >> 2) & 1) * 256 + (4 * g + (i & 3)) * 16);
      const v4u z = {0u, 0u, 0u, 0u};
      qa0 = (i >> 3) ? z : qf;
      qa1 = (i >> 3) ? qf : z;
    }
    const f4 zero = {0.f, 0.f, 0.f, 0.f};
    const int tn = NPK + nlk;
    const int scw = L_SC + wave * SCW;
    const int sca = scw + ((lane >> 2) & 3) * 16 + (2 * (lane & 1) + (lane >> 5)) * 4 + ((lane >> 4) & 1) * 2;
    const int scs = 16 * (lane & 1);
    float mx = -INFINITY;
    unsigned sv[NPK + 2 * K_NL];
    {
      f4 D0 = mfma(qa0, ring[0], zero); PIN(D0); ring[0] = LOADK(tn, 0); SB();
      D0 = mfma(qa1, ring[1], D0);      PIN(D0); ring[1] = LOADK(tn, 1); SB();
      f4 D1 = mfma(qa0, ring[2], zero); PIN(D1); throttle(3); ring[2] = LOADK(tn, 2); SB();
      D1 = mfma(qa1, ring[3], D1);      PIN(D1); throttle(3); ring[3] = LOADK(tn, 3); SB();
      sv[0] = score_pair(D0, D1);
      D0 = mfma(qa0, ring[4], zero); D0 = mfma(qa1, ring[5], D0);
      D1 = mfma(qa0, ring[6], zero); D1 = mfma(qa1, ring[7], D1);
      sv[1] = score_pair(D0, D1);
      PIN(sv[1]);
      static_assert(NPK == 6 && K_NL == 2, "the interleave below is written for four register tiles and four parked tiles");
#define WAITING(t) do { \
        if ((t) < NPK) { \
          D0 = mfma(qa0, pre[(4 * (t) - 8) & (P - 1)], zero); D0 = mfma(qa1, pre[(4 * (t) - 7) & (P - 1)], D0); \
          D1 = mfma(qa0, pre[(4 * (t) - 6) & (P - 1)], zero); D1 = mfma(qa1, pre[(4 * (t) - 5) & (P - 1)], D1); \
          sv[t] = score_pair(D0, D1); \
        } else { \
          sv[t] = 0xff80ff80u;        \
          if ((t) - NPK < nlk) { \
            const int a = kvl + ((t) - NPK) * 4096 + lane * 16; \
            D0 = mfma(qa0, lds128(L, a), zero); D0 = mfma(qa1, lds128(L, a + 1024), D0); \
            D1 = mfma(qa0, lds128(L, a + 2048), zero); D1 = mfma(qa1, lds128(L, a + 3072), D1); \
            sv[t] = score_pair(D0, D1); \
          } \
        } \
        PIN(sv[t]); } while (0)
      throttle(3); ring[4] = LOADK(tn + 1, 0); SB(); WAITING(2); WAITING(3);
      throttle(3); ring[5] = LOADK(tn + 1, 1); SB(); WAITING(4); WAITING(5);
      throttle(3); ring[6] = LOADK(tn + 1, 2); SB();
      VMWAIT(7);
      WAITING(6); WAITING(7);
      throttle(3); ring[7] = LOADK(tn + 1, 3); SB(); WAITING(8); WAITING(9);
#undef WAITING
      LGWAIT();
      lds_add(L, C_KVL);
      lds_wait(L, C_KVL, 8);
#pragma unroll
      for (int t = 0; t < NPK + 2 * K_NL; ++t) {
        if (t < tn) {
          mx = fmaxf(mx, fmaxf(bflo(sv[t]), bfhi(sv[t])));
          *(LAS u16*)(L + sca + t * 64) = (u16)(sv[t] >> scs);
        }
      }
    }
#pragma unroll 1
    for (int j = tn; j < ntw; j += 2) {
      if (j >= K_TRIG_WO && j < K_TRIG_WO + 2 && lane == 0 && wave == 0) stw(L, C_TRIG, 1u);
      pace();
      const bool more = j + 2 < ntw;
      const unsigned char* nb = more ? kb : vb;
      const int nj = more ? j + 2 : 0, no = more ? koff : voff;
#define LOADN(jj, u) __builtin_amdgcn_raw_buffer_load_b128(mkr(nb, 1u << 20), no, (jj) * 32768 + wb + (u) * 1024, AUX_NT)
      f4 D0 = mfma(qa0, ring[0], zero); PIN(D0); throttle(rdigit(K_RPAT, 0) + (radd > 0 ? 1u : 0u)); ring[0] = LOADN(nj, 0); SB();
      D0 = mfma(qa1, ring[1], D0);      PIN(D0); throttle(rdigit(K_RPAT, 1)); ring[1] = LOADN(nj, 1); SB();
      f4 D1 = mfma(qa0, ring[2], zero); PIN(D1); throttle(rdigit(K_RPAT, 2) + (radd > 2 ? 1u : 0u)); ring[2] = LOADN(nj, 2); SB();
      D1 = mfma(qa1, ring[3], D1);      PIN(D1); throttle(rdigit(K_RPAT, 3) - (radd < -1 ? 1u : 0u)); ring[3] = LOADN(nj, 3); SB();
      const unsigned s0 = score_pair(D0, D1);
      mx = fmaxf(mx, fmaxf(bflo(s0), bfhi(s0)));
      *(LAS u16*)(L + sca + j * 64) = (u16)(s0 >> scs);
      D0 = mfma(qa0, ring[4], zero); PIN(D0); throttle(rdigit(K_RPAT, 4) + (radd > 1 ? 1u : 0u)); ring[4] = LOADN(nj + 1, 0); SB();
      D0 = mfma(qa1, ring[5], D0);   PIN(D0); throttle(rdigit(K_RPAT, 5)); ring[5] = LOADN(nj + 1, 1); SB();
      D1 = mfma(qa0, ring[6], zero); PIN(D1); throttle(rdigit(K_RPAT, 6) + (radd > 3 ? 1u : 0u)); ring[6] = LOADN(nj + 1, 2); SB();
      D1 = mfma(qa1, ring[7], D1);   PIN(D1); throttle(rdigit(K_RPAT, 7) - (radd < 0 ? 1u : 0u)); ring[7] = LOADN(nj + 1, 3); SB();
#undef LOADN
      const unsigned s1 = score_pair(D0, D1);
      mx = fmaxf(mx, fmaxf(bflo(s1), bfhi(s1)));
      *(LAS u16*)(L + sca + (j + 1) * 64) = (u16)(s1 >> scs);
    }
    mx = fmaxf(mx, dpp<ROR4>(mx)); mx = fmaxf(mx, dpp<ROR8>(mx)); mx = x32max(mx);
    float m0h, m1h, l0 = 0.f, l1 = 0.f;
    {
      auto t = __builtin_amdgcn_permlane16_swap(asu(mx), asu(mx), false, false);
      m0h = asf(t[0]); m1h = asf(t[1]);
      LGWAIT();
#pragma unroll 3
      for (int i = lane; i < ntw * 16; i += 64) {
        const unsigned s = *(LAS unsigned*)(L + scw + i * 4);
        const float p0 = __builtin_amdgcn_exp2f((bflo(s) - m0h) * LOG2E), p1 = __builtin_amdgcn_exp2f((bfhi(s) - m1h) * LOG2E);
        l0 += p0; l1 += p1;
        *(LAS unsigned*)(L + scw + i * 4) = pk(p0, p1);
      }
      l0 = x32sum(x16sum(row16sum(l0))); l1 = x32sum(x16sum(row16sum(l1)));
      LGWAIT();
    }
    f4 acc = zero;
#pragma unroll 1
    for (int j = 0; j < ntw; j += 2) {
      const bool tail = j + K_TTAIL / 4 >= ntw;
      pace();
      const v4u pa = lds128(L, scw + j * 64 + g * 16), pb = lds128(L, scw + j * 64 + 64 + g * 16);
      acc = mfma(pfrag(pa.x, sel), ring[0], acc); PIN(acc); throttle(tail ? K_RTAIL : rdigit(K_RPAT, 0) + (radd > 0 ? 1u : 0u)); ring[0] = LOADV(j + 2, 0); SB();
      acc = mfma(pfrag(pa.y, sel), ring[1], acc); PIN(acc); throttle(tail ? K_RTAIL : rdigit(K_RPAT, 1)); ring[1] = LOADV(j + 2, 1); SB();
      acc = mfma(pfrag(pa.z, sel), ring[2], acc); PIN(acc); throttle(tail ? K_RTAIL : rdigit(K_RPAT, 2) + (radd > 2 ? 1u : 0u)); ring[2] = LOADV(j + 2, 2); SB();
      acc = mfma(pfrag(pa.w, sel), ring[3], acc); PIN(acc); throttle(tail ? K_RTAIL : rdigit(K_RPAT, 3) - (radd < -1 ? 1u : 0u)); ring[3] = LOADV(j + 2, 3); SB();
      acc = mfma(pfrag(pb.x, sel), ring[4], acc); PIN(acc); throttle(tail ? K_RTAIL : rdigit(K_RPAT, 4) + (radd > 1 ? 1u : 0u)); ring[4] = LOADV(j + 3, 0); SB();
      acc = mfma(pfrag(pb.y, sel), ring[5], acc); PIN(acc); throttle(tail ? K_RTAIL : rdigit(K_RPAT, 5)); ring[5] = LOADV(j + 3, 1); SB();
      acc = mfma(pfrag(pb.z, sel), ring[6], acc); PIN(acc); throttle(tail ? K_RTAIL : rdigit(K_RPAT, 6) + (radd > 3 ? 1u : 0u)); ring[6] = LOADV(j + 3, 2); SB();
      acc = mfma(pfrag(pb.w, sel), ring[7], acc); PIN(acc); throttle(tail ? K_RTAIL : rdigit(K_RPAT, 7) - (radd < 0 ? 1u : 0u)); ring[7] = LOADV(j + 3, 3); SB();
    }
#undef LOADK
#undef LOADV
    if (lds_add(L, C_END) == 7u && lane == 0) stw(L, C_TRIG, 2u);
    LAS float* mg = (LAS float*)(L + scw);
    const int cc = lane & 15;
    *(LAS f2*)(mg + 8 + 8 * cc + 2 * g) = f2{acc.x, acc.y};
    *(LAS f2*)(mg + 8 + 128 + 8 * cc + 2 * g) = f2{acc.z, acc.w};
    if (lane == 0) *(LAS f4*)mg = f4{m0h, l0, m1h, l1};
    LGWAIT();
    if (lds_add(L, C_MRG) == 7u) {
      MEMBAR();
      const int h = lane >> 5, d0 = 4 * (lane & 31);
      LAS float* m0 = (LAS float*)(L + L_SC);
      f2 ml[8]; f4 a[8];
#pragma unroll
      for (int w = 0; w < 8; ++w) { ml[w] = *(LAS f2*)(m0 + w * (SCW / 4) + 2 * h); a[w] = *(LAS f4*)(m0 + w * (SCW / 4) + 8 + 128 * h + d0); }
      float M = ml[0].x;
#pragma unroll
      for (int w = 1; w < 8; ++w) M = fmaxf(M, ml[w].x);
      float Ls = 0.f; f4 O = {0.f, 0.f, 0.f, 0.f};
#pragma unroll
      for (int w = 0; w < 8; ++w) { const float e = __builtin_amdgcn_exp2f((ml[w].x - M) * LOG2E); Ls += e * ml[w].y; O += e * a[w]; }
      const float inv = __builtin_amdgcn_rcpf(Ls);
      const unsigned o0 = pk(O.x * inv, O.y * inv), o1 = pk(O.z * inv, O.w * inv);
      const unsigned n0 = dppu<QX1>(o0), n1 = dppu<QX1>(o1);
      if (!(lane & 1)) stsc1(san4(v4u{o0, o1, n0, n1}), rws, WS_OP + aslot * 512 + (h * 128 + d0) * 2);
      const unsigned M1 = __builtin_amdgcn_readlane(asu(M), 32), L1 = __builtin_amdgcn_readlane(asu(Ls), 32);
      if (lane == 0) stsc1(san4(v4u{asu(M), asu(Ls), M1, L1}), rws, WS_ML + aslot * 16);
    }
  } else {
    RAWBAR();
    const int sw = wave - 8, stid = tid - 512;
    const u64 Es = __hip_atomic_load((GAS u64*)(p.ws + WS_EPOCH), __ATOMIC_RELAXED, __HIP_MEMORY_SCOPE_AGENT);
    const int fz = (q4 >> 1) | ((g & 1) << 1);
    auto mod_mfma = [&]() {
      f4 D[3] = {{0.f, 0.f, 0.f, 0.f}, {0.f, 0.f, 0.f, 0.f}, {0.f, 0.f, 0.f, 0.f}};
#pragma unroll
      for (int ks = 0; ks < 18; ++ks) {
        const v4u wf = trfrag(L, L_W + (32 * ks + 8 * g + q4) * 128 + (sw ^ fz) * 32 + p4 * 8, 4 * 128);
        const v4u sf = lds128(L, L_SIMG + (lane & 7) * XSTR + (32 * ks + 8 * g) * 2);
        D[ks % 3] = mfma(wf, sf, D[ks % 3]);
      }
      const f4 Ds = (D[0] + D[1]) + D[2];
      if ((lane & 15) < 8) stsc1_2(v2u{san(pk(Ds.x, Ds.y)), san(pk(Ds.z, Ds.w))}, rws, WS_MOD + ((lane & 7) * NMOD + wg * 64 + sw * 16 + 4 * g) * 2);
    };
    const u64 tagv = mix64((((u64)__builtin_amdgcn_readfirstlane((unsigned)(Es >> 32)) << 32) | (unsigned)__builtin_amdgcn_readfirstlane((unsigned)Es)) + 1);
    const unsigned tg0 = (unsigned)tagv, tg1 = (unsigned)(tagv >> 32);
    if (isMc) {
      if (sw == 0) {
        const v4u cv = __builtin_amdgcn_raw_buffer_load_b128(mkr(p.c, NB * DM * 2), tid * 16, 0, 0);
        v4u o;
#pragma unroll
        for (int e = 0; e < 4; ++e) o[e] = pk(silu_acc(bflo(cv[e])), silu_acc(bfhi(cv[e])));
        *(LAS v4u*)(L + L_SIMG + (tid / 72) * XSTR + (tid % 72) * 16) = o;
        LGWAIT();
        lds_add(L, C_SYNC1);
      }
      lds_wait(L, C_SYNC1, 1); lds_wait(L, C_DMA0, 8);
      mod_mfma();
    } else if (isQ) {
      const int qb = 2 * sw + (lane >> 5), qj = lane & 31;
      const bool qact = qj < 24;
      const rsrc_t rx = mkr(p.x, NB * DM * 2);
      v4u xv[3];
#pragma unroll
      for (int t = 0; t < 3; ++t) xv[t] = __builtin_amdgcn_raw_buffer_load_b128(rx, qact ? (qb * DM + 192 * t + 8 * qj) * 2 : 0x7ffffff0, 0, 0);
      float xf[3][8];
      float sum = 0.f;
#pragma unroll
      for (int t = 0; t < 3; ++t)
#pragma unroll
        for (int e = 0; e < 4; ++e) { xf[t][2 * e] = bflo(xv[t][e]); xf[t][2 * e + 1] = bfhi(xv[t][e]); sum += xf[t][2 * e] + xf[t][2 * e + 1]; }
      sum = x16sum(row16sum(sum));
      const float mean = sum * (1.f / DM);
      float vs = 0.f;
#pragma unroll
      for (int t = 0; t < 3; ++t)
#pragma unroll
        for (int e = 0; e < 8; ++e) { const float d = xf[t][e] - mean; vs += qact ? d * d : 0.f; }
      vs = x16sum(row16sum(vs));
      const float mb = rb(mean), den = rb(__builtin_sqrtf(rb(rb(vs * (1.f / DM)) + 1e-5f)));
      float ln[8];
#pragma unroll
      for (int e = 0; e < 8; ++e) { const float xe = qkq == 0 ? xf[0][e] : (qkq == 1 ? xf[1][e] : xf[2][e]); ln[e] = rb(rb(xe - mb) / den); }
      lds_wait(L, C_DMA0, 8);
      v4u wf[6];
#pragma unroll
      for (int ks = 0; ks < 6; ++ks) wf[ks] = trfrag(L, L_W + (32 * ks + 8 * g + q4) * 128 + (sw ^ fz) * 32 + p4 * 8, 4 * 128);
      v4u sa, ca;
      const int mo = qact ? WS_MOD + (qb * NMOD + 192 * qkq + 8 * qj) * 2 : 0x7ffffff0;
      if (sw == 0) {
        const int pw = (lane < 3 ? 0 : 6) + 3 * qkq + lane;
        const int fo = lane < 6 ? WS_CLR + pw * 8 : 0x7ffffff0, io = lane < 6 ? WS_MOD + (7 * NMOD + pw * 64 + 62) * 2 : 0x7ffffff0;
        bool cleared = false;
        for (unsigned spins = 0;;) {
          MEMBAR();
          if (!cleared) { const v2u fl = ldsc1_2(rws, fo); cleared = __all(lane >= 6 || (fl.x == tg0 && fl.y == tg1)); }
          else if (__all(__builtin_amdgcn_raw_buffer_load_b32(rws, io, 0, AUX_SC1) != SENT)) break;
          if (give_up(spins, L)) break;
          __builtin_amdgcn_s_sleep(1);
        }
        if (lane == 0) stw(L, C_HS, 1u);
      } else lds_wait(L, C_HS, 1);
      for (unsigned spins = 0;;) {
        MEMBAR();
        sa = ldsc1(rws, mo); ca = ldsc1(rws, qact ? mo + DM * 2 : mo);
        if (__all(umax(max4(sa), max4(ca)) != SENT)) break;
        if (give_up(spins, L)) break;
        __builtin_amdgcn_s_sleep(1);
      }
      v4u h;
#pragma unroll
      for (int e = 0; e < 4; ++e) {
        const unsigned c1 = pk(1.f + bflo(ca[e]), 1.f + bfhi(ca[e]));
        const unsigned m1 = pk(ln[2 * e] * bflo(c1), ln[2 * e + 1] * bfhi(c1));
        h[e] = pk(bflo(m1) + bflo(sa[e]), bfhi(m1) + bfhi(sa[e]));
      }
      if (qact) *(LAS v4u*)(L + L_HIMG + qb * HSTR + qj * 16) = h;
      LGWAIT();
      lds_add(L, C_SYNC1); lds_wait(L, C_SYNC1, 4);
      f4 D0 = {0.f, 0.f, 0.f, 0.f}, D1 = {0.f, 0.f, 0.f, 0.f};
#pragma unroll
      for (int ks = 0; ks < 6; ks += 2) {
        D0 = mfma(wf[ks], lds128(L, L_HIMG + (lane & 7) * HSTR + (32 * ks + 8 * g) * 2), D0);
        D1 = mfma(wf[ks + 1], lds128(L, L_HIMG + (lane & 7) * HSTR + (32 * ks + 32 + 8 * g) * 2), D1);
      }
      if ((lane & 15) < 8) stsc1(san4(__builtin_bit_cast(v4u, D0 + D1)), rws, WS_QP + ((qkq * 8 + (lane & 7)) * NQ + qcg * 64 + sw * 16 + 4 * g) * 4);
    }
    if (sw == 0) {
      v4u a0, a1, a2;
      const int qo = WS_QP + (ab * NQ + akv * 256 + 4 * lane) * 4;
      const int fo = lane < 12 ? WS_CLR + (54 + 12 * akv + lane) * 8 : 0x7ffffff0;
      const int io = lane < 12 ? WS_QP + (((lane % 3) * 8 + ab) * NQ + (4 * akv + lane / 3) * 64 + 63) * 4 : 0x7ffffff0;
      bool cleared = false;
      for (unsigned spins = 0;;) {
        MEMBAR();
        if (!cleared) { const v2u fl = ldsc1_2(rws, fo); cleared = __all(lane >= 12 || (fl.x == tg0 && fl.y == tg1)); }
        else if (__all(__builtin_amdgcn_raw_buffer_load_b32(rws, io, 0, AUX_SC1) != SENT)) break;
        if (give_up(spins, L)) break;
        __builtin_amdgcn_s_sleep(1);
      }
      for (unsigned spins = 0;;) {
        MEMBAR();
        a0 = ldsc1(rws, qo); a1 = ldsc1(rws, qo + NB * NQ * 4); a2 = ldsc1(rws, qo + 2 * NB * NQ * 4);
        if (__all(umax(umax(max4(a0), max4(a1)), max4(a2)) != SENT)) break;
        if (give_up(spins, L)) break;
        __builtin_amdgcn_s_sleep(1);
      }
      const f4 s = (__builtin_bit_cast(f4, a0) + __builtin_bit_cast(f4, a1)) + __builtin_bit_cast(f4, a2);
      *(LAS v2u*)(L + L_Q + lane * 8) = v2u{pk(s.x, s.y), pk(s.z, s.w)};
    }
    RAWBAR();
    if (K_TLBW && sw == 2) {
      unsigned t = __builtin_amdgcn_raw_buffer_load_b32(mkr((const unsigned char*)p.V + ((size_t)wg << 20), 1 << 20), lane == 0 ? 0 : 0x7ffffff0, 0, AUX_NT);
      if (isF) {
        const int po = lane < 4 ? lane * 1769000 : 0x7ffffff0;
        t ^= __builtin_amdgcn_raw_buffer_load_b32(mkr(p.wg, DM * DFF * 2), po, 0, AUX_NT) ^ __builtin_amdgcn_raw_buffer_load_b32(mkr(p.wu, DM * DFF * 2), po, 0, AUX_NT);
        t ^= __builtin_amdgcn_raw_buffer_load_b32(mkr(p.wd, DFF * DM * 2), lane == 0 ? f * 32 * DM * 2 : 0x7ffffff0, 0, AUX_NT);
        t ^= __builtin_amdgcn_raw_buffer_load_b32(mkr(p.out, NB * DM * 2), lane == 0 ? 0 : 0x7ffffff0, 0, AUX_NT);
      }
      asm volatile("" ::"v"(t));
    }
    if (isF && sw == 3) {
      const v4u ss = {SENT, SENT, SENT, SENT};
#pragma unroll 3
      for (int j = 0; j < 9; ++j) stsc1(ss, rws, WS_DP + ((j * 8 + (lane >> 3)) * 144 + f) * 128 + (lane & 7) * 16);
      VMWAIT(0);
      if (lane == 0) __hip_atomic_store((GAS u64*)(p.ws + WS_CLR2) + f, tagv, __ATOMIC_RELAXED, __HIP_MEMORY_SCOPE_AGENT);
    }
    if (isM && !isMc) {
      lds_wait(L, C_DMA0, 8); lds_wait(L, C_KVL, 8);
      const rsrc_t rw = mkr(p.wm, DM * NMOD * 2);
      const int c16 = lane & 7;
#pragma unroll 1
      for (int j = 0; j < 18; ++j) {
        const int i = sw * 18 + j;
        const int fs = ((lane >> 4) & 1) | ((i & 1) << 1);
        dma16(rw, (lane >> 3) * (NMOD * 2) + ((((c16 >> 1) ^ fs) << 1) | (c16 & 1)) * 16 + wg * 128, i * 8 * (NMOD * 2), ldsb + L_W + i * 1024);
        __builtin_amdgcn_s_sleep(K_WOPACE);
      }
      const rsrc_t rc = mkr(p.c, NB * DM * 2);
#pragma unroll 1
      for (int j = 0; j < 3; ++j) {
        const int ch = stid + 256 * j;
        const v4u cv = __builtin_amdgcn_raw_buffer_load_b128(rc, ch * 16, 0, 0);
        v4u o;
#pragma unroll
        for (int e = 0; e < 4; ++e) o[e] = pk(silu_acc(bflo(cv[e])), silu_acc(bfhi(cv[e])));
        if (ch < 576) *(LAS v4u*)(L + L_SIMG + (ch / 72) * XSTR + (ch % 72) * 16) = o;
      }
      VMWAIT(0); LGWAIT();
      lds_add(L, C_SYNC1); lds_wait(L, C_SYNC1, 4);
      mod_mfma();
    }
    if ((isF || isW) && sw == 3) {
      for (unsigned spins = 0;;) {
        MEMBAR();
        const v4u a = ldsc1(rws, WS_CLR + lane * 32), b = ldsc1(rws, WS_CLR + lane * 32 + 16);
        bool ok = a.x == tg0 && a.y == tg1 && a.z == tg0 && a.w == tg1 && b.x == tg0 && b.y == tg1 && b.z == tg0 && b.w == tg1;
        if (isF) {
          const v4u c2 = ldsc1(rws, lane < 36 ? WS_CLR2 + lane * 32 : 0x7ffffff0), d2 = ldsc1(rws, lane < 36 ? WS_CLR2 + lane * 32 + 16 : 0x7ffffff0);
          ok = ok && (lane >= 36 || (c2.x == tg0 && c2.y == tg1 && c2.z == tg0 && c2.w == tg1 && d2.x == tg0 && d2.y == tg1 && d2.z == tg0 && d2.w == tg1));
        }
        if (__all(ok)) break;
        if (give_up(spins, L)) break;
        __builtin_amdgcn_s_sleep(127);
      }
      if (lane == 0) stw(L, C_HS2, 1u);
    }
    if (isW || isF) {
      if (isM && !isMc) { lds_add(L, C_MDONE); lds_wait(L, C_MDONE, 4); }
      lds_wait(L, C_KVL, 8);
      const int sc = ((lane & 3) ^ ((lane >> 4) & 2)) * 16;
      if (isW) {
        const rsrc_t rw = mkr(p.wo, NQ * DM * 2);
        const int vo = (lane >> 2) * (DM * 2) + sc + wng * 64;
        lds_wait<16>(L, C_TRIG, 1);
#pragma unroll 1
        for (int j = 0; j < 32; ++j) { const int i = j * 4 + sw; dma16(rw, vo, (wsig * 2048 + i * 16) * DM * 2, ldsb + L_W + i * 1024); __builtin_amdgcn_s_sleep(K_WOPACE); }
        VMWAIT(0);
        lds_add(L, C_DMA1);
      } else {
        const rsrc_t rg = mkr(p.wg, DM * DFF * 2), ru = mkr(p.wu, DM * DFF * 2), rd = mkr(p.wd, DFF * DM * 2);
        const int vo = (lane >> 2) * (DFF * 2) + sc + f * 64;
        lds_wait<4>(L, C_TRIG, 2);
#pragma unroll 1
        for (int j = 0; j < 9; ++j) {
          const int i = j * 4 + sw;
          throttle(K_DMAR); dma16(rg, vo, i * 16 * DFF * 2, ldsb + L_W + i * 1024);
          throttle(K_DMAR); dma16(ru, vo, i * 16 * DFF * 2, ldsb + L_WU + i * 1024);
        }
#pragma unroll 1
        for (int j = 0; j < 9; ++j) {
          throttle(K_DMAR);
          if (j == K_DMAR - 1) lds_add(L, C_DMA1);
          const int i = j * 4 + sw; dma16(rd, lane * 16, f * 32 * DM * 2 + i * 1024, ldsb + L_WD + i * 1024);
        }
        static_assert(K_DMAR >= 1 && K_DMAR <= 6, "the throttle counts up to six");
        VMWAIT(0);
        lds_add(L, C_DMA2);
      }
    }
  }

  if (isW) {
    const int cc = lane & 15;
    if (wave < 8) {
      const int pid = (cc & 7) * 32 + (wsig * 8 + wave) * 2 + (cc >> 3);
      lds_wait(L, C_HS2, 1);
      lds_wait(L, C_DMA1, 4);
      v4u wf[2][4][2];
#pragma unroll
      for (int hh = 0; hh < 2; ++hh)
#pragma unroll
        for (int ks = 0; ks < 4; ++ks)
#pragma unroll
          for (int nt = 0; nt < 2; ++nt) wf[hh][ks][nt] = trfrag(L, L_W + (wave * 256 + hh * 128 + 32 * ks + 8 * g + q4) * 64 + (nt ^ (g & 1)) * 32 + p4 * 8, 4 * 64);
      v4u ml, fr[2][4];
      for (unsigned spins = 0;;) {
        MEMBAR();
        ml = ldsc1(rws, WS_ML + pid * 16);
        const int nv = __builtin_popcountll(__builtin_amdgcn_ballot_w64(max4(ml) != SENT));
        if (nv >= 4 * 15) break;
        if (give_up(spins, L)) break;
        if (nv < 4 * 12) __builtin_amdgcn_s_sleep(24); else __builtin_amdgcn_s_sleep(1);
      }
      for (unsigned spins = 0;;) {
        MEMBAR();
        ml = ldsc1(rws, WS_ML + pid * 16);
#pragma unroll
        for (int hh = 0; hh < 2; ++hh)
#pragma unroll
          for (int ks = 0; ks < 4; ++ks) fr[hh][ks] = ldsc1(rws, WS_OP + pid * 512 + (hh * 128 + 32 * ks + 8 * g) * 2);
        unsigned mx = max4(ml);
#pragma unroll
        for (int hh = 0; hh < 2; ++hh)
#pragma unroll
          for (int ks = 0; ks < 4; ++ks) mx = umax(mx, max4(fr[hh][ks]));
        if (__all(mx != SENT)) break;
        if (give_up(spins, L)) break;
      }
      float wgt[2];
#pragma unroll
      for (int hh = 0; hh < 2; ++hh) {
        const float M = asf(ml[2 * hh]), Lv = asf(ml[2 * hh + 1]);
        const float M2 = dpp<ROR8>(M), L2 = dpp<ROR8>(Lv), Mx = fmaxf(M, M2);
        const float e1 = __builtin_amdgcn_exp2f((M - Mx) * LOG2E) * Lv, e2 = __builtin_amdgcn_exp2f((M2 - Mx) * LOG2E) * L2;
        wgt[hh] = e1 * __builtin_amdgcn_rcpf(e1 + e2);
      }
#pragma unroll
      for (int nt = 0; nt < 2; ++nt) {
        f4 d0 = {0.f, 0.f, 0.f, 0.f}, d1 = {0.f, 0.f, 0.f, 0.f};
#pragma unroll
        for (int ks = 0; ks < 4; ++ks) { d0 = mfma(wf[0][ks][nt], fr[0][ks], d0); d1 = mfma(wf[1][ks][nt], fr[1][ks], d1); }
        f4 t = wgt[0] * d0 + wgt[1] * d1;
        t.x += dpp<ROR8>(t.x); t.y += dpp<ROR8>(t.y); t.z += dpp<ROR8>(t.z); t.w += dpp<ROR8>(t.w);
        if (cc < 8) *(LAS f4*)(L + L_RED + (((wave * 2 + nt) * 4 + g) * 8 + cc) * 16) = t;
      }
      LGWAIT();
      if (lds_add(L, C_RED) == 7u) {
        MEMBAR();
        f4 s = {0.f, 0.f, 0.f, 0.f};
#pragma unroll
        for (int w = 0; w < 8; ++w) s += *(LAS f4*)(L + L_RED + (w * 64 + lane) * 16);
        const int nt = lane >> 5, rr = (lane >> 3) & 3, b = lane & 7;
        stsc1(san4(__builtin_bit_cast(v4u, s)), rws, WS_YP + ((wsig * 8 + b) * DM + wng * 32 + nt * 16 + 4 * rr) * 4);
      }
    }
  } else if (isF) {
    const bool isR = f < 72;
    const int rb_ = f / 9, rc0 = (f % 9) * 64;
    unsigned gbw = 0;
    const int gt = wave & 3, kt = wave >> 2;
    v4u wf[6], wd[4];
    const int dw = wave - 3;
    bool have_wd = false;
    auto read_wd = [&]() {
      lds_wait(L, C_DMA2, 4);
#pragma unroll
      for (int j = 0; j < 4; ++j) wd[j] = trfrag(L, L_WD + (8 * g + q4) * (DM * 2) + (dw >= 0 ? 4 * dw + j : 0) * 32 + p4 * 8, 4 * DM * 2);
    };
    if (wave < 8) {
      const int b = wave;
      const bool act = lane < 48;
      const rsrc_t rx = mkr(p.x, NB * DM * 2);
      v2u xv[3], ga[3], sb[3], cb[3];
      lds_wait(L, C_HS2, 1);
#pragma unroll
      for (int t = 0; t < 3; ++t) xv[t] = __builtin_amdgcn_raw_buffer_load_b64(rx, act ? (b * DM + 4 * (lane + 48 * t)) * 2 : 0x7ffffff0, 0, 0);
      for (unsigned spins = 0;;) {
        MEMBAR();
        unsigned mx = 0;
#pragma unroll
        for (int t = 0; t < 3; ++t) {
          const int o = act ? WS_MOD + (b * NMOD + 4 * (lane + 48 * t)) * 2 : 0x7ffffff0;
          ga[t] = ldsc1_2(rws, act ? o + 2 * DM * 2 : o); sb[t] = ldsc1_2(rws, act ? o + 3 * DM * 2 : o); cb[t] = ldsc1_2(rws, act ? o + 4 * DM * 2 : o);
          mx = umax(mx, umax(umax(umax(ga[t].x, ga[t].y), umax(sb[t].x, sb[t].y)), umax(cb[t].x, cb[t].y)));
        }
        if (wave == 0) { gbw = __builtin_amdgcn_raw_buffer_load_b32(rws, isR ? WS_MOD + (rb_ * NMOD + 5 * DM + rc0 + (lane & ~1)) * 2 : 0x7ffffff0, 0, AUX_SC1); mx = umax(mx, gbw); }
        if (__all(mx != SENT)) break;
        if (give_up(spins, L)) break;
      }
      float xf[12], gf[12], cf[12], sf[12];
#pragma unroll
      for (int t = 0; t < 3; ++t)
#pragma unroll
        for (int e = 0; e < 2; ++e) {
          xf[4 * t + 2 * e] = bflo(xv[t][e]); xf[4 * t + 2 * e + 1] = bfhi(xv[t][e]);
          gf[4 * t + 2 * e] = bflo(ga[t][e]); gf[4 * t + 2 * e + 1] = bfhi(ga[t][e]);
          cf[4 * t + 2 * e] = 1.f + bflo(cb[t][e]); cf[4 * t + 2 * e + 1] = 1.f + bfhi(cb[t][e]);
          sf[4 * t + 2 * e] = bflo(sb[t][e]); sf[4 * t + 2 * e + 1] = bfhi(sb[t][e]);
        }
#pragma unroll
      for (int e = 0; e < 12; ++e) asm volatile("" : "+v"(xf[e]), "+v"(gf[e]), "+v"(cf[e]), "+v"(sf[e]));
      lds_wait(L, C_DMA1, 4);
#pragma unroll
      for (int k6 = 0; k6 < 6; ++k6) wf[k6] = trfrag(L, ((gt >> 1) ? L_WU : L_W) + (32 * (kt * 6 + k6) + 8 * g + q4) * 64 + ((gt & 1) ^ (g & 1)) * 32 + p4 * 8, 4 * 64);
      if (wave >= 3 && __builtin_amdgcn_readfirstlane(ldsw(L, C_DMA2)) >= 4u) { read_wd(); have_wd = true; }
      if (wave == 0) {
        for (unsigned spins = 0;;) {
          MEMBAR();
          const unsigned v = __builtin_amdgcn_raw_buffer_load_b32(rws, lane == 0 ? WS_YP + (((f & 1) * 8 + (f & 7)) * DM + (f % 18) * 32) * 4 : 0x7ffffff0, 0, AUX_SC1);
          if (__builtin_amdgcn_readfirstlane(v) != SENT) break;
          if (give_up(spins, L)) break;
          __builtin_amdgcn_s_sleep(1);
        }
        if (lane == 0) stw(L, C_Y, 1u);
      } else lds_wait(L, C_Y, 1);
      v4u y0[3], y1[3];
      for (unsigned spins = 0;;) {
        MEMBAR();
        unsigned mx = 0;
#pragma unroll
        for (int t = 0; t < 3; ++t) {
          const int o = act ? WS_YP + (b * DM + 4 * (lane + 48 * t)) * 4 : 0x7ffffff0;
          y0[t] = ldsc1(rws, o); y1[t] = ldsc1(rws, act ? o + NB * DM * 4 : o);
        }
#pragma unroll
        for (int t = 0; t < 3; ++t) mx = umax(mx, umax(max4(y0[t]), max4(y1[t])));
        if (__all(mx != SENT)) break;
        if (give_up(spins, L)) break;
      }
      float x1[12];
#pragma unroll
      for (int e = 0; e < 12; ++e) x1[e] = __builtin_fmaf(gf[e], asf(y0[e >> 2][e & 3]) + asf(y1[e >> 2][e & 3]), xf[e]);
      const float sh = asf(__builtin_amdgcn_readfirstlane(asu(x1[0])));
      float s1 = 0.f, s2 = 0.f;
#pragma unroll
      for (int e = 0; e < 12; ++e) { const float d = x1[e] - sh; s1 += d; s2 = __builtin_fmaf(d, d, s2); }
      s1 = act ? s1 : 0.f; s2 = act ? s2 : 0.f;
      s1 += dpp<ROR1>(s1); s2 += dpp<ROR1>(s2); s1 += dpp<ROR2>(s1); s2 += dpp<ROR2>(s2);
      s1 += dpp<ROR4>(s1); s2 += dpp<ROR4>(s2); s1 += dpp<ROR8>(s1); s2 += dpp<ROR8>(s2);
      s1 = x16sum(s1); s2 = x16sum(s2); s1 = x32sum(s1); s2 = x32sum(s2);
      const float dm = s1 * (1.f / DM), var = fmaxf(__builtin_fmaf(-dm, dm, s2 * (1.f / DM)), 0.f);
      const float rstd = __builtin_amdgcn_rsqf(var + 1e-5f), mo = -(sh + dm) * rstd;
#pragma unroll
      for (int t = 0; t < 3; ++t) {
        float hv[4];
#pragma unroll
        for (int e = 0; e < 4; ++e) hv[e] = __builtin_fmaf(__builtin_fmaf(x1[4 * t + e], rstd, mo), cf[4 * t + e], sf[4 * t + e]);
        if (act) {
          *(LAS v2u*)(L + L_H2 + b * XSTR + 8 * (lane + 48 * t)) = v2u{pk(hv[0], hv[1]), pk(hv[2], hv[3])};
          const int col = 4 * (lane + 48 * t) - rc0;
          if (isR && b == rb_ && col >= 0 && col < 64) *(LAS f4*)(L + L_X1S + col * 4) = f4{x1[4 * t], x1[4 * t + 1], x1[4 * t + 2], x1[4 * t + 3]};
        }
      }
    } else {
      lds_wait(L, C_DMA1, 4);
#pragma unroll
      for (int k6 = 0; k6 < 6; ++k6) wf[k6] = trfrag(L, ((gt >> 1) ? L_WU : L_W) + (32 * (kt * 6 + k6) + 8 * g + q4) * 64 + ((gt & 1) ^ (g & 1)) * 32 + p4 * 8, 4 * 64);
      lds_wait(L, C_Y, 1);
      if (__builtin_amdgcn_readfirstlane(ldsw(L, C_DMA2)) >= 4u) { read_wd(); have_wd = true; }
    }
    RAWBAR();
    {
      f4 D0 = {0.f, 0.f, 0.f, 0.f}, D1 = {0.f, 0.f, 0.f, 0.f};
#pragma unroll
      for (int k6 = 0; k6 < 6; k6 += 2) {
        D0 = mfma(wf[k6], lds128(L, L_H2 + (lane & 7) * XSTR + (32 * (kt * 6 + k6) + 8 * g) * 2), D0);
        D1 = mfma(wf[k6 + 1], lds128(L, L_H2 + (lane & 7) * XSTR + (32 * (kt * 6 + k6 + 1) + 8 * g) * 2), D1);
      }
      if ((lane & 15) < 8) *(LAS f4*)(L + L_GU + (((kt * 4 + gt) * 4 + g) * 8 + (lane & 7)) * 16) = D0 + D1;
    }
    RAWBAR();
    if (!have_wd && wave >= 4) read_wd();
    if (tid < 256) {
      const int b = tid & 7, fc = tid >> 3;
      const int o = (((fc >> 4) * 4 + ((fc >> 2) & 3)) * 8 + b) * 16 + (fc & 3) * 4;
      float gs = 0.f, us = 0.f;
#pragma unroll
      for (int k3 = 0; k3 < 3; ++k3) { gs += *(LAS float*)(L + L_GU + k3 * 2048 + o); us += *(LAS float*)(L + L_GU + k3 * 2048 + 1024 + o); }
      const float a = rb(gelu_t(rb(gs))) * rb(us);
      *(LAS u16*)(L + L_ACT + b * ASTR + fc * 2) = (u16)pk(a, a);
      if (!have_wd && wave == 3) read_wd();
    }
    RAWBAR();
    if (wave >= 3) {
      const v4u af = lds128(L, L_ACT + (lane & 7) * ASTR + g * 16);
      const int b = lane & 7, hi = (lane >> 3) & 1;
      const f4 z4 = {0.f, 0.f, 0.f, 0.f};
      const f4 T0 = mfma(wd[0], af, z4), T1 = mfma(wd[1], af, z4), T2 = mfma(wd[2], af, z4), T3 = mfma(wd[3], af, z4);
      const f4 Da = hi ? T2 : T0, Db = hi ? T3 : T1;
      stsc1(san4(v4u{pk(Da.x, Da.y), pk(Da.z, Da.w), pk(Db.x, Db.y), pk(Db.z, Db.w)}), rws, WS_DP + ((b * 9 + dw) * 144 + f) * 128 + (hi * 4 + g) * 16);
    }
    if (isR) {
      v4u a, b2;
      const int o = WS_DP + f * 144 * 128 + tid * 16;
      for (unsigned spins = 0;;) {
        MEMBAR();
        a = ldsc1(rws, o); b2 = ldsc1(rws, tid < 384 ? o + 768 * 16 : 0x7ffffff0);
        if (__all(umax(max4(a), max4(b2)) != SENT)) break;
        if (give_up(spins, L)) break;
      }
      float s[8];
#pragma unroll
      for (int e = 0; e < 4; ++e) { s[2 * e] = bflo(a[e]) + bflo(b2[e]); s[2 * e + 1] = bfhi(a[e]) + bfhi(b2[e]); }
#pragma unroll
      for (int e = 0; e < 8; ++e) { float v = s[e]; v += dpp<ROR8>(v); v = x32sum(x16sum(v)); s[e] = v; }
      if (lane < 8) {
        *(LAS f4*)(L + L_RED2 + (wave * 8 + lane) * 32) = f4{s[0], s[1], s[2], s[3]};
        *(LAS f4*)(L + L_RED2 + (wave * 8 + lane) * 32 + 16) = f4{s[4], s[5], s[6], s[7]};
      }
      RAWBAR();
    }
    if (isR && wave == 0) {
      const int ro = (((lane >> 5) * 4 + ((lane >> 2) & 3)) * 8 + ((lane >> 4) & 1) * 4 + (lane & 3)) * 4;
      float dn = 0.f;
#pragma unroll
      for (int w = 0; w < 12; ++w) dn += *(LAS float*)(L + L_RED2 + w * 256 + ro);
      const float gv = (lane & 1) ? bfhi(gbw) : bflo(gbw);
      const float o = *(LAS float*)(L + L_X1S + lane * 4) + rb(gv * rb(dn));
      const unsigned two = pk(o, dpp<QX1>(o));
      if (!(lane & 1)) *(GAS unsigned*)(p.out + rb_ * DM + rc0 + lane) = two;
      if (wg == 0 && lane == 0) {
        const u64 E = ((u64)ldsw(L, C_E1) << 32) | ldsw(L, C_E0);
        __hip_atomic_store((GAS u64*)(p.ws + WS_EPOCH), E + 1, __ATOMIC_RELAXED, __HIP_MEMORY_SCOPE_AGENT);
      }
    }
  }
}

}

extern "C" void kernel_launch(void* const* d_in, const int* in_sizes, int n_in, void* d_out, int out_size, void* d_ws, size_t ws_size, hipStream_t stream) {
  ak::Params p;
  p.x = (const ak::u16*)d_in[0]; p.K = (const ak::u16*)d_in[1]; p.V = (const ak::u16*)d_in[2];
  p.wq = (const ak::u16*)d_in[3]; p.wo = (const ak::u16*)d_in[4];
  p.wg = (const ak::u16*)d_in[6]; p.wu = (const ak::u16*)d_in[7]; p.wd = (const ak::u16*)d_in[8];
  p.c = (const ak::u16*)d_in[9]; p.wm = (const ak::u16*)d_in[10];
  p.out = (ak::u16*)d_out; p.ws = (unsigned char*)d_ws;
  (void)hipFuncSetAttribute((const void*)ak::fused_kernel, hipFuncAttributeMaxDynamicSharedMemorySize, ak::L_END);
  ak::fused_kernel<<<ak::NWG, ak::NTHR, ak::L_END, stream>>>(p);
}
```

```cpp
#include <hip/hip_runtime.h>
#include <stdint.h>

namespace ak {

typedef unsigned short u16;
typedef unsigned long long u64;
typedef unsigned v4u __attribute__((ext_vector_type(4)));
typedef unsigned v2u __attribute__((ext_vector_type(2)));
typedef float f4 __attribute__((ext_vector_type(4)));
typedef float f2 __attribute__((ext_vector_type(2)));
typedef __bf16 bf8 __attribute__((ext_vector_type(8)));
typedef __bf16 bf2 __attribute__((ext_vector_type(2)));
typedef short s4v __attribute__((ext_vector_type(4)));
#define LAS __attribute__((address_space(3)))
#define GAS __attribute__((address_space(1)))
typedef __amdgpu_buffer_rsrc_t rsrc_t;

constexpr int K_RPAT = 23333333;
constexpr int K_RTAIL = 5;
constexpr int K_TTAIL = 40;
constexpr int K_RA_W = 2;
constexpr int K_RA_MC = 1;
constexpr int K_WFB = 1;
constexpr int K_WFB_B = 5;
constexpr int K_WFB_A = 2;
constexpr int K_WFB_B2 = 9;
constexpr int K_WFB_A2 = -2;
constexpr int K_PF = 3;
constexpr int K_NP = 2;
constexpr int K_NL = 2;
constexpr int K_TRIG_WO = 14;
constexpr int K_WOPACE = 20;
constexpr int K_DMAR = 5;

constexpr int NB = 8, DM = 576, DFF = 4608, NMOD = 3456, NQ = 4096;
constexpr int NWG = 256, NTHR = 768;
constexpr int NP = K_NP, P = NP * 8;
__host__ __device__ constexpr unsigned rdigit(unsigned v, int k) { for (int i = 7; i > k; --i) v /= 10; return v % 10; }
constexpr float LOG2E = 1.4426950408889634f;
constexpr float SCALE = 0.08838834764831845f;
constexpr unsigned SENT = 0xFFFFFFFFu;
constexpr int AUX_NT = 2, AUX_SC1 = 16;
constexpr unsigned SPIN_CAP = 1u << 15, LDS_CAP = 1u << 18;

constexpr int WS_EPOCH = 0;
constexpr int WS_CLR = 4096, WS_CLR2 = 6144;
constexpr int WS_MOD = 8192;
constexpr int WS_QP = 65536;
constexpr int WS_OP = WS_QP + 3 * NB * NQ * 4;
constexpr int WS_ML = WS_OP + NWG * 512;
constexpr int WS_YP = WS_ML + NWG * 16;
constexpr int WS_DP = WS_YP + 2 * NB * DM * 4;
constexpr int WS_END = WS_DP + 72 * 144 * 128;

constexpr int L_W = 0;
constexpr int L_SIMG = 73728;
constexpr int L_HIMG = 24576;
constexpr int HSTR = 400;
constexpr int L_WU = 36864, L_WD = 73728;
constexpr int L_GU = 131072;
constexpr int L_ACT = 110592;
constexpr int ASTR = 80;
constexpr int L_RED2 = L_ACT + 640;
constexpr int L_X1S = L_RED2 + 3072;
constexpr int L_H2 = L_X1S + 256;
constexpr int XSTR = 1168;
static_assert(L_H2 + 8 * XSTR <= 131072, "ffn lds");
constexpr int L_RED = 131072;
constexpr int SCW = 2304;
constexpr int L_SC = 141312;
constexpr int L_KV = 28672, L_KVM = 83968;
constexpr int L_Q = 160000;
constexpr int L_CTL = L_Q + 512;
constexpr int L_WML = L_CTL + 128;
constexpr int L_END = L_WML + 128;
static_assert(L_KV + K_NL * 65536 <= L_Q && L_KVM + 65536 <= L_Q && L_SC + 8 * SCW <= L_Q && L_END <= 163840, "lds map");
enum { C_SYNC1 = 0, C_HS = 1, C_TRIG = 2, C_MRG = 3, C_DEAD = 4, C_DMA0 = 5, C_DMA1 = 6, C_HS2 = 7, C_KVL = 10, C_Y = 11, C_DMA2 = 12, C_E0 = 8, C_E1 = 9, C_RED = 16, C_MDONE = 17, C_END = 18, C_PRG = 19, C_WML = 20 };

struct Params {
  const u16 *x, *K, *V, *wq, *wo, *wg, *wu, *wd, *c, *wm;
  u16* out;
  unsigned char* ws;
};

__device__ __forceinline__ float asf(unsigned u) { return __builtin_bit_cast(float, u); }
__device__ __forceinline__ unsigned asu(float f) { return __builtin_bit_cast(unsigned, f); }
__device__ __forceinline__ float bflo(unsigned u) { return asf(u << 16); }
__device__ __forceinline__ float bfhi(unsigned u) { return asf(u & 0xffff0000u); }
__device__ __forceinline__ unsigned pk(float a, float b) {
  f2 v = {a, b};
  return __builtin_bit_cast(unsigned, __builtin_convertvector(v, bf2));
}
__device__ __forceinline__ float rb(float a) { return bflo(pk(a, a)); }
__device__ __forceinline__ unsigned san(unsigned u) { return u < 0xFFFFFFFEu ? u : 0xFFFFFFFEu; }
__device__ __forceinline__ v4u san4(v4u v) { return v4u{san(v.x), san(v.y), san(v.z), san(v.w)}; }
__device__ __forceinline__ unsigned umax(unsigned a, unsigned b) { return a > b ? a : b; }
__device__ __forceinline__ unsigned max4(v4u v) { return umax(umax(v.x, v.y), umax(v.z, v.w)); }
template <int C> __device__ __forceinline__ float dpp(float x) {
  return asf((unsigned)__builtin_amdgcn_mov_dpp((int)asu(x), C, 0xf, 0xf, true));
}
template <int C> __device__ __forceinline__ unsigned dppu(unsigned x) { return (unsigned)__builtin_amdgcn_mov_dpp((int)x, C, 0xf, 0xf, true); }
constexpr int ROR1 = 0x121, ROR2 = 0x122, ROR4 = 0x124, ROR8 = 0x128, QX1 = 0xB1, QX2 = 0x4E;
__device__ __forceinline__ float x32max(float x) { auto t = __builtin_amdgcn_permlane32_swap(asu(x), asu(x), false, false); return fmaxf(asf(t[0]), asf(t[1])); }
__device__ __forceinline__ float x32sum(float x) { auto t = __builtin_amdgcn_permlane32_swap(asu(x), asu(x), false, false); return asf(t[0]) + asf(t[1]); }
__device__ __forceinline__ float x16sum(float x) { auto t = __builtin_amdgcn_permlane16_swap(asu(x), asu(x), false, false); return asf(t[0]) + asf(t[1]); }
__device__ __forceinline__ float row16sum(float x) { x += dpp<ROR1>(x); x += dpp<ROR2>(x); x += dpp<ROR4>(x); x += dpp<ROR8>(x); return x; }
__device__ __forceinline__ f4 mfma(v4u a, v4u b, f4 c) {
  return __builtin_amdgcn_mfma_f32_16x16x32_bf16(__builtin_bit_cast(bf8, a), __builtin_bit_cast(bf8, b), c, 0, 0, 0);
}
__device__ __forceinline__ rsrc_t mkr(const void* p, unsigned bytes) { return __builtin_amdgcn_make_buffer_rsrc((void*)p, 0, bytes, 0x00020000); }
__device__ __forceinline__ v4u ldsc1(rsrc_t r, int off) { return __builtin_amdgcn_raw_buffer_load_b128(r, off, 0, AUX_SC1); }
__device__ __forceinline__ v2u ldsc1_2(rsrc_t r, int off) { return __builtin_amdgcn_raw_buffer_load_b64(r, off, 0, AUX_SC1); }
__device__ __forceinline__ void stsc1(v4u v, rsrc_t r, int off) { __builtin_amdgcn_raw_buffer_store_b128(v, r, off, 0, AUX_SC1); }
__device__ __forceinline__ void stsc1_2(v2u v, rsrc_t r, int off) { __builtin_amdgcn_raw_buffer_store_b64(v, r, off, 0, AUX_SC1); }
#define MEMBAR() asm volatile("" ::: "memory")
#define VMWAIT(n) asm volatile("s_waitcnt vmcnt(" #n ")" ::: "memory")
#define LGWAIT() asm volatile("s_waitcnt lgkmcnt(0)" ::: "memory")
#define RAWBAR() asm volatile("s_waitcnt lgkmcnt(0)\n\ts_barrier" ::: "memory")
__device__ __forceinline__ void dma16(rsrc_t r, int voff, unsigned soff, unsigned ldsa) {
  unsigned keep;
  asm volatile("s_mov_b32 %0, m0\n\ts_mov_b32 m0, %1\n\ts_nop 2\n\tbuffer_load_dwordx4 %2, %3, %4 offen nt lds\n\ts_mov_b32 m0, %0" : "=&s"(keep) : "s"(ldsa), "v"(voff), "s"(r), "s"(soff) : "memory");
}
__device__ __forceinline__ v4u ld_hidden(rsrc_t r, int voff) {
  v4u v;
  asm volatile("buffer_load_dwordx4 %0, %1, %2, 0 offen" : "=&v"(v) : "v"(voff), "s"(r) : "memory");
  return v;
}
__device__ __forceinline__ v4u lds128(LAS unsigned char* L, int off) { return *(LAS v4u*)(L + off); }
__device__ __forceinline__ v4u trfrag(LAS unsigned char* L, int a0, int d) {
  s4v lo = __builtin_amdgcn_ds_read_tr16_b64_v4i16((LAS s4v*)(L + a0));
  s4v hi = __builtin_amdgcn_ds_read_tr16_b64_v4i16((LAS s4v*)(L + a0 + d));
  v2u l = __builtin_bit_cast(v2u, lo), h = __builtin_bit_cast(v2u, hi);
  return v4u{l.x, l.y, h.x, h.y};
}
__device__ __forceinline__ unsigned ldsw(LAS unsigned char* L, int w) { return *(volatile LAS unsigned*)(L + L_CTL + 4 * w); }
__device__ __forceinline__ void stw(LAS unsigned char* L, int w, unsigned v) { *(volatile LAS unsigned*)(L + L_CTL + 4 * w) = v; }
__device__ __forceinline__ unsigned lds_add(LAS unsigned char* L, int w) {
  unsigned o = 0;
  if ((threadIdx.x & 63) == 0) o = __hip_atomic_fetch_add((LAS unsigned*)(L + L_CTL + 4 * w), 1u, __ATOMIC_RELAXED, __HIP_MEMORY_SCOPE_WORKGROUP);
  return __builtin_amdgcn_readfirstlane(o);
}
template <int SLP = 1> __device__ __forceinline__ void lds_wait(LAS unsigned char* L, int w, unsigned n) {
  for (unsigned spins = 0; __builtin_amdgcn_readfirstlane(ldsw(L, w)) < n; ++spins) {
    if (__builtin_amdgcn_readfirstlane(ldsw(L, C_DEAD))) break;
    if (spins > LDS_CAP) { stw(L, C_DEAD, 1u); break; }
    __builtin_amdgcn_s_sleep(SLP);
  }
  MEMBAR();
}
__device__ __forceinline__ bool give_up(unsigned& spins, LAS unsigned char* L) {
  if (++spins > SPIN_CAP) { stw(L, C_DEAD, 1u); return true; }
  return (spins & 15u) == 0u && __builtin_amdgcn_readfirstlane(ldsw(L, C_DEAD)) != 0u;
}
__device__ __forceinline__ u64 mix64(u64 z) {
  z += 0x9E3779B97F4A7C15ull;
  z = (z ^ (z >> 30)) * 0xBF58476D1CE4E5B9ull;
  z = (z ^ (z >> 27)) * 0x94D049BB133111EBull;
  return z ^ (z >> 31);
}
__device__ __forceinline__ void throttle(unsigned r) {
  asm volatile("s_cmp_gt_u32 %0, 6\n\ts_cbranch_scc1 9f\n\ts_waitcnt vmcnt(5)\n\t"
               "s_cmp_gt_u32 %0, 5\n\ts_cbranch_scc1 9f\n\ts_waitcnt vmcnt(4)\n\t"
               "s_cmp_gt_u32 %0, 4\n\ts_cbranch_scc1 9f\n\ts_waitcnt vmcnt(3)\n\t"
               "s_cmp_gt_u32 %0, 3\n\ts_cbranch_scc1 9f\n\ts_waitcnt vmcnt(2)\n\t"
               "s_cmp_gt_u32 %0, 2\n\ts_cbranch_scc1 9f\n\ts_waitcnt vmcnt(1)\n\t"
               "s_cmp_gt_u32 %0, 1\n\ts_cbranch_scc1 9f\n\ts_waitcnt vmcnt(0)\n9:" ::"s"(r) : "scc", "memory");
}

__device__ __forceinline__ float sigm(float z) { return __builtin_amdgcn_rcpf(1.f + __builtin_amdgcn_exp2f(-LOG2E * z)); }
__device__ __forceinline__ float silu_acc(float a) {
  const float hi = 1.44269502162933349609375f, lo = 1.92596299112661746e-8f;
  const float t0 = -a * hi;
  const float r = __builtin_fmaf(-a, lo, __builtin_fmaf(-a, hi, -t0));
  const float e0 = __builtin_amdgcn_exp2f(fminf(t0, 126.f));
  const float d = 1.f + __builtin_fmaf(e0, r * 0.693147180559945f, e0);
  const float rc = __builtin_amdgcn_rcpf(d), q = a * rc;
  return __builtin_fmaf(__builtin_fmaf(-q, d, a), rc, q);
}
__device__ __forceinline__ float gelu_t(float x) { const float z = 0.7978845608028654f * (x + 0.044715f * x * x * x); return x * sigm(2.f * z); }

__device__ __forceinline__ float quadsum(f4 d) {
  float t = dpp<0x00>(d.x);
  t += dpp<0x55>(d.y); t += dpp<0xAA>(d.z); t += dpp<0xFF>(d.w);
  return t;
}
__device__ __forceinline__ unsigned score_pair(f4 D0, f4 D1) {
  const unsigned r1 = pk(quadsum(D0), quadsum(D1));
  return pk(bflo(r1) * SCALE, bfhi(r1) * SCALE);
}
__device__ __forceinline__ v4u pfrag(unsigned v, const unsigned (&sel)[4]) {
  return v4u{__builtin_amdgcn_perm(v, v, sel[0]), __builtin_amdgcn_perm(v, v, sel[1]), __builtin_amdgcn_perm(v, v, sel[2]), __builtin_amdgcn_perm(v, v, sel[3])};
}

__global__ void __launch_bounds__(NTHR, 3) fused_kernel(Params p) {
  extern __shared__ __attribute__((aligned(16))) unsigned char smem[];
  LAS unsigned char* L = (LAS unsigned char*)smem;
  const int tid = threadIdx.x, lane = tid & 63, wave = __builtin_amdgcn_readfirstlane(tid >> 6);
  const int wg = blockIdx.x;
  const unsigned ldsb = (unsigned)(size_t)L;
  if (tid < 32) *(LAS unsigned*)(L + L_CTL + 4 * tid) = 0u;
  const rsrc_t rws = mkr(p.ws, WS_END);
  const int xl = wg & 7, xi = wg >> 3;
  const bool isM = wg < 54, isMc = wg < 18, isQ = wg >= 54 && wg < 246;
  const bool isF = wg < 144;
  const int f = xl * 18 + xi;
  const int wpair = xl + 8 * ((xi - 18) >> 1);
  const bool isW = wg >= 144 && wg < 192 && wpair < 18;
  const int wsig = wpair / 9, wng = 2 * (wpair % 9) + ((xi - 18) & 1);
  const int ahead = wg >> 1, aslot = wg;
  const int ab = ahead >> 4, akv = ahead & 15;
  const int qi = wg - 54, qcg = qi / 3, qkq = qi % 3;
  const int g = lane >> 4, q4 = (lane & 15) >> 2, p4 = lane & 3;

  if (wave < 8) {
    v2u E7 = {0u, 0u};
    if (wave == 7) {
      asm volatile("buffer_load_dwordx2 %0, %1, %2, 0 offen sc1" : "=&v"(E7) : "v"(WS_EPOCH), "s"(rws) : "memory");
      const v4u ss = {SENT, SENT, SENT, SENT};
      if (isM) stsc1(ss, rws, WS_MOD + ((lane >> 3) * NMOD + wg * 64) * 2 + (lane & 7) * 16);
      if (isQ) {
#pragma unroll
        for (int j = 0; j < 2; ++j) stsc1(ss, rws, WS_QP + (((qkq * 8 + 4 * j + (lane >> 4)) * NQ) + qcg * 64) * 4 + (lane & 15) * 16);
      }
      if (lane <= 32) stsc1(ss, rws, lane < 32 ? WS_OP + aslot * 512 + lane * 16 : WS_ML + aslot * 16);
      if (isW) stsc1(ss, rws, WS_YP + ((wsig * 8 + (lane >> 3)) * DM + wng * 32) * 4 + (lane & 7) * 16);
    }
    v4u cvm = {0u, 0u, 0u, 0u};
    if (isMc) cvm = ld_hidden(mkr(p.c, NB * DM * 2), tid * 16);
    if (isMc || isQ) {
      const int stride = isM ? NMOD * 2 : NQ * 2, n = isM ? 9 : 3;
      const rsrc_t rw = isM ? mkr(p.wm, DM * NMOD * 2) : mkr(p.wq, DM * NQ * 2);
      const int cb = isM ? wg * 128 : qcg * 128, r0 = isM ? 0 : qkq * 192;
      const int c16 = lane & 7;
#pragma unroll 1
      for (int j = 0; j < n; ++j) {
        const int i = wave * n + j;
        const int fz = ((lane >> 4) & 1) | ((i & 1) << 1);
        dma16(rw, (lane >> 3) * stride + ((((c16 >> 1) ^ fz) << 1) | (c16 & 1)) * 16 + cb, (r0 + i * 8) * stride, ldsb + L_W + i * 1024);
      }
    }
    auto say_cleared = [&]() {
      if (wave == 7) {
        const unsigned e0 = __builtin_amdgcn_readfirstlane(E7.x), e1 = __builtin_amdgcn_readfirstlane(E7.y);
        if (lane == 0) { __hip_atomic_store((GAS u64*)(p.ws + WS_CLR) + wg, mix64((((u64)e1 << 32) | e0) + 1), __ATOMIC_RELAXED, __HIP_MEMORY_SCOPE_AGENT); stw(L, C_E0, e0); stw(L, C_E1, e1); }
        LGWAIT();
      }
    };
    if (isMc) {
      RAWBAR();
      asm volatile("s_waitcnt vmcnt(9)" : "+v"(cvm), "+v"(E7)::"memory");
      __builtin_amdgcn_sched_barrier(0);
      say_cleared();
      v4u o;
#pragma unroll
      for (int e = 0; e < 4; ++e) o[e] = pk(silu_acc(bflo(cvm[e])), silu_acc(bfhi(cvm[e])));
      *(LAS v4u*)(L + L_SIMG + (tid / 72) * XSTR + (tid % 72) * 16) = o;
      asm volatile("s_waitcnt vmcnt(0) lgkmcnt(0)" ::: "memory");
      lds_add(L, C_DMA0);
    }
    const unsigned char* kb = (const unsigned char*)p.K + ((size_t)wg << 20);
    const unsigned char* vb = (const unsigned char*)p.V + ((size_t)wg << 20);
    const int koff = ((lane >> 2) & 3) * 256 + (g * 4 + (lane & 3)) * 16, voff = lane * 16;
    const int wb = wave * 4096;
    constexpr int ntw = 32;
    const int radd0 = isW ? K_RA_W : (isMc ? K_RA_MC : 0);
    int radd = radd0, step = 0;
    auto pace = [&]() {
      if (K_WFB) {
        const int pg = (int)lds_add(L, C_PRG) - 8 * step;
        radd = radd0 + (pg > K_WFB_B2 ? 2 : (pg > K_WFB_B ? 1 : (pg < K_WFB_A2 ? -2 : (pg < K_WFB_A ? -1 : 0))));
        ++step;
      }
    };
#define LOADK(j, u) __builtin_amdgcn_raw_buffer_load_b128(mkr(kb, (j) < ntw ? (1u << 20) : 0u), koff, (j) * 32768 + wb + (u) * 1024, AUX_NT)
#define LOADV(j, u) __builtin_amdgcn_raw_buffer_load_b128(mkr(vb, (j) < ntw ? (1u << 20) : 0u), voff, (j) * 32768 + wb + (u) * 1024, AUX_NT)
#define SB() __builtin_amdgcn_sched_barrier(0)
#define PIN(x) do { asm volatile("" : "+v"(x)); SB(); } while (0)
    constexpr int NPK = 2 * (NP + 1);
    v4u ring[8], pre[P];
#pragma unroll
    for (int n = 0; n < 8 + P; ++n) {
      if (n == K_PF && !isMc) { RAWBAR(); asm volatile("s_waitcnt vmcnt(%1)" : "+v"(E7) : "n"(K_PF) : "memory"); __builtin_amdgcn_sched_barrier(0); say_cleared(); lds_add(L, C_DMA0); }
      if (n >= K_PF) throttle(K_PF);
      if (n < 8) ring[n] = LOADK(n >> 2, n & 3); else pre[n - 8] = LOADK(n >> 2, n & 3);
      SB();
    }
    const int nl = isMc ? 1 : K_NL;
    const int nlk = 2 * nl;
    const int kvl = (isMc ? L_KVM : L_KV) + wave * (nl * 8192);
    {
      const rsrc_t rk = mkr(kb, 1 << 20);
#pragma unroll 1
      for (int n = 0; n < 4 * nlk; ++n) {
        throttle(K_PF);
        dma16(rk, koff, (NPK + (n >> 2)) * 32768 + wb + (n & 3) * 1024, ldsb + kvl + n * 1024);
      }
    }
    unsigned sel[4];
    {
      const int i = lane & 15, beta = i >> 2, pos = i & 1;
      const unsigned b0 = 2 * ((i >> 1) & 1), b1 = b0 + 1;
#pragma unroll
      for (int w = 0; w < 4; ++w) sel[w] = beta != w ? 0x0c0c0c0cu : (pos ? ((b1 << 24) | (b0 << 16) | 0x0c0cu) : (0x0c0c0000u | (b1 << 8) | b0));
    }
    RAWBAR();
    v4u qa0, qa1;
    {
      const int i = lane & 15;
      const v4u qf = lds128(L, L_Q + ((i >> 2) & 1) * 256 + (4 * g + (i & 3)) * 16);
      const v4u z = {0u, 0u, 0u, 0u};
      qa0 = (i >> 3) ? z : qf;
      qa1 = (i >> 3) ? qf : z;
    }
    const f4 zero = {0.f, 0.f, 0.f, 0.f};
    const int tn = NPK + nlk;
    const int scw = L_SC + wave * SCW;
    const int sca = scw + ((lane >> 2) & 3) * 16 + (2 * (lane & 1) + (lane >> 5)) * 4 + ((lane >> 4) & 1) * 2;
    const int scs = 16 * (lane & 1);
    float mx = -INFINITY;
    unsigned sv[NPK + 2 * K_NL];
    {
      f4 D0 = mfma(qa0, ring[0], zero); PIN(D0); ring[0] = LOADK(tn, 0); SB();
      D0 = mfma(qa1, ring[1], D0);      PIN(D0); ring[1] = LOADK(tn, 1); SB();
      f4 D1 = mfma(qa0, ring[2], zero); PIN(D1); throttle(3); ring[2] = LOADK(tn, 2); SB();
      D1 = mfma(qa1, ring[3], D1);      PIN(D1); throttle(3); ring[3] = LOADK(tn, 3); SB();
      sv[0] = score_pair(D0, D1);
      D0 = mfma(qa0, ring[4], zero); D0 = mfma(qa1, ring[5], D0);
      D1 = mfma(qa0, ring[6], zero); D1 = mfma(qa1, ring[7], D1);
      sv[1] = score_pair(D0, D1);
      PIN(sv[1]);
      static_assert(NPK == 6 && K_NL == 2, "the interleave below is written for four register tiles and four parked tiles");
#define WAITING(t) do { \
        if ((t) < NPK) { \
          D0 = mfma(qa0, pre[(4 * (t) - 8) & (P - 1)], zero); D0 = mfma(qa1, pre[(4 * (t) - 7) & (P - 1)], D0); \
          D1 = mfma(qa0, pre[(4 * (t) - 6) & (P - 1)], zero); D1 = mfma(qa1, pre[(4 * (t) - 5) & (P - 1)], D1); \
          sv[t] = score_pair(D0, D1); \
        } else { \
          sv[t] = 0xff80ff80u;        \
          if ((t) - NPK < nlk) { \
            const int a = kvl + ((t) - NPK) * 4096 + lane * 16; \
            D0 = mfma(qa0, lds128(L, a), zero); D0 = mfma(qa1, lds128(L, a + 1024), D0); \
            D1 = mfma(qa0, lds128(L, a + 2048), zero); D1 = mfma(qa1, lds128(L, a + 3072), D1); \
            sv[t] = score_pair(D0, D1); \
          } \
        } \
        PIN(sv[t]); } while (0)
      throttle(3); ring[4] = LOADK(tn + 1, 0); SB(); WAITING(2); WAITING(3);
      throttle(3); ring[5] = LOADK(tn + 1, 1); SB(); WAITING(4); WAITING(5);
      throttle(3); ring[6] = LOADK(tn + 1, 2); SB();
      VMWAIT(7);
      WAITING(6); WAITING(7);
      throttle(3); ring[7] = LOADK(tn + 1, 3); SB(); WAITING(8); WAITING(9);
#undef WAITING
      LGWAIT();
      lds_add(L, C_KVL);
      lds_wait(L, C_KVL, 8);
#pragma unroll
      for (int t = 0; t < NPK + 2 * K_NL; ++t) {
        if (t < tn) {
          mx = fmaxf(mx, fmaxf(bflo(sv[t]), bfhi(sv[t])));
          *(LAS u16*)(L + sca + t * 64) = (u16)(sv[t] >> scs);
        }
      }
    }
#pragma unroll 1
    for (int j = tn; j < ntw; j += 2) {
      if (j >= K_TRIG_WO && j < K_TRIG_WO + 2 && lane == 0 && wave == 0) stw(L, C_TRIG, 1u);
      pace();
      const bool more = j + 2 < ntw;
      const unsigned char* nb = more ? kb : vb;
      const int nj = more ? j + 2 : 0, no = more ? koff : voff;
#define LOADN(jj, u) __builtin_amdgcn_raw_buffer_load_b128(mkr(nb, 1u << 20), no, (jj) * 32768 + wb + (u) * 1024, AUX_NT)
      f4 D0 = mfma(qa0, ring[0], zero); PIN(D0); throttle(rdigit(K_RPAT, 0) + (radd > 0 ? 1u : 0u)); ring[0] = LOADN(nj, 0); SB();
      D0 = mfma(qa1, ring[1], D0);      PIN(D0); throttle(rdigit(K_RPAT, 1)); ring[1] = LOADN(nj, 1); SB();
      f4 D1 = mfma(qa0, ring[2], zero); PIN(D1); throttle(rdigit(K_RPAT, 2) + (radd > 2 ? 1u : 0u)); ring[2] = LOADN(nj, 2); SB();
      D1 = mfma(qa1, ring[3], D1);      PIN(D1); throttle(rdigit(K_RPAT, 3) - (radd < -1 ? 1u : 0u)); ring[3] = LOADN(nj, 3); SB();
      const unsigned s0 = score_pair(D0, D1);
      mx = fmaxf(mx, fmaxf(bflo(s0), bfhi(s0)));
      *(LAS u16*)(L + sca + j * 64) = (u16)(s0 >> scs);
      D0 = mfma(qa0, ring[4], zero); PIN(D0); throttle(rdigit(K_RPAT, 4) + (radd > 1 ? 1u : 0u)); ring[4] = LOADN(nj + 1, 0); SB();
      D0 = mfma(qa1, ring[5], D0);   PIN(D0); throttle(rdigit(K_RPAT, 5)); ring[5] = LOADN(nj + 1, 1); SB();
      D1 = mfma(qa0, ring[6], zero); PIN(D1); throttle(rdigit(K_RPAT, 6) + (radd > 3 ? 1u : 0u)); ring[6] = LOADN(nj + 1, 2); SB();
      D1 = mfma(qa1, ring[7], D1);   PIN(D1); throttle(rdigit(K_RPAT, 7) - (radd < 0 ? 1u : 0u)); ring[7] = LOADN(nj + 1, 3); SB();
#undef LOADN
      const unsigned s1 = score_pair(D0, D1);
      mx = fmaxf(mx, fmaxf(bflo(s1), bfhi(s1)));
      *(LAS u16*)(L + sca + (j + 1) * 64) = (u16)(s1 >> scs);
    }
    mx = fmaxf(mx, dpp<ROR4>(mx)); mx = fmaxf(mx, dpp<ROR8>(mx)); mx = x32max(mx);
    float m0h, m1h, l0 = 0.f, l1 = 0.f;
    {
      auto t = __builtin_amdgcn_permlane16_swap(asu(mx), asu(mx), false, false);
      m0h = asf(t[0]); m1h = asf(t[1]);
      LGWAIT();
#pragma unroll 3
      for (int i = lane; i < ntw * 16; i += 64) {
        const unsigned s = *(LAS unsigned*)(L + scw + i * 4);
        const float p0 = __builtin_amdgcn_exp2f((bflo(s) - m0h) * LOG2E), p1 = __builtin_amdgcn_exp2f((bfhi(s) - m1h) * LOG2E);
        l0 += p0; l1 += p1;
        *(LAS unsigned*)(L + scw + i * 4) = pk(p0, p1);
      }
      l0 = x32sum(x16sum(row16sum(l0))); l1 = x32sum(x16sum(row16sum(l1)));
      if (lane == 0) *(LAS f4*)(L + L_WML + wave * 16) = f4{m0h, l0, m1h, l1};
      LGWAIT();
      lds_add(L, C_WML);
    }
    float M0 = -INFINITY, M1 = -INFINITY, L0 = 0.f, L1 = 0.f, f0 = 0.f, f1 = 0.f;
    f4 acc = zero;
#pragma unroll 1
    for (int j = 0; j < ntw; j += 2) {
      const bool tail = j + K_TTAIL / 4 >= ntw;
      pace();
      if (j == ntw - 4) {
        lds_wait(L, C_WML, 8);
        f4 wm[8];
#pragma unroll
        for (int w = 0; w < 8; ++w) { wm[w] = *(LAS f4*)(L + L_WML + w * 16); M0 = fmaxf(M0, wm[w].x); M1 = fmaxf(M1, wm[w].z); }
#pragma unroll
        for (int w = 0; w < 8; ++w) { L0 += __builtin_amdgcn_exp2f((wm[w].x - M0) * LOG2E) * wm[w].y; L1 += __builtin_amdgcn_exp2f((wm[w].z - M1) * LOG2E) * wm[w].w; }
        f0 = __builtin_amdgcn_exp2f((m0h - M0) * LOG2E) * __builtin_amdgcn_rcpf(L0); f1 = __builtin_amdgcn_exp2f((m1h - M1) * LOG2E) * __builtin_amdgcn_rcpf(L1);
      }
      const v4u pa = lds128(L, scw + j * 64 + g * 16), pb = lds128(L, scw + j * 64 + 64 + g * 16);
      acc = mfma(pfrag(pa.x, sel), ring[0], acc); PIN(acc); throttle(tail ? K_RTAIL : rdigit(K_RPAT, 0) + (radd > 0 ? 1u : 0u)); ring[0] = LOADV(j + 2, 0); SB();
      acc = mfma(pfrag(pa.y, sel), ring[1], acc); PIN(acc); throttle(tail ? K_RTAIL : rdigit(K_RPAT, 1)); ring[1] = LOADV(j + 2, 1); SB();
      acc = mfma(pfrag(pa.z, sel), ring[2], acc); PIN(acc); throttle(tail ? K_RTAIL : rdigit(K_RPAT, 2) + (radd > 2 ? 1u : 0u)); ring[2] = LOADV(j + 2, 2); SB();
      acc = mfma(pfrag(pa.w, sel), ring[3], acc); PIN(acc); throttle(tail ? K_RTAIL : rdigit(K_RPAT, 3) - (radd < -1 ? 1u : 0u)); ring[3] = LOADV(j + 2, 3); SB();
      acc = mfma(pfrag(pb.x, sel), ring[4], acc); PIN(acc); throttle(tail ? K_RTAIL : rdigit(K_RPAT, 4) + (radd > 1 ? 1u : 0u)); ring[4] = LOADV(j + 3, 0); SB();
      acc = mfma(pfrag(pb.y, sel), ring[5], acc); PIN(acc); throttle(tail ? K_RTAIL : rdigit(K_RPAT, 5)); ring[5] = LOADV(j + 3, 1); SB();
      acc = mfma(pfrag(pb.z, sel), ring[6], acc); PIN(acc); throttle(tail ? K_RTAIL : rdigit(K_RPAT, 6) + (radd > 3 ? 1u : 0u)); ring[6] = LOADV(j + 3, 2); SB();
      acc = mfma(pfrag(pb.w, sel), ring[7], acc); PIN(acc); throttle(tail ? K_RTAIL : rdigit(K_RPAT, 7) - (radd < 0 ? 1u : 0u)); ring[7] = LOADV(j + 3, 3); SB();
    }
#undef LOADK
#undef LOADV
    if (lds_add(L, C_END) == 7u && lane == 0) stw(L, C_TRIG, 2u);
    LAS float* mg = (LAS float*)(L + scw);
    const int cc = lane & 15;
    *(LAS f2*)(mg + 8 * cc + 2 * g) = f2{acc.x * f0, acc.y * f0};
    *(LAS f2*)(mg + 128 + 8 * cc + 2 * g) = f2{acc.z * f1, acc.w * f1};
    LGWAIT();
    if (lds_add(L, C_MRG) == 7u) {
      MEMBAR();
      const int h = lane >> 5, d0 = 4 * (lane & 31);
      f4 O = {0.f, 0.f, 0.f, 0.f};
#pragma unroll
      for (int w = 0; w < 8; ++w) O += *(LAS f4*)(L + L_SC + w * SCW + (128 * h + d0) * 4);
      const unsigned o0 = pk(O.x, O.y), o1 = pk(O.z, O.w);
      const unsigned n0 = dppu<QX1>(o0), n1 = dppu<QX1>(o1);
      if (!(lane & 1)) stsc1(san4(v4u{o0, o1, n0, n1}), rws, WS_OP + aslot * 512 + (h * 128 + d0) * 2);
      if (lane == 0) stsc1(san4(v4u{asu(M0), asu(L0), asu(M1), asu(L1)}), rws, WS_ML + aslot * 16);
    }
  } else {
    RAWBAR();
    const int sw = wave - 8, stid = tid - 512;
    const u64 Es = __hip_atomic_load((GAS u64*)(p.ws + WS_EPOCH), __ATOMIC_RELAXED, __HIP_MEMORY_SCOPE_AGENT);
    const int fz = (q4 >> 1) | ((g & 1) << 1);
    auto mod_mfma = [&]() {
      f4 D[3] = {{0.f, 0.f, 0.f, 0.f}, {0.f, 0.f, 0.f, 0.f}, {0.f, 0.f, 0.f, 0.f}};
#pragma unroll
      for (int ks = 0; ks < 18; ++ks) {
        const v4u wf = trfrag(L, L_W + (32 * ks + 8 * g + q4) * 128 + (sw ^ fz) * 32 + p4 * 8, 4 * 128);
        const v4u sf = lds128(L, L_SIMG + (lane & 7) * XSTR + (32 * ks + 8 * g) * 2);
        D[ks % 3] = mfma(wf, sf, D[ks % 3]);
      }
      const f4 Ds = (D[0] + D[1]) + D[2];
      if ((lane & 15) < 8) stsc1_2(v2u{san(pk(Ds.x, Ds.y)), san(pk(Ds.z, Ds.w))}, rws, WS_MOD + ((lane & 7) * NMOD + wg * 64 + sw * 16 + 4 * g) * 2);
    };
    const u64 tagv = mix64((((u64)__builtin_amdgcn_readfirstlane((unsigned)(Es >> 32)) << 32) | (unsigned)__builtin_amdgcn_readfirstlane((unsigned)Es)) + 1);
    const unsigned tg0 = (unsigned)tagv, tg1 = (unsigned)(tagv >> 32);
    if (isMc) {
      if (sw == 0) {
        const v4u cv = __builtin_amdgcn_raw_buffer_load_b128(mkr(p.c, NB * DM * 2), tid * 16, 0, 0);
        v4u o;
#pragma unroll
        for (int e = 0; e < 4; ++e) o[e] = pk(silu_acc(bflo(cv[e])), silu_acc(bfhi(cv[e])));
        *(LAS v4u*)(L + L_SIMG + (tid / 72) * XSTR + (tid % 72) * 16) = o;
        LGWAIT();
        lds_add(L, C_SYNC1);
      }
      lds_wait(L, C_SYNC1, 1); lds_wait(L, C_DMA0, 8);
      mod_mfma();
    } else if (isQ) {
      const int qb = 2 * sw + (lane >> 5), qj = lane & 31;
      const bool qact = qj < 24;
      const rsrc_t rx = mkr(p.x, NB * DM * 2);
      v4u xv[3];
#pragma unroll
      for (int t = 0; t < 3; ++t) xv[t] = __builtin_amdgcn_raw_buffer_load_b128(rx, qact ? (qb * DM + 192 * t + 8 * qj) * 2 : 0x7ffffff0, 0, 0);
      float xf[3][8];
      float sum = 0.f;
#pragma unroll
      for (int t = 0; t < 3; ++t)
#pragma unroll
        for (int e = 0; e < 4; ++e) { xf[t][2 * e] = bflo(xv[t][e]); xf[t][2 * e + 1] = bfhi(xv[t][e]); sum += xf[t][2 * e] + xf[t][2 * e + 1]; }
      sum = x16sum(row16sum(sum));
      const float mean = sum * (1.f / DM);
      float vs = 0.f;
#pragma unroll
      for (int t = 0; t < 3; ++t)
#pragma unroll
        for (int e = 0; e < 8; ++e) { const float d = xf[t][e] - mean; vs += qact ? d * d : 0.f; }
      vs = x16sum(row16sum(vs));
      const float mb = rb(mean), den = rb(__builtin_sqrtf(rb(rb(vs * (1.f / DM)) + 1e-5f)));
      float ln[8];
#pragma unroll
      for (int e = 0; e < 8; ++e) { const float xe = qkq == 0 ? xf[0][e] : (qkq == 1 ? xf[1][e] : xf[2][e]); ln[e] = rb(rb(xe - mb) / den); }
      lds_wait(L, C_DMA0, 8);
      v4u wf[6];
#pragma unroll
      for (int ks = 0; ks < 6; ++ks) wf[ks] = trfrag(L, L_W + (32 * ks + 8 * g + q4) * 128 + (sw ^ fz) * 32 + p4 * 8, 4 * 128);
      v4u sa, ca;
      const int mo = qact ? WS_MOD + (qb * NMOD + 192 * qkq + 8 * qj) * 2 : 0x7ffffff0;
      if (sw == 0) {
        const int pw = (lane < 3 ? 0 : 6) + 3 * qkq + lane;
        const int fo = lane < 6 ? WS_CLR + pw * 8 : 0x7ffffff0, io = lane < 6 ? WS_MOD + (7 * NMOD + pw * 64 + 62) * 2 : 0x7ffffff0;
        bool cleared = false;
        for (unsigned spins = 0;;) {
          MEMBAR();
          if (!cleared) { const v2u fl = ldsc1_2(rws, fo); cleared = __all(lane >= 6 || (fl.x == tg0 && fl.y == tg1)); }
          else if (__all(__builtin_amdgcn_raw_buffer_load_b32(rws, io, 0, AUX_SC1) != SENT)) break;
          if (give_up(spins, L)) break;
          __builtin_amdgcn_s_sleep(1);
        }
        if (lane == 0) stw(L, C_HS, 1u);
      } else lds_wait(L, C_HS, 1);
      for (unsigned spins = 0;;) {
        MEMBAR();
        sa = ldsc1(rws, mo); ca = ldsc1(rws, qact ? mo + DM * 2 : mo);
        if (__all(umax(max4(sa), max4(ca)) != SENT)) break;
        if (give_up(spins, L)) break;
        __builtin_amdgcn_s_sleep(1);
      }
      v4u h;
#pragma unroll
      for (int e = 0; e < 4; ++e) {
        const unsigned c1 = pk(1.f + bflo(ca[e]), 1.f + bfhi(ca[e]));
        const unsigned m1 = pk(ln[2 * e] * bflo(c1), ln[2 * e + 1] * bfhi(c1));
        h[e] = pk(bflo(m1) + bflo(sa[e]), bfhi(m1) + bfhi(sa[e]));
      }
      if (qact) *(LAS v4u*)(L + L_HIMG + qb * HSTR + qj * 16) = h;
      LGWAIT();
      lds_add(L, C_SYNC1); lds_wait(L, C_SYNC1, 4);
      f4 D0 = {0.f, 0.f, 0.f, 0.f}, D1 = {0.f, 0.f, 0.f, 0.f};
#pragma unroll
      for (int ks = 0; ks < 6; ks += 2) {
        D0 = mfma(wf[ks], lds128(L, L_HIMG + (lane & 7) * HSTR + (32 * ks + 8 * g) * 2), D0);
        D1 = mfma(wf[ks + 1], lds128(L, L_HIMG + (lane & 7) * HSTR + (32 * ks + 32 + 8 * g) * 2), D1);
      }
      if ((lane & 15) < 8) stsc1(san4(__builtin_bit_cast(v4u, D0 + D1)), rws, WS_QP + ((qkq * 8 + (lane & 7)) * NQ + qcg * 64 + sw * 16 + 4 * g) * 4);
    }
    if (sw == 0) {
      v4u a0, a1, a2;
      const int qo = WS_QP + (ab * NQ + akv * 256 + 4 * lane) * 4;
      const int fo = lane < 12 ? WS_CLR + (54 + 12 * akv + lane) * 8 : 0x7ffffff0;
      const int io = lane < 12 ? WS_QP + (((lane % 3) * 8 + ab) * NQ + (4 * akv + lane / 3) * 64 + 63) * 4 : 0x7ffffff0;
      bool cleared = false;
      for (unsigned spins = 0;;) {
        MEMBAR();
        if (!cleared) { const v2u fl = ldsc1_2(rws, fo); cleared = __all(lane >= 12 || (fl.x == tg0 && fl.y == tg1)); }
        else if (__all(__builtin_amdgcn_raw_buffer_load_b32(rws, io, 0, AUX_SC1) != SENT)) break;
        if (give_up(spins, L)) break;
        __builtin_amdgcn_s_sleep(1);
      }
      for (unsigned spins = 0;;) {
        MEMBAR();
        a0 = ldsc1(rws, qo); a1 = ldsc1(rws, qo + NB * NQ * 4); a2 = ldsc1(rws, qo + 2 * NB * NQ * 4);
        if (__all(umax(umax(max4(a0), max4(a1)), max4(a2)) != SENT)) break;
        if (give_up(spins, L)) break;
        __builtin_amdgcn_s_sleep(1);
      }
      const f4 s = (__builtin_bit_cast(f4, a0) + __builtin_bit_cast(f4, a1)) + __builtin_bit_cast(f4, a2);
      *(LAS v2u*)(L + L_Q + lane * 8) = v2u{pk(s.x, s.y), pk(s.z, s.w)};
    }
    RAWBAR();
    if (isF && sw == 3) {
      const v4u ss = {SENT, SENT, SENT, SENT};
#pragma unroll 3
      for (int j = 0; j < 9; ++j) stsc1(ss, rws, WS_DP + ((j * 8 + (lane >> 3)) * 144 + f) * 128 + (lane & 7) * 16);
      VMWAIT(0);
      if (lane == 0) __hip_atomic_store((GAS u64*)(p.ws + WS_CLR2) + f, tagv, __ATOMIC_RELAXED, __HIP_MEMORY_SCOPE_AGENT);
    }
    if (isM && !isMc) {
      lds_wait(L, C_DMA0, 8); lds_wait(L, C_KVL, 8);
      const rsrc_t rw = mkr(p.wm, DM * NMOD * 2);
      const int c16 = lane & 7;
#pragma unroll 1
      for (int j = 0; j < 18; ++j) {
        const int i = sw * 18 + j;
        const int fs = ((lane >> 4) & 1) | ((i & 1) << 1);
        dma16(rw, (lane >> 3) * (NMOD * 2) + ((((c16 >> 1) ^ fs) << 1) | (c16 & 1)) * 16 + wg * 128, i * 8 * (NMOD * 2), ldsb + L_W + i * 1024);
        __builtin_amdgcn_s_sleep(K_WOPACE);
      }
      const rsrc_t rc = mkr(p.c, NB * DM * 2);
#pragma unroll 1
      for (int j = 0; j < 3; ++j) {
        const int ch = stid + 256 * j;
        const v4u cv = __builtin_amdgcn_raw_buffer_load_b128(rc, ch * 16, 0, 0);
        v4u o;
#pragma unroll
        for (int e = 0; e < 4; ++e) o[e] = pk(silu_acc(bflo(cv[e])), silu_acc(bfhi(cv[e])));
        if (ch < 576) *(LAS v4u*)(L + L_SIMG + (ch / 72) * XSTR + (ch % 72) * 16) = o;
      }
      VMWAIT(0); LGWAIT();
      lds_add(L, C_SYNC1); lds_wait(L, C_SYNC1, 4);
      mod_mfma();
    }
    if ((isF || isW) && sw == 3) {
      for (unsigned spins = 0;;) {
        MEMBAR();
        const v4u a = ldsc1(rws, WS_CLR + lane * 32), b = ldsc1(rws, WS_CLR + lane * 32 + 16);
        bool ok = a.x == tg0 && a.y == tg1 && a.z == tg0 && a.w == tg1 && b.x == tg0 && b.y == tg1 && b.z == tg0 && b.w == tg1;
        if (isF) {
          const v4u c2 = ldsc1(rws, lane < 36 ? WS_CLR2 + lane * 32 : 0x7ffffff0), d2 = ldsc1(rws, lane < 36 ? WS_CLR2 + lane * 32 + 16 : 0x7ffffff0);
          ok = ok && (lane >= 36 || (c2.x == tg0 && c2.y == tg1 && c2.z == tg0 && c2.w == tg1 && d2.x == tg0 && d2.y == tg1 && d2.z == tg0 && d2.w == tg1));
        }
        if (__all(ok)) break;
        if (give_up(spins, L)) break;
        __builtin_amdgcn_s_sleep(127);
      }
      if (lane == 0) stw(L, C_HS2, 1u);
    }
    if (isW || isF) {
      if (isM && !isMc) { lds_add(L, C_MDONE); lds_wait(L, C_MDONE, 4); }
      lds_wait(L, C_KVL, 8);
      const int sc = ((lane & 3) ^ ((lane >> 4) & 2)) * 16;
      if (isW) {
        const rsrc_t rw = mkr(p.wo, NQ * DM * 2);
        const int vo = (lane >> 2) * (DM * 2) + sc + wng * 64;
        lds_wait<16>(L, C_TRIG, 1);
#pragma unroll 1
        for (int j = 0; j < 32; ++j) { const int i = j * 4 + sw; dma16(rw, vo, (wsig * 2048 + i * 16) * DM * 2, ldsb + L_W + i * 1024); __builtin_amdgcn_s_sleep(K_WOPACE); }
        VMWAIT(0);
        lds_add(L, C_DMA1);
      } else {
        const rsrc_t rg = mkr(p.wg, DM * DFF * 2), ru = mkr(p.wu, DM * DFF * 2), rd = mkr(p.wd, DFF * DM * 2);
        const int vo = (lane >> 2) * (DFF * 2) + sc + f * 64;
        lds_wait<4>(L, C_TRIG, 2);
#pragma unroll 1
        for (int j = 0; j < 9; ++j) {
          const int i = j * 4 + sw;
          throttle(K_DMAR); dma16(rg, vo, i * 16 * DFF * 2, ldsb + L_W + i * 1024);
          throttle(K_DMAR); dma16(ru, vo, i * 16 * DFF * 2, ldsb + L_WU + i * 1024);
        }
#pragma unroll 1
        for (int j = 0; j < 9; ++j) {
          throttle(K_DMAR);
          if (j == K_DMAR - 1) lds_add(L, C_DMA1);
          const int i = j * 4 + sw; dma16(rd, lane * 16, f * 32 * DM * 2 + i * 1024, ldsb + L_WD + i * 1024);
        }
        static_assert(K_DMAR >= 1 && K_DMAR <= 6, "the throttle counts up to six");
        VMWAIT(0);
        lds_add(L, C_DMA2);
      }
    }
  }

  if (isW) {
    const int cc = lane & 15;
    if (wave < 8) {
      const int pid = (cc & 7) * 32 + (wsig * 8 + wave) * 2 + (cc >> 3);
      lds_wait(L, C_HS2, 1);
      lds_wait(L, C_DMA1, 4);
      v4u wf[2][4][2];
#pragma unroll
      for (int hh = 0; hh < 2; ++hh)
#pragma unroll
        for (int ks = 0; ks < 4; ++ks)
#pragma unroll
          for (int nt = 0; nt < 2; ++nt) wf[hh][ks][nt] = trfrag(L, L_W + (wave * 256 + hh * 128 + 32 * ks + 8 * g + q4) * 64 + (nt ^ (g & 1)) * 32 + p4 * 8, 4 * 64);
      v4u ml, fr[2][4];
      for (unsigned spins = 0;;) {
        MEMBAR();
        ml = ldsc1(rws, WS_ML + pid * 16);
        const int nv = __builtin_popcountll(__builtin_amdgcn_ballot_w64(max4(ml) != SENT));
        if (nv >= 4 * 15) break;
        if (give_up(spins, L)) break;
        if (nv < 4 * 12) __builtin_amdgcn_s_sleep(24); else __builtin_amdgcn_s_sleep(1);
      }
      for (unsigned spins = 0;;) {
        MEMBAR();
        ml = ldsc1(rws, WS_ML + pid * 16);
#pragma unroll
        for (int hh = 0; hh < 2; ++hh)
#pragma unroll
          for (int ks = 0; ks < 4; ++ks) fr[hh][ks] = ldsc1(rws, WS_OP + pid * 512 + (hh * 128 + 32 * ks + 8 * g) * 2);
        unsigned mx = max4(ml);
#pragma unroll
        for (int hh = 0; hh < 2; ++hh)
#pragma unroll
          for (int ks = 0; ks < 4; ++ks) mx = umax(mx, max4(fr[hh][ks]));
        if (__all(mx != SENT)) break;
        if (give_up(spins, L)) break;
      }
      float wgt[2];
#pragma unroll
      for (int hh = 0; hh < 2; ++hh) {
        const float M = asf(ml[2 * hh]), Lv = asf(ml[2 * hh + 1]);
        const float M2 = dpp<ROR8>(M), L2 = dpp<ROR8>(Lv), Mx = fmaxf(M, M2);
        const float e1 = __builtin_amdgcn_exp2f((M - Mx) * LOG2E) * Lv, e2 = __builtin_amdgcn_exp2f((M2 - Mx) * LOG2E) * L2;
        wgt[hh] = e1 * __builtin_amdgcn_rcpf(e1 + e2);
      }
#pragma unroll
      for (int nt = 0; nt < 2; ++nt) {
        f4 d0 = {0.f, 0.f, 0.f, 0.f}, d1 = {0.f, 0.f, 0.f, 0.f};
#pragma unroll
        for (int ks = 0; ks < 4; ++ks) { d0 = mfma(wf[0][ks][nt], fr[0][ks], d0); d1 = mfma(wf[1][ks][nt], fr[1][ks], d1); }
        f4 t = wgt[0] * d0 + wgt[1] * d1;
        t.x += dpp<ROR8>(t.x); t.y += dpp<ROR8>(t.y); t.z += dpp<ROR8>(t.z); t.w += dpp<ROR8>(t.w);
        if (cc < 8) *(LAS f4*)(L + L_RED + (((wave * 2 + nt) * 4 + g) * 8 + cc) * 16) = t;
      }
      LGWAIT();
      if (lds_add(L, C_RED) == 7u) {
        MEMBAR();
        f4 s = {0.f, 0.f, 0.f, 0.f};
#pragma unroll
        for (int w = 0; w < 8; ++w) s += *(LAS f4*)(L + L_RED + (w * 64 + lane) * 16);
        const int nt = lane >> 5, rr = (lane >> 3) & 3, b = lane & 7;
        stsc1(san4(__builtin_bit_cast(v4u, s)), rws, WS_YP + ((wsig * 8 + b) * DM + wng * 32 + nt * 16 + 4 * rr) * 4);
      }
    }
  } else if (isF) {
    const bool isR = f < 72;
    const int rb_ = f / 9, rc0 = (f % 9) * 64;
    unsigned gbw = 0;
    const int gp = wave & 1, kt = wave >> 1;
    v4u wf[6], wd[4];
    const int dw = wave - 3;
    bool have_wd = false;
    auto read_wd = [&]() {
      lds_wait(L, C_DMA2, 4);
#pragma unroll
      for (int j = 0; j < 4; ++j) wd[j] = trfrag(L, L_WD + (8 * g + q4) * (DM * 2) + (dw >= 0 ? 4 * dw + j : 0) * 32 + p4 * 8, 4 * DM * 2);
    };
    if (wave < 8) {
      const int b = wave;
      const bool act = lane < 48;
      const rsrc_t rx = mkr(p.x, NB * DM * 2);
      v2u xv[3], ga[3], sb[3], cb[3];
      lds_wait(L, C_HS2, 1);
#pragma unroll
      for (int t = 0; t < 3; ++t) xv[t] = __builtin_amdgcn_raw_buffer_load_b64(rx, act ? (b * DM + 4 * (lane + 48 * t)) * 2 : 0x7ffffff0, 0, 0);
      for (unsigned spins = 0;;) {
        MEMBAR();
        unsigned mx = 0;
#pragma unroll
        for (int t = 0; t < 3; ++t) {
          const int o = act ? WS_MOD + (b * NMOD + 4 * (lane + 48 * t)) * 2 : 0x7ffffff0;
          ga[t] = ldsc1_2(rws, act ? o + 2 * DM * 2 : o); sb[t] = ldsc1_2(rws, act ? o + 3 * DM * 2 : o); cb[t] = ldsc1_2(rws, act ? o + 4 * DM * 2 : o);
          mx = umax(mx, umax(umax(umax(ga[t].x, ga[t].y), umax(sb[t].x, sb[t].y)), umax(cb[t].x, cb[t].y)));
        }
        if (wave == 0) { gbw = __builtin_amdgcn_raw_buffer_load_b32(rws, isR ? WS_MOD + (rb_ * NMOD + 5 * DM + rc0 + (lane & ~1)) * 2 : 0x7ffffff0, 0, AUX_SC1); mx = umax(mx, gbw); }
        if (__all(mx != SENT)) break;
        if (give_up(spins, L)) break;
      }
      float xf[12], gf[12], cf[12], sf[12];
#pragma unroll
      for (int t = 0; t < 3; ++t)
#pragma unroll
        for (int e = 0; e < 2; ++e) {
          xf[4 * t + 2 * e] = bflo(xv[t][e]); xf[4 * t + 2 * e + 1] = bfhi(xv[t][e]);
          gf[4 * t + 2 * e] = bflo(ga[t][e]); gf[4 * t + 2 * e + 1] = bfhi(ga[t][e]);
          cf[4 * t + 2 * e] = 1.f + bflo(cb[t][e]); cf[4 * t + 2 * e + 1] = 1.f + bfhi(cb[t][e]);
          sf[4 * t + 2 * e] = bflo(sb[t][e]); sf[4 * t + 2 * e + 1] = bfhi(sb[t][e]);
        }
#pragma unroll
      for (int e = 0; e < 12; ++e) asm volatile("" : "+v"(xf[e]), "+v"(gf[e]), "+v"(cf[e]), "+v"(sf[e]));
      lds_wait(L, C_DMA1, 4);
#pragma unroll
      for (int k6 = 0; k6 < 6; ++k6) wf[k6] = trfrag(L, (gp ? L_WU : L_W) + (32 * (kt * 3 + (k6 >> 1)) + 8 * g + q4) * 64 + ((k6 & 1) ^ (g & 1)) * 32 + p4 * 8, 4 * 64);
      if (wave >= 3 && __builtin_amdgcn_readfirstlane(ldsw(L, C_DMA2)) >= 4u) { read_wd(); have_wd = true; }
      if (wave == 0) {
        for (unsigned spins = 0;;) {
          MEMBAR();
          const unsigned v = __builtin_amdgcn_raw_buffer_load_b32(rws, lane == 0 ? WS_YP + (((f & 1) * 8 + (f & 7)) * DM + (f % 18) * 32) * 4 : 0x7ffffff0, 0, AUX_SC1);
          if (__builtin_amdgcn_readfirstlane(v) != SENT) break;
          if (give_up(spins, L)) break;
          __builtin_amdgcn_s_sleep(1);
        }
        if (lane == 0) stw(L, C_Y, 1u);
      } else lds_wait(L, C_Y, 1);
      v4u y0[3], y1[3];
      for (unsigned spins = 0;;) {
        MEMBAR();
        unsigned mx = 0;
#pragma unroll
        for (int t = 0; t < 3; ++t) {
          const int o = act ? WS_YP + (b * DM + 4 * (lane + 48 * t)) * 4 : 0x7ffffff0;
          y0[t] = ldsc1(rws, o); y1[t] = ldsc1(rws, act ? o + NB * DM * 4 : o);
        }
#pragma unroll
        for (int t = 0; t < 3; ++t) mx = umax(mx, umax(max4(y0[t]), max4(y1[t])));
        if (__all(mx != SENT)) break;
        if (give_up(spins, L)) break;
      }
      float x1[12];
#pragma unroll
      for (int e = 0; e < 12; ++e) x1[e] = __builtin_fmaf(gf[e], asf(y0[e >> 2][e & 3]) + asf(y1[e >> 2][e & 3]), xf[e]);
      const float sh = asf(__builtin_amdgcn_readfirstlane(asu(x1[0])));
      float s1 = 0.f, s2 = 0.f;
#pragma unroll
      for (int e = 0; e < 12; ++e) { const float d = x1[e] - sh; s1 += d; s2 = __builtin_fmaf(d, d, s2); }
      s1 = act ? s1 : 0.f; s2 = act ? s2 : 0.f;
      s1 += dpp<ROR1>(s1); s2 += dpp<ROR1>(s2); s1 += dpp<ROR2>(s1); s2 += dpp<ROR2>(s2);
      s1 += dpp<ROR4>(s1); s2 += dpp<ROR4>(s2); s1 += dpp<ROR8>(s1); s2 += dpp<ROR8>(s2);
      s1 = x16sum(s1); s2 = x16sum(s2); s1 = x32sum(s1); s2 = x32sum(s2);
      const float dm = s1 * (1.f / DM), var = fmaxf(__builtin_fmaf(-dm, dm, s2 * (1.f / DM)), 0.f);
      const float rstd = __builtin_amdgcn_rsqf(var + 1e-5f), mo = -(sh + dm) * rstd;
#pragma unroll
      for (int t = 0; t < 3; ++t) {
        float hv[4];
#pragma unroll
        for (int e = 0; e < 4; ++e) hv[e] = __builtin_fmaf(__builtin_fmaf(x1[4 * t + e], rstd, mo), cf[4 * t + e], sf[4 * t + e]);
        if (act) {
          *(LAS v2u*)(L + L_H2 + b * XSTR + 8 * (lane + 48 * t)) = v2u{pk(hv[0], hv[1]), pk(hv[2], hv[3])};
          const int col = 4 * (lane + 48 * t) - rc0;
          if (isR && b == rb_ && col >= 0 && col < 64) *(LAS f4*)(L + L_X1S + col * 4) = f4{x1[4 * t], x1[4 * t + 1], x1[4 * t + 2], x1[4 * t + 3]};
        }
      }
    } else {
      lds_wait(L, C_DMA1, 4);
#pragma unroll
      for (int k6 = 0; k6 < 6; ++k6) wf[k6] = trfrag(L, (gp ? L_WU : L_W) + (32 * (kt * 3 + (k6 >> 1)) + 8 * g + q4) * 64 + ((k6 & 1) ^ (g & 1)) * 32 + p4 * 8, 4 * 64);
      lds_wait(L, C_Y, 1);
      if (__builtin_amdgcn_readfirstlane(ldsw(L, C_DMA2)) >= 4u) { read_wd(); have_wd = true; }
    }
    RAWBAR();
    {
      f4 D0 = {0.f, 0.f, 0.f, 0.f}, D1 = {0.f, 0.f, 0.f, 0.f};
#pragma unroll
      for (int k3 = 0; k3 < 3; ++k3) {
        const v4u hf = lds128(L, L_H2 + (lane & 7) * XSTR + (32 * (kt * 3 + k3) + 8 * g) * 2);
        D0 = mfma(wf[2 * k3], hf, D0); D1 = mfma(wf[2 * k3 + 1], hf, D1);
      }
      *(LAS f4*)(L + L_GU + (((kt * 4 + gp * 2 + ((lane >> 3) & 1)) * 4 + g) * 8 + (lane & 7)) * 16) = (lane & 8) ? D1 : D0;
    }
    RAWBAR();
    if (!have_wd && wave >= 4) read_wd();
    if (tid < 256) {
      const int b = tid & 7, fc = tid >> 3;
      const int o = (((fc >> 4) * 4 + ((fc >> 2) & 3)) * 8 + b) * 16 + (fc & 3) * 4;
      float gs = 0.f, us = 0.f;
#pragma unroll
      for (int k3 = 0; k3 < 6; ++k3) { gs += *(LAS float*)(L + L_GU + k3 * 2048 + o); us += *(LAS float*)(L + L_GU + k3 * 2048 + 1024 + o); }
      const float a = rb(gelu_t(rb(gs))) * rb(us);
      *(LAS u16*)(L + L_ACT + b * ASTR + fc * 2) = (u16)pk(a, a);
      if (!have_wd && wave == 3) read_wd();
    }
    RAWBAR();
    if (wave >= 3) {
      const v4u af = lds128(L, L_ACT + (lane & 7) * ASTR + g * 16);
      const int b = lane & 7, hi = (lane >> 3) & 1;
      const f4 z4 = {0.f, 0.f, 0.f, 0.f};
      const f4 T0 = mfma(wd[0], af, z4), T1 = mfma(wd[1], af, z4), T2 = mfma(wd[2], af, z4), T3 = mfma(wd[3], af, z4);
      const f4 Da = hi ? T2 : T0, Db = hi ? T3 : T1;
      stsc1(san4(v4u{pk(Da.x, Da.y), pk(Da.z, Da.w), pk(Db.x, Db.y), pk(Db.z, Db.w)}), rws, WS_DP + ((b * 9 + dw) * 144 + f) * 128 + (hi * 4 + g) * 16);
    }
    if (isR) {
      v4u a, b2;
      const int o = WS_DP + f * 144 * 128 + tid * 16;
      for (unsigned spins = 0;;) {
        MEMBAR();
        a = ldsc1(rws, o); b2 = ldsc1(rws, tid < 384 ? o + 768 * 16 : 0x7ffffff0);
        if (__all(umax(max4(a), max4(b2)) != SENT)) break;
        if (give_up(spins, L)) break;
      }
      float s[8];
#pragma unroll
      for (int e = 0; e < 4; ++e) { s[2 * e] = bflo(a[e]) + bflo(b2[e]); s[2 * e + 1] = bfhi(a[e]) + bfhi(b2[e]); }
#pragma unroll
      for (int e = 0; e < 8; ++e) { float v = s[e]; v += dpp<ROR8>(v); v = x32sum(x16sum(v)); s[e] = v; }
      if (lane < 8) {
        *(LAS f4*)(L + L_RED2 + (wave * 8 + lane) * 32) = f4{s[0], s[1], s[2], s[3]};
        *(LAS f4*)(L + L_RED2 + (wave * 8 + lane) * 32 + 16) = f4{s[4], s[5], s[6], s[7]};
      }
      RAWBAR();
    }
    if (isR && wave == 0) {
      const int ro = (((lane >> 5) * 4 + ((lane >> 2) & 3)) * 8 + ((lane >> 4) & 1) * 4 + (lane & 3)) * 4;
      float dn = 0.f;
#pragma unroll
      for (int w = 0; w < 12; ++w) dn += *(LAS float*)(L + L_RED2 + w * 256 + ro);
      const float gv = (lane & 1) ? bfhi(gbw) : bflo(gbw);
      const float o = *(LAS float*)(L + L_X1S + lane * 4) + rb(gv * rb(dn));
      const unsigned two = pk(o, dpp<QX1>(o));
      if (!(lane & 1)) *(GAS unsigned*)(p.out + rb_ * DM + rc0 + lane) = two;
      if (wg == 0 && lane == 0) {
        const u64 E = ((u64)ldsw(L, C_E1) << 32) | ldsw(L, C_E0);
        __hip_atomic_store((GAS u64*)(p.ws + WS_EPOCH), E + 1, __ATOMIC_RELAXED, __HIP_MEMORY_SCOPE_AGENT);
      }
    }
  }
}

}

extern "C" void kernel_launch(void* const* d_in, const int* in_sizes, int n_in, void* d_out, int out_size, void* d_ws, size_t ws_size, hipStream_t stream) {
  ak::Params p;
  p.x = (const ak::u16*)d_in[0]; p.K = (const ak::u16*)d_in[1]; p.V = (const ak::u16*)d_in[2];
  p.wq = (const ak::u16*)d_in[3]; p.wo = (const ak::u16*)d_in[4];
  p.wg = (const ak::u16*)d_in[6]; p.wu = (const ak::u16*)d_in[7]; p.wd = (const ak::u16*)d_in[8];
  p.c = (const ak::u16*)d_in[9]; p.wm = (const ak::u16*)d_in[10];
  p.out = (ak::u16*)d_out; p.ws = (unsigned char*)d_ws;
  (void)hipFuncSetAttribute((const void*)ak::fused_kernel, hipFuncAttributeMaxDynamicSharedMemorySize, ak::L_END);
  ak::fused_kernel<<<ak::NWG, ak::NTHR, ak::L_END, stream>>>(p);
}
```
